```python
import torch

def _softmax(x, axis=-1):
    return torch.softmax(x, dim=axis)
def _gelu(x):
    return torch.nn.functional.gelu(x, approximate="tanh")
def _silu(x):
    return torch.nn.functional.silu(x)
def _sigmoid(x):
    return torch.sigmoid(x)
def _layernorm(x, axis=-1, eps=1e-5):
    m = x.mean(dim=axis, keepdim=True)
    v = x.var(dim=axis, keepdim=True, unbiased=False)
    return (x - m) / torch.sqrt(v + eps)
def _scatter_add(tgt, idx, src, axis=0):
    return tgt.clone().index_add_(axis, idx, src)
def _segment_sum(src, seg, n):
    out = torch.zeros((n,) + src.shape[1:], dtype=src.dtype)
    return out.index_add_(0, seg, src)
def _pad(x, pw):
    flat = tuple(v for pair in reversed(pw) for v in pair)
    return torch.nn.functional.pad(x, flat)

kernel_name = 'recurrent_scan__sliding_block__dense_activation_squared_relu__prompt'

def setup_inputs():

    _w = (lambda a: (lambda t: t.to(torch.bfloat16) if t.is_floating_point() else t)(torch.as_tensor(a)))
    def _A_setup():
        B, S, D = 1, 2048, 640
        return {
            "x": _w((lambda *s: torch.randn(*s, dtype=torch.bfloat16) * 0.05)(B, S, D)),
            "wqkv": _w((lambda *s: torch.randn(*s, dtype=torch.bfloat16) * 0.05)(D, 3 * D)),
            "wo": _w((lambda *s: torch.randn(*s, dtype=torch.bfloat16) * 0.05)(D, D)),
        }
    _A_in = _A_setup()

    def _B_setup():
        B, S, D, F = 1, 2048, 640, 1536
        return {
            "x": _w((lambda *s: torch.randn(*s, dtype=torch.bfloat16) * 0.05)(B, S, D)),
            "w1": _w((lambda *s: torch.randn(*s, dtype=torch.bfloat16) * 0.05)(D, F)),
            "b1": _w((lambda *s: torch.randn(*s, dtype=torch.bfloat16) * 0.4)(F)),
            "w2": _w((lambda *s: torch.randn(*s, dtype=torch.bfloat16) * 0.05)(F, D)),
            "b2": _w((lambda *s: torch.randn(*s, dtype=torch.bfloat16) * 0.8)(D)),
        }
    _B_in = _B_setup()

    return {
        "A_x": _A_in["x"],
        "A_wqkv": _A_in["wqkv"],
        "A_wo": _A_in["wo"],
        "B_x": _B_in["x"],
        "B_w1": _B_in["w1"],
        "B_b1": _B_in["b1"],
        "B_w2": _B_in["w2"],
        "B_b2": _B_in["b2"],
    }

def reference(A_x, A_wqkv, A_wo, B_x, B_w1, B_b1, B_w2, B_b2):
    def _A_ref(x, wqkv, wo):
        B, S, D = x.shape
        H = 32; Dh = D // H; W = 256
        qkv = (lambda x, *d: x.permute(*d))((x @ wqkv).reshape(B, S, 3, H, Dh), 2, 0, 3, 1, 4)
        q, k, v = qkv[0], qkv[1], qkv[2]
        scores = torch.einsum("bhqd,bhkd->bhqk", q, k) * (Dh ** -0.5)
        idx = torch.arange(S)
        mask = (idx[:, None] - idx[None, :] >= 0) & (idx[:, None] - idx[None, :] < W)
        scores = torch.where(mask[None, None], scores, -1e9)
        p = _softmax(scores, axis=-1)
        o = torch.einsum("bhqk,bhkd->bhqd", p, v)
        return (lambda x, *d: x.permute(*d))(o, 0, 2, 1, 3).reshape(B, S, D) @ wo

    def _B_ref(x, w1, b1, w2, b2):
        h = torch.einsum("bsd,df->bsf", x, w1) + b1
        zero = torch.zeros_like(h)
        r = torch.where(h > zero, h, zero)
        h = r * r
        out = torch.einsum("bsf,fd->bsd", h, w2) + b2
        return out

    x = A_x
    xn = _layernorm(x, axis=-1)
    s = _A_ref(xn, A_wqkv, A_wo)
    g = _silu(_B_ref(xn, B_w1, B_b1, B_w2, B_b2))
    return x + s * g
```

```cpp
#include <hip/hip_runtime.h>
#include <hip/hip_bf16.h>
typedef unsigned short u16;
__device__ __forceinline__ float bf2f(u16 b){ return __uint_as_float(((unsigned)b)<<16); }
__device__ __forceinline__ u16 f2bf(float f){ __hip_bfloat16 h = __float2bfloat16(f); return *(u16*)&h; }
constexpr int S=2048,D=640,H=32,DH=20,W=256,F=1536;
__global__ void k_ln(const u16* x, u16* xn){
  int r=blockIdx.x; int t=threadIdx.x;
  float v[10]; float s=0;
  for(int i=0;i<10;i++){ v[i]=bf2f(x[r*D+t+64*i]); s+=v[i]; }
  for(int o=32;o;o>>=1) s+=__shfl_xor(s,o);
  float m=s/D; float q=0;
  for(int i=0;i<10;i++){ float d=v[i]-m; q+=d*d; }
  for(int o=32;o;o>>=1) q+=__shfl_xor(q,o);
  float rs=1.0f/sqrtf(q/D+1e-5f);
  for(int i=0;i<10;i++) xn[r*D+t+64*i]=f2bf((v[i]-m)*rs);
}
template<int MODE>
__global__ void k_gemm(const u16* A,const u16* B,const u16* bias,u16* C,float* Cf,int M,int N,int K){
  int n=blockIdx.x*64+threadIdx.x; int m=blockIdx.y;
  float acc=0;
  for(int k=0;k<K;k++) acc+=bf2f(A[m*K+k])*bf2f(B[k*N+n]);
  if(MODE==0) C[m*N+n]=f2bf(acc);
  if(MODE==1){ float h=acc+bf2f(bias[n]); h=h>0?h:0; C[m*N+n]=f2bf(h*h);}
  if(MODE==2){ Cf[m*N+n]=acc+bf2f(bias[n]); }
  if(MODE==3){ Cf[m*N+n]=acc; }
}
__global__ void k_attn(const u16* qkv,u16* o){
  int q=blockIdx.x*64+threadIdx.x; int h=blockIdx.y;
  float qv[DH]; for(int d=0;d<DH;d++) qv[d]=bf2f(qkv[q*1920+h*DH+d]);
  float m=-1e30f,l=0,acc[DH]; for(int d=0;d<DH;d++) acc[d]=0;
  int k0=q-W+1; if(k0<0)k0=0;
  for(int k=k0;k<=q;k++){
    float s=0; for(int d=0;d<DH;d++) s+=qv[d]*bf2f(qkv[k*1920+640+h*DH+d]);
    s*=0.22360679775f;
    float mn=fmaxf(m,s); float a=__expf(m-mn); float p=__expf(s-mn);
    l=l*a+p; for(int d=0;d<DH;d++) acc[d]=acc[d]*a+p*bf2f(qkv[k*1920+1280+h*DH+d]);
    m=mn;
  }
  for(int d=0;d<DH;d++) o[q*D+h*DH+d]=f2bf(acc[d]/l);
}
__global__ void k_fin(const u16* x,const float* s,const float* g,u16* out){
  int i=blockIdx.x*256+threadIdx.x;
  float gv=g[i]; float sg=gv/(1.f+__expf(-gv));
  out[i]=f2bf(bf2f(x[i])+s[i]*sg);
}
extern "C" void kernel_launch(void* const* d_in, const int* in_sizes, int n_in, void* d_out, int out_size, void* d_ws, size_t ws_size, hipStream_t stream){
  const u16* x=(const u16*)d_in[0]; const u16* wqkv=(const u16*)d_in[1]; const u16* wo=(const u16*)d_in[2];
  const u16* w1=(const u16*)d_in[4]; const u16* b1=(const u16*)d_in[5]; const u16* w2=(const u16*)d_in[6]; const u16* b2=(const u16*)d_in[7];
  char* ws=(char*)d_ws;
  u16* xn=(u16*)ws; u16* qkv=(u16*)(ws+(8<<20)); u16* h=(u16*)(ws+(24<<20)); u16* o=(u16*)(ws+(40<<20));
  float* s=(float*)(ws+(48<<20)); float* g=(float*)(ws+(56<<20));
  k_ln<<<S,64,0,stream>>>(x,xn);
  k_gemm<0><<<dim3(1920/64,S),64,0,stream>>>(xn,wqkv,nullptr,qkv,nullptr,S,1920,D);
  k_gemm<1><<<dim3(F/64,S),64,0,stream>>>(xn,w1,b1,h,nullptr,S,F,D);
  k_attn<<<dim3(S/64,H),64,0,stream>>>(qkv,o);
  k_gemm<3><<<dim3(D/64,S),64,0,stream>>>(o,wo,nullptr,nullptr,s,S,D,D);
  k_gemm<2><<<dim3(D/64,S),64,0,stream>>>(h,w2,b2,nullptr,g,S,D,F);
  k_fin<<<S*D/256,256,0,stream>>>(x,s,g,(u16*)d_out);
}
```

```cpp
#include <hip/hip_runtime.h>
#include <type_traits>

typedef unsigned short u16;
typedef unsigned u32x4 __attribute__((ext_vector_type(4)));
typedef unsigned u32x2 __attribute__((ext_vector_type(2)));
typedef float f32x4 __attribute__((ext_vector_type(4)));
typedef float f32x16 __attribute__((ext_vector_type(16)));
typedef short s16x8 __attribute__((ext_vector_type(8)));
typedef short s16x4 __attribute__((ext_vector_type(4)));
typedef __bf16 bf16x2_t __attribute__((ext_vector_type(2)));
#define LAS __attribute__((address_space(3)))
#define GAS __attribute__((address_space(1)))
#define DI __device__ __forceinline__
#define SB() __builtin_amdgcn_sched_barrier(0)
template <int N> using IC = std::integral_constant<int, N>;

constexpr int S_ = 2048, D_ = 640, F_ = 1536, NQKV = 1920;

constexpr unsigned WS_QK = 0;
constexpr unsigned WS_VT = 0x500000;
constexpr unsigned WS_ACT = 0x800000;
constexpr unsigned WS_WT = 0x1100000;
constexpr unsigned WS_FLAG = 0x1400000;
constexpr unsigned WS_STAT = 0x1410000;
constexpr unsigned WS_CSP = 0x1420000;
constexpr unsigned WS_BYTES = 0x14a0000;
constexpr int FL_EPOCH = 0, FL_1 = 16, FL_2 = FL_1 + 256, FL_3 = FL_2 + 256;

constexpr int L_B = 0;
constexpr int L_A = 73728;
constexpr int L_K = 110592;
constexpr int L_V = L_K + 24576;
constexpr int L_AUX = 155648;
constexpr int AUX_ST = L_AUX;
constexpr int AUX_CS = AUX_ST + 1024;
constexpr int AUX_CF = AUX_CS + 512;
constexpr int AUX_B1 = AUX_CF + 512;
constexpr int AUX_BF = AUX_B1 + 256;
constexpr int AUX_C = AUX_BF + 512;
constexpr int AUX_W = AUX_C + 32;
constexpr int L_T = 65536;
constexpr int LDS_BYTES = 163840;
constexpr int P3_STAGE = 36864;
constexpr float QSCALE = 0.22360679774997896f * 1.4426950408889634f;

template <int I, int N, class Fn> DI void sfor(Fn&& f) {
  if constexpr (I < N) { f(IC<I>{}); sfor<I + 1, N>(f); }
}
template <int N> DI void wait_bar() { SB(); asm volatile("s_waitcnt vmcnt(%0) lgkmcnt(0)\n\ts_barrier" ::"n"(N) : "memory"); SB(); }
DI void lds_bar() { SB(); asm volatile("s_waitcnt lgkmcnt(0)\n\ts_barrier" ::: "memory"); SB(); }
DI void drain() { asm volatile("s_waitcnt vmcnt(0)" ::: "memory"); }
template <int IMM> DI void glds16(unsigned voff, const void* sbase, unsigned ldsbase) {
  asm volatile("s_add_i32 m0, %2, %3\n\ts_nop 0\n\tglobal_load_lds_dwordx4 %0, %1" ::"v"(voff), "s"(sbase), "s"(ldsbase), "n"(IMM) : "memory", "scc");
}
template <int IMM> DI void glds16c(unsigned voff, const void* sbase, unsigned ldsbase) {
  asm volatile("s_add_i32 m0, %2, %3\n\ts_nop 0\n\tglobal_load_lds_dwordx4 %0, %1 sc1" ::"v"(voff), "s"(sbase), "s"(ldsbase), "n"(IMM) : "memory", "scc");
}
typedef float f32x2 __attribute__((ext_vector_type(2)));
DI f32x2 fma2(f32x2 a, f32x2 b, f32x2 c) { return __builtin_elementwise_fma(a, b, c); }
DI unsigned pk2v(f32x2 v) { bf16x2_t p = {(__bf16)v.x, (__bf16)v.y}; return __builtin_bit_cast(unsigned, p); }
DI float max3(float a, float b, float c) { float r; asm("v_max3_f32 %0, %1, %2, %3" : "=v"(r) : "v"(a), "v"(b), "v"(c)); return r; }
DI float relu(float a) { float r; asm("v_max_f32 %0, 0, %1" : "=v"(r) : "v"(a)); return r; }
DI float bf2f(unsigned b) { return __uint_as_float(b << 16); }
DI unsigned pk2(float lo, float hi) { bf16x2_t p = {(__bf16)lo, (__bf16)hi}; return __builtin_bit_cast(unsigned, p); }
DI f32x16 mfma32(u32x4 a, u32x4 b, f32x16 c) {
  return __builtin_amdgcn_mfma_f32_32x32x16_bf16(__builtin_bit_cast(s16x8, a), __builtin_bit_cast(s16x8, b), c, 0, 0, 0);
}
DI f32x4 mfma16(u32x4 a, u32x4 b, f32x4 c) {
  return __builtin_amdgcn_mfma_f32_16x16x32_bf16(__builtin_bit_cast(s16x8, a), __builtin_bit_cast(s16x8, b), c, 0, 0, 0);
}
DI u32x4 lds128(unsigned a, int off = 0) { return *(const LAS u32x4*)((const LAS char*)(size_t)a + off); }
DI u32x4 tr_frag(unsigned a, int off, int second) {
  s16x4 lo = __builtin_amdgcn_ds_read_tr16_b64_v4i16((LAS s16x4*)((LAS char*)(size_t)a + off));
  s16x4 hi = __builtin_amdgcn_ds_read_tr16_b64_v4i16((LAS s16x4*)((LAS char*)(size_t)a + off + second));
  u32x2 x = __builtin_bit_cast(u32x2, lo), y = __builtin_bit_cast(u32x2, hi);
  return u32x4{x.x, x.y, y.x, y.y};
}
DI float dot2(unsigned a, unsigned b, float c) {
  return __builtin_amdgcn_fdot2_f32_bf16(__builtin_bit_cast(bf16x2_t, a), __builtin_bit_cast(bf16x2_t, b), c, false);
}
DI float sum8(u32x4 v, float c) {
  const unsigned one = 0x3f803f80u;
  c = dot2(v.x, one, c); c = dot2(v.y, one, c); c = dot2(v.z, one, c); return dot2(v.w, one, c);
}
DI float sq8(u32x4 v, float c) { c = dot2(v.x, v.x, c); c = dot2(v.y, v.y, c); c = dot2(v.z, v.z, c); return dot2(v.w, v.w, c); }
DI float vsum8(u32x4 v) {
  const float a = __uint_as_float(v.x << 16) + __uint_as_float(v.y << 16), b = __uint_as_float(v.x & 0xffff0000u) + __uint_as_float(v.y & 0xffff0000u);
  const float c = __uint_as_float(v.z << 16) + __uint_as_float(v.w << 16), d = __uint_as_float(v.z & 0xffff0000u) + __uint_as_float(v.w & 0xffff0000u);
  return (a + b) + (c + d);
}
DI void granule_store(char* ws, unsigned off, float v, unsigned tag) {
  __hip_atomic_store((GAS unsigned long long*)(ws + off), ((unsigned long long)tag << 32) | __float_as_uint(v), __ATOMIC_RELAXED, __HIP_MEMORY_SCOPE_AGENT);
}
DI unsigned long long granule_load(char* ws, unsigned off) {
  return __hip_atomic_load((GAS unsigned long long*)(ws + off), __ATOMIC_RELAXED, __HIP_MEMORY_SCOPE_AGENT);
}
DI float xhalf_add(float v) {
  auto r = __builtin_amdgcn_permlane32_swap(__float_as_uint(v), __float_as_uint(v), false, false);
  return __uint_as_float(r[0]) + __uint_as_float(r[1]);
}
DI float xhalf_max(float v) {
  auto r = __builtin_amdgcn_permlane32_swap(__float_as_uint(v), __float_as_uint(v), false, false);
  return fmaxf(__uint_as_float(r[0]), __uint_as_float(r[1]));
}
DI u32x4 pack_feat8(unsigned ax, unsigned ay, unsigned bx, unsigned by) {
  auto rx = __builtin_amdgcn_permlane32_swap(ax, bx, false, false);
  auto ry = __builtin_amdgcn_permlane32_swap(ay, by, false, false);
  return u32x4{rx[0], ry[0], rx[1], ry[1]};
}
DI f32x2 lo2(const f32x4& v) { return f32x2{v.x, v.y}; }
DI f32x2 hi2(const f32x4& v) { return f32x2{v.z, v.w}; }
template <int I> DI f32x2 pair(const f32x16& v) { return f32x2{v[2 * I], v[2 * I + 1]}; }
DI int opaque(int v) { asm volatile("" : "+v"(v)); return v; }
typedef __amdgpu_buffer_rsrc_t rsrc_t;
DI void st128c(rsrc_t r, unsigned off, u32x4 v) { __builtin_amdgcn_raw_buffer_store_b128(v, r, off, 0, 16); }
DI void st64c(rsrc_t r, unsigned off, u32x2 v) { __builtin_amdgcn_raw_buffer_store_b64(v, r, off, 0, 16); }
DI u32x4 ld128c(rsrc_t r, unsigned off) { return __builtin_amdgcn_raw_buffer_load_b128(r, off, 0, 16); }
DI u32x2 ld64c(rsrc_t r, unsigned off) { return __builtin_amdgcn_raw_buffer_load_b64(r, off, 0, 16); }
DI unsigned flag_load(char* ws, int idx) {
  return __hip_atomic_load((GAS unsigned*)(ws + WS_FLAG) + idx * 16, __ATOMIC_RELAXED, __HIP_MEMORY_SCOPE_AGENT);
}
DI void flag_store(char* ws, int idx, unsigned v) {
  __hip_atomic_store((GAS unsigned*)(ws + WS_FLAG) + idx * 16, v, __ATOMIC_RELAXED, __HIP_MEMORY_SCOPE_AGENT);
}
DI unsigned read_epoch(const char* ws) {
  unsigned e;
  asm volatile("s_load_dword %0, %1, 0x0\n\ts_waitcnt lgkmcnt(0)" : "=s"(e) : "s"(ws + WS_FLAG + FL_EPOCH * 64) : "memory");
  return e;
}
DI void flags_wait(char* ws, int idx, unsigned tag) {
  for (int spin = 0; spin < (1 << 22); spin++) {
    const bool ok = idx < 0 || flag_load(ws, idx) == tag;
    if (__all(ok)) break;
    __builtin_amdgcn_s_sleep(1);
  }
  __builtin_amdgcn_fence(__ATOMIC_ACQUIRE, "wavefront");
}

#define STAMP_DECL
#define STAMP(i)
#define STAMP_DUMP(k)

DI void attention_task(rsrc_t rws, unsigned lds0, int w, int lane, int qi, int cgA, int cgX, int odd, u32x4 qf0, u32x4 qf1) {
  const int r32 = lane & 31, h = lane >> 5;
  const int q0 = 256 * qi + 32 * w;
  const unsigned ka = lds0 + L_K + h * 8192 + (32 * w + r32) * 16;
  const unsigned kx = lds0 + L_K + 16384 + (32 * w + r32) * 16;
  f32x16 sc[9];
  const f32x16 zero = {};
  {
    u32x4 k0[9], k1[9];
#pragma unroll
    for (int t = 0; t < 9; t++) k0[t] = lds128(ka, t * 512);
#pragma unroll
    for (int t = 0; t < 9; t++) k1[t] = lds128(kx, t * 512);
    SB();
#pragma unroll
    for (int t = 0; t < 9; t++) sc[t] = mfma32(k0[t], qf0, zero);
#pragma unroll
    for (int t = 0; t < 9; t++) sc[t] = mfma32(k1[t], qf1, sc[t]);
  }
  const unsigned va = r32 < 20 ? lds0 + L_V + (2 * w) * 640 + h * 320 + r32 * 16 : lds0 + AUX_C + (r32 == 20 ? 0 : 16);
  const int vstep = r32 < 20 ? 640 : 0;
  u32x4 vf[18];
#pragma unroll
  for (int t = 0; t < 18; t++) vf[t] = lds128(va + t * vstep);
  if (qi == 0) {
    const int tmin = 8 - w;
    sfor<0, 8>([&](auto T) {
      constexpr int t = T;
      if (t < tmin) {
#pragma unroll
        for (int i = 0; i < 16; i++) sc[t][i] = -INFINITY;
      }
    });
  }
#pragma unroll
  for (int i = 0; i < 16; i++) {
    const int kr = (i & 3) + 8 * (i >> 2) + 4 * h;
    if (kr <= r32) sc[0][i] = -INFINITY;
    if (kr > r32) sc[8][i] = -INFINITY;
  }
  float mxa[4];
#pragma unroll
  for (int i = 0; i < 4; i++) mxa[i] = max3(sc[0][4 * i], sc[0][4 * i + 1], max3(sc[0][4 * i + 2], sc[0][4 * i + 3], sc[0][4 * i + 3]));
#pragma unroll
  for (int t = 1; t < 9; t++)
#pragma unroll
    for (int i = 0; i < 8; i++) mxa[i & 3] = max3(mxa[i & 3], sc[t][2 * i], sc[t][2 * i + 1]);
  float mx = max3(mxa[0], mxa[1], max3(mxa[2], mxa[3], mxa[3]));
  {
    auto r = __builtin_amdgcn_permlane32_swap(__float_as_uint(mx), __float_as_uint(mx), false, false);
    mx = max3(__uint_as_float(r[0]), __uint_as_float(r[1]), __uint_as_float(r[1]));
  }
  f32x16 o = {};
#pragma unroll
  for (int t = 0; t < 9; t++) {
    float p[16];
#pragma unroll
    for (int i = 0; i < 16; i++) p[i] = __builtin_amdgcn_exp2f(sc[t][i] - mx);
#pragma unroll
    for (int sp = 0; sp < 2; sp++) {
      const u32x4 pf = {pk2(p[8 * sp], p[8 * sp + 1]), pk2(p[8 * sp + 2], p[8 * sp + 3]), pk2(p[8 * sp + 4], p[8 * sp + 5]),
                        pk2(p[8 * sp + 6], p[8 * sp + 7])};
      o = mfma32(vf[2 * t + sp], pf, o);
    }
  }
  auto lr = __builtin_amdgcn_permlane32_swap(__float_as_uint(o[8]), __float_as_uint(o[8]), false, false);
  const float inv = 1.0f / __uint_as_float(lr[1]);
  const unsigned ob = WS_ACT + 192u * S_ * 16;
  st128c(rws, ob + ((cgA + h) * S_ + q0 + r32) * 16,
         u32x4{pk2(o[0] * inv, o[1] * inv), pk2(o[2] * inv, o[3] * inv), pk2(o[4] * inv, o[5] * inv), pk2(o[6] * inv, o[7] * inv)});
  if (h == 0) st64c(rws, ob + (cgX * S_ + q0 + r32) * 16 + odd * 8, u32x2{pk2(o[8] * inv, o[9] * inv), pk2(o[10] * inv, o[11] * inv)});
}
DI void load_q(rsrc_t rws, int w, int lane, int qi, int cgA, int cgX, int odd, u32x4& qf0, u32x4& qf1) {
  const int r32 = lane & 31, h = lane >> 5, q0 = 256 * qi + 32 * w;
  qf0 = ld128c(rws, WS_QK + ((cgA + h) * S_ + q0 + r32) * 16);
  const u32x2 xq = ld64c(rws, WS_QK + (cgX * S_ + q0 + r32) * 16 + odd * 8);
  const unsigned x0 = h ? 0u : xq.x, x1 = h ? 0u : xq.y;
  qf1 = odd ? u32x4{0u, 0u, x0, x1} : u32x4{x0, x1, 0u, 0u};
}

__global__ __launch_bounds__(512, 2) void k_fused(const u16* __restrict__ x, const u16* __restrict__ wqkv, const u16* __restrict__ wo,
                                                  const u16* __restrict__ w1, const u16* __restrict__ b1, const u16* __restrict__ w2,
                                                  const u16* __restrict__ b2, char* __restrict__ ws, u16* __restrict__ out) {
  extern __shared__ __attribute__((aligned(16))) char smem[];
  LAS char* L = (LAS char*)smem;
  const unsigned lds0 = (unsigned)(size_t)L;
  const int tid = threadIdx.x, lane = tid & 63, w = __builtin_amdgcn_readfirstlane(tid >> 6);
  const int b = blockIdx.x, xcd = b & 7, bi = b >> 3;
  const int s = w & 3;
  const rsrc_t rws = __builtin_amdgcn_make_buffer_rsrc(ws, 0, WS_BYTES, 0x00020000);
  const int rb = (xcd & 1) * 8 + (bi >> 2), j = (xcd >> 1) * 4 + (bi & 3);
  const int row0 = rb * 128;
  const int kc = (5 * rb) >> 1, rot = kc >> 2, kc0 = kc & 3, kc1 = kc0 + 2 + (rb & 1);
  const int hd = xcd * 4 + (bi & 3), qi = bi >> 2;
  const int odd = hd & 1, cgA = (hd >> 1) * 5 + (odd ? 3 : 0), cgX = (hd >> 1) * 5 + 2;
  const int rb3 = (xcd >> 1) * 8 + (bi >> 2), cgp = (xcd & 1) * 4 + (bi & 3);
  const int row3 = rb3 * 64, n0 = cgp * 80;
  STAMP_DECL;
  STAMP(0);
  const int r32 = lane & 31, h = lane >> 5;
  const int blk = (lane >> 4) & 1, q4 = (lane & 15) >> 2, p4 = lane & 3;
  const unsigned a0 = lds0 + L_A + (32 * s + r32) * 128 + ((h ^ ((r32 >> 1) & 7)) * 16);
  auto rdA = [&](int p, int t) -> u32x4 { return lds128(a0 ^ (t * 32), (p % 5) * 16384); };
  const unsigned bb = lds0 + L_B + (8 * h + q4) * 256 + (4 * q4 + 2 * blk + (p4 >> 1)) * 16 + 8 * (p4 & 1);
  const unsigned fa = lds0 + L_B + (8 * h + q4) * 192 + (2 * blk + (p4 >> 1)) * 16 + 8 * (p4 & 1);
  constexpr int FP[5] = {32768, 49152, 61440, 0, 16384};
  auto rdB = [&](int p, int t, int u) { return tr_frag(bb ^ (64 * u), (p & 3) * 16384 + t * 4096, 1024); };
  auto rdF = [&](int f, int t, int u) { return tr_frag(fa, FP[f % 5] + t * 3072 + u * 64, 768); };

  if (w < 4) {
    const unsigned tag = read_epoch(ws) + 1u;
    u32x4 af[20], at[4];
    auto getA = [&](auto P, auto T) -> u32x4& { if constexpr (P < 5) return af[4 * P + T]; else return at[T]; };
    {
      const int row = row0 + 8 * j + 2 * s + h;
      float v[20];
#pragma unroll
      for (int i = 0; i < 5; i++) {
        const u32x2 q = *(const u32x2*)(x + (size_t)row * D_ + 128 * i + 4 * r32);
        v[4 * i] = bf2f(q.x & 0xffffu); v[4 * i + 1] = bf2f(q.x >> 16); v[4 * i + 2] = bf2f(q.y & 0xffffu); v[4 * i + 3] = bf2f(q.y >> 16);
      }
      float sm = 0.f;
#pragma unroll
      for (int i = 0; i < 20; i++) sm += v[i];
#pragma unroll
      for (int o = 16; o; o >>= 1) sm += __shfl_xor(sm, o, 32);
      const float mu = sm * (1.0f / D_);
      float sq = 0.f;
#pragma unroll
      for (int i = 0; i < 20; i++) sq = __builtin_fmaf(v[i] - mu, v[i] - mu, sq);
#pragma unroll
      for (int o = 16; o; o >>= 1) sq += __shfl_xor(sq, o, 32);
      const float r = 1.0f / sqrtf(sq * (1.0f / D_) + 1e-5f);
      if (r32 < 2) granule_store(ws, WS_STAT + row * 16 + r32 * 8, r32 ? r : mu * r, tag);
    }
    unsigned long long stg = 0;
    const unsigned stoff = WS_STAT + (row0 + 32 * s + r32) * 16 + h * 8;
    auto conv = [&](int hc) {
      const int kb = hc / 5, cb = hc % 5;
      const unsigned dst = WS_WT + ((kb * 4 + 2 * (s >> 1) + h) * D_ + cb * 128 + 64 * (s & 1) + r32) * 16;
      const unsigned src = (bb ^ (128 * (s & 1))) + (s >> 1) * 4096;
      st128c(rws, dst, tr_frag(src, L_T, 1024));
      st128c(rws, dst + 512, tr_frag(src ^ 64, L_T, 1024));
    };
    f32x16 acc[4] = {};
    auto body = [&](auto MODE) {
      u32x4 bs[3][4];
      auto mm = [&](auto N, int u) {
        constexpr int n = N;
        if constexpr (MODE != 2) {
          const u32x4 a = getA(IC<n / 4>{}, IC<n % 4>{});
          if constexpr (MODE == 0) acc[u] = mfma32(bs[n % 3][u], a, acc[u]); else acc[u] = mfma32(a, bs[n % 3][u], acc[u]);
        }
      };
      auto rB = [&](int g, int t, int u) { if constexpr (MODE != 2) bs[(4 * g + t) % 3][u] = rdB(g, t, u); };
      sfor<0, 10>([&](auto G) {
        constexpr int g = G;
        lds_bar();
        STAMP(1 + g);
#pragma unroll
        for (int u = 0; u < 4; u++) rB(g, 0, u);
        sfor<1, 4>([&](auto T) {
          constexpr int t = T;
          SB();
#pragma unroll
          for (int u = 0; u < 4; u++) {
            if constexpr (4 * g + t - 3 >= 0) mm(IC<(4 * g + t - 3 >= 0 ? 4 * g + t - 3 : 0)>{}, u);
            if (t == 1 && u == 3) getA(G, IC<0>{}) = rdA(g, 0);
            if (t == 1 && u == 3) getA(G, IC<1>{}) = rdA(g, 1);
            if (t == 2 && u == 3) getA(G, IC<2>{}) = rdA(g, 2);
            if (t == 3 && u == 0) getA(G, IC<3>{}) = rdA(g, 3);
            rB(g, t, u);
            SB();
          }
        });
#pragma unroll
        for (int u = 0; u < 4; u++) mm(IC<4 * g + 1>{}, u);
        if constexpr (g == 8) stg = granule_load(ws, stoff);
        if constexpr (g == 3) conv(b);
        if constexpr (g == 7) { if (b < 84) conv(256 + b); }
      });
#pragma unroll
      for (int u = 0; u < 4; u++) mm(IC<38>{}, u);
#pragma unroll
      for (int u = 0; u < 4; u++) mm(IC<39>{}, u);
    };
    if (j < 10) body(IC<0>{}); else if (j < 15) body(IC<1>{}); else body(IC<2>{});
    STAMP(11);
    float mur, rs;
    {
      for (int spin = 0; spin < (1 << 22) && !__all((unsigned)(stg >> 32) == tag); spin++) stg = granule_load(ws, stoff);
      auto r = __builtin_amdgcn_permlane32_swap((unsigned)stg, (unsigned)stg, false, false);
      mur = __uint_as_float(r[0]); rs = __uint_as_float(r[1]);
    }
    const int l2 = opaque(lane), r2 = l2 & 31, h2 = l2 >> 5;
    if (j >= 10 && h2 == 0) {
      ((LAS float*)(L + AUX_ST))[32 * s + r2] = mur;
      ((LAS float*)(L + AUX_ST))[128 + 32 * s + r2] = rs;
    }
    lds_bar();
    if (j < 10) {
      const float sc = (j < 5) ? QSCALE : 1.0f;
      const f32x2 a2 = {rs * sc, rs * sc}, b2q = {-mur * sc, -mur * sc};
      const unsigned dst = WS_QK + ((16 * j + h2) * S_ + row0 + 32 * s + r2) * 16;
      const unsigned csa = lds0 + AUX_CS + 16 * h2;
      sfor<0, 2>([&](auto UU) {
        f32x4 cs[8];
#pragma unroll
        for (int i = 0; i < 8; i++) cs[i] = __builtin_bit_cast(f32x4, lds128(csa, (64 * UU + 8 * i) * 4));
        SB();
        sfor<0, 4>([&](auto C) {
          constexpr int u = 2 * UU + C / 2, k = C % 2, ci = 2 * C;
          const unsigned p0 = pk2v(fma2(pair<4 * k>(acc[u]), a2, lo2(cs[ci]) * b2q)), p1 = pk2v(fma2(pair<4 * k + 1>(acc[u]), a2, hi2(cs[ci]) * b2q));
          const unsigned p2 = pk2v(fma2(pair<4 * k + 2>(acc[u]), a2, lo2(cs[ci + 1]) * b2q)), p3 = pk2v(fma2(pair<4 * k + 3>(acc[u]), a2, hi2(cs[ci + 1]) * b2q));
          st128c(rws, dst + (4 * u + 2 * k) * (S_ * 16), pack_feat8(p0, p1, p2, p3));
        });
      });
    } else if (j < 15) {
      f32x4 murv[4], rsv[4];
      float csv[4];
#pragma unroll
      for (int g = 0; g < 4; g++) {
        murv[g] = *(const LAS f32x4*)(L + AUX_ST + (32 * s + 8 * g + 4 * h2) * 4);
        rsv[g] = *(const LAS f32x4*)(L + AUX_ST + 512 + (32 * s + 8 * g + 4 * h2) * 4);
        csv[g] = -*(const LAS float*)(L + AUX_CS + (32 * g + r2) * 4);
      }
      sfor<0, 4>([&](auto U) {
        constexpr int u = U;
        const f32x2 cs2 = {csv[u], csv[u]};
        const int colv = 128 * (j - 10) + 32 * u + r2;
        const int head = colv / 20, d = colv - head * 20;
        const int dd = (head & 1) ? (d < 4 ? d + 16 : d - 4) : d;
        const int slot = dd < 16 ? ((dd & 3) + 8 * ((dd >> 2) & 1) + 4 * (dd >> 3)) : dd;
        const unsigned dst = WS_VT + ((head * 128 + (row0 + 32 * s) / 16) * 2 + h2) * 320 + slot * 16;
        sfor<0, 2>([&](auto SP) {
          constexpr int sp = SP;
          const unsigned p0 = pk2v(fma2(pair<4 * sp>(acc[u]), lo2(rsv[2 * sp]), cs2 * lo2(murv[2 * sp])));
          const unsigned p1 = pk2v(fma2(pair<4 * sp + 1>(acc[u]), hi2(rsv[2 * sp]), cs2 * hi2(murv[2 * sp])));
          const unsigned p2 = pk2v(fma2(pair<4 * sp + 2>(acc[u]), lo2(rsv[2 * sp + 1]), cs2 * lo2(murv[2 * sp + 1])));
          const unsigned p3 = pk2v(fma2(pair<4 * sp + 3>(acc[u]), hi2(rsv[2 * sp + 1]), cs2 * hi2(murv[2 * sp + 1])));
          st128c(rws, dst + sp * 640, u32x4{p0, p1, p2, p3});
        });
      });
    }
    STAMP(12);
    f32x16 facc[3] = {};
    {
      u32x4 bs[3][3];
      auto pos = [](int f) { return (f + 5) % 10; };
      auto mm = [&](auto N, int u) {
        constexpr int n = N;
        facc[u] = mfma32(bs[n % 3][u], getA(IC<(n / 4 + 5) % 10>{}, IC<n % 4>{}), facc[u]);
      };
      sfor<0, 10>([&](auto Fs) {
        constexpr int f = Fs;
        if constexpr (f == 5) wait_bar<0>();
        else if constexpr (f > 0) lds_bar();
        STAMP(13 + f);
#pragma unroll
        for (int u = 0; u < 3; u++) bs[(4 * f) % 3][u] = rdF(f, 0, u);
        sfor<1, 4>([&](auto T) {
          constexpr int t = T;
          SB();
#pragma unroll
          for (int u = 0; u < 3; u++) {
            if constexpr (4 * f + t - 3 >= 0) mm(IC<(4 * f + t - 3 >= 0 ? 4 * f + t - 3 : 0)>{}, u);
            if constexpr (f < 5) {
              if (t == 1 && u == 2) at[0] = rdA(pos(f), 0);
              if (t == 1 && u == 2) at[1] = rdA(pos(f), 1);
              if (t == 2 && u == 2) at[2] = rdA(pos(f), 2);
              if (t == 3 && u == 0) at[3] = rdA(pos(f), 3);
            }
            bs[(4 * f + t) % 3][u] = rdF(f, t, u);
            SB();
          }
        });
#pragma unroll
        for (int u = 0; u < 3; u++) mm(IC<4 * f + 1>{}, u);
      });
#pragma unroll
      for (int u = 0; u < 3; u++) mm(IC<38>{}, u);
#pragma unroll
      for (int u = 0; u < 3; u++) mm(IC<39>{}, u);
    }
    STAMP(23);
    {
      const int l3 = opaque(lane), r3 = l3 & 31, h3 = l3 >> 5;
      while (true) {
        const u32x4 rdy = *(const volatile LAS u32x4*)(L + AUX_W);
        if (__builtin_amdgcn_readfirstlane(rdy.x & rdy.y & rdy.z) == 1u) break;
      }
      asm volatile("" ::: "memory");
      const unsigned dst = WS_ACT + ((12 * j + h3) * S_ + row0 + 32 * s + r3) * 16;
      const unsigned ca = lds0 + AUX_CF + 16 * h3;
      const f32x2 rs2 = {rs, rs}, nm2 = {-mur, -mur};
      auto act = [&](f32x2 a, f32x2 cs, f32x2 bv) {
        f32x2 t = fma2(a, rs2, fma2(cs, nm2, bv));
        t = f32x2{relu(t.x), relu(t.y)};
        return pk2v(t * t);
      };
      sfor<0, 3>([&](auto U) {
        constexpr int u = U;
        f32x4 cs[4], bv[4];
#pragma unroll
        for (int i = 0; i < 4; i++) {
          cs[i] = __builtin_bit_cast(f32x4, lds128(ca, (32 * u + 8 * i) * 4));
          bv[i] = __builtin_bit_cast(f32x4, lds128(ca, AUX_BF - AUX_CF + (32 * u + 8 * i) * 4));
        }
        SB();
        sfor<0, 2>([&](auto K) {
          constexpr int k = K;
          const unsigned p0 = act(pair<4 * k>(facc[u]), lo2(cs[2 * k]), lo2(bv[2 * k])), p1 = act(pair<4 * k + 1>(facc[u]), hi2(cs[2 * k]), hi2(bv[2 * k]));
          const unsigned p2 = act(pair<4 * k + 2>(facc[u]), lo2(cs[2 * k + 1]), lo2(bv[2 * k + 1]));
          const unsigned p3 = act(pair<4 * k + 3>(facc[u]), hi2(cs[2 * k + 1]), hi2(bv[2 * k + 1]));
          st128c(rws, dst + (4 * u + 2 * k) * (S_ * 16), pack_feat8(p0, p1, p2, p3));
        });
      });
    }
    STAMP(24);
    while (__builtin_amdgcn_readfirstlane(*(const volatile LAS unsigned*)(L + AUX_W + 16 + 4 * s)) != 1u) __builtin_amdgcn_s_sleep(1);
    asm volatile("" ::: "memory");
    u32x4 qf0, qf1;
    load_q(rws, w, lane, qi, cgA, cgX, odd, qf0, qf1);
    wait_bar<0>();
    STAMP(25);
    attention_task(rws, lds0, w, lane, qi, cgA, cgX, odd, qf0, qf1);
    STAMP(26);
    const int tok = row3 + 16 * s + (lane & 15);
    u32x2 xv[5], bv[5];
#pragma unroll
    for (int i = 0; i < 5; i++) {
      xv[i] = *(const u32x2*)(x + (size_t)tok * D_ + n0 + 16 * i + 4 * (lane >> 4));
      bv[i] = *(const u32x2*)(b2 + n0 + 16 * i + 4 * (lane >> 4));
    }
    f32x4 accf[5] = {}, accs[5] = {};
    const unsigned ar = lds0 + s * 1024 + lane * 16, wr = lds0 + 4096 + lane * 16;
    u32x4 fa3[3], fw3[3][5];
    auto rd3 = [&](int so, int t, int set) {
      fa3[set] = lds128(ar + so, t * 9216);
#pragma unroll
      for (int i = 0; i < 5; i++) fw3[set][i] = lds128(wr + so, t * 9216 + i * 1024);
    };
    auto mm3 = [&](int set, f32x4* acc) {
#pragma unroll
      for (int i = 0; i < 5; i++) acc[i] = mfma16(fw3[set][i], fa3[set], acc[i]);
    };
    auto stage3 = [&](int st, auto R, f32x4* accp, f32x4* acc, bool first) {
      constexpr int r = R;
      const int so = (st & 3) * P3_STAGE;
      rd3(so, 0, r % 3); SB();
      if (!first) mm3((r + 1) % 3, accp);
      rd3(so, 1, (r + 1) % 3); SB();
      if (!first) mm3((r + 2) % 3, accp);
      rd3(so, 2, (r + 2) % 3); SB();
      mm3(r % 3, acc);
      rd3(so, 3, r % 3); SB();
      mm3((r + 1) % 3, acc);
    };
    wait_bar<0>();
    STAMP(27);
    stage3(0, IC<0>{}, accf, accf, true);
    lds_bar(); stage3(1, IC<1>{}, accf, accf, false);
    lds_bar(); stage3(2, IC<2>{}, accf, accf, false);
    for (int st = 3; st < 12; st += 3) {
      if (st == 6 && w == 0) flags_wait(ws, lane < 32 ? FL_3 + lane * 8 + (rb3 >> 2) : -1, tag);
      lds_bar(); stage3(st, IC<0>{}, accf, accf, false);
      lds_bar(); stage3(st + 1, IC<1>{}, accf, accf, false);
      lds_bar(); stage3(st + 2, IC<2>{}, accf, accf, false);
    }
    STAMP(28);
    lds_bar(); stage3(12, IC<0>{}, accf, accs, false);
    lds_bar(); stage3(13, IC<1>{}, accs, accs, false);
    lds_bar(); stage3(14, IC<2>{}, accs, accs, false);
    lds_bar(); stage3(15, IC<0>{}, accs, accs, false);
    lds_bar(); stage3(16, IC<1>{}, accs, accs, false);
    mm3(0, accs); mm3(1, accs);
    STAMP(29);
#pragma unroll
    for (int i = 0; i < 5; i++) {
      const float xf[4] = {bf2f(xv[i].x & 0xffffu), bf2f(xv[i].x >> 16), bf2f(xv[i].y & 0xffffu), bf2f(xv[i].y >> 16)};
      const float bf[4] = {bf2f(bv[i].x & 0xffffu), bf2f(bv[i].x >> 16), bf2f(bv[i].y & 0xffffu), bf2f(bv[i].y >> 16)};
      float r[4];
#pragma unroll
      for (int e = 0; e < 4; e++) {
        const float g = accf[i][e] + bf[e];
        const float sg = g * __builtin_amdgcn_rcpf(1.0f + __builtin_amdgcn_exp2f(-1.4426950408889634f * g));
        r[e] = xf[e] + accs[i][e] * sg;
      }
      *(u32x2*)(out + (size_t)tok * D_ + n0 + 16 * i + 4 * (lane >> 4)) = u32x2{pk2(r[0], r[1]), pk2(r[2], r[3])};
    }
    STAMP(30);
    if (b == 0 && w == 0) {
#pragma unroll
      for (int i = 0; i < 4; i++) flags_wait(ws, FL_2 + 64 * i + lane, tag);
      if (lane == 0) flag_store(ws, FL_EPOCH, tag);
    }
    if (w == 0) { STAMP_DUMP(0); }
  } else {
    const char* xbase = (const char*)(x + (size_t)row0 * D_);
    const char* fbase = (const char*)(w1 + 96 * j);
    const char* qbase = (const char*)(wqkv + 128 * (j < 15 ? j : 14));
    unsigned voffA[4], voffB[4], voffF[3];
#pragma unroll
    for (int e = 0; e < 4; e++) {
      voffA[e] = ((32 * s + 8 * e + (lane >> 3)) * D_ + (((lane & 7) ^ (4 * (e & 1) + (lane >> 4))) * 8)) * 2;
      voffB[e] = (16 * s + 4 * e + (lane >> 4)) * (NQKV * 2) + ((lane & 15) ^ ((lane >> 4) << 2)) * 16;
    }
    const unsigned voffT = (8 * s + (lane >> 4)) * (D_ * 2) + ((lane & 15) ^ ((lane >> 4) << 2)) * 16;
#pragma unroll
    for (int e = 0; e < 3; e++) { const int Lb = e * 1024 + lane * 16; voffF[e] = (16 * s + Lb / 192) * (F_ * 2) + Lb % 192; }
    const unsigned l4 = lds0 + s * 4096, l3k = lds0 + s * 3072;
    auto kst = [&](int p) { const int v = p + rot; return v >= 10 ? v - 10 : v; };
    auto dmaP = [&](auto P, auto E, const char* xs, const char* qs) {
      constexpr int p = P, e = E;
      if constexpr (e < 4) glds16<L_A + (p % 5) * 16384 + e * 1024>(voffA[e], xs, l4);
      else glds16<L_B + (p & 3) * 16384 + (e - 4) * 1024>(voffB[e - 4], qs, l4);
    };
    auto dmaF = [&](auto Fs) {
      constexpr int f = Fs;
      const char* src = fbase + (size_t)(kst((f + 5) % 10) * (64 * F_ * 2));
      glds16<L_B + FP[f % 5]>(voffF[0], src, l3k); glds16<L_B + FP[f % 5] + 1024>(voffF[1], src, l3k); glds16<L_B + FP[f % 5] + 2048>(voffF[2], src, l3k);
    };
    auto dmaT = [&](int hc) {
      const unsigned kb = (unsigned)hc / 5u, cb = hc - kb * 5;
      const size_t base = kb < 48 ? (size_t)w2 : (size_t)wo - (size_t)48 * 32 * D_ * 2;
      const char* src = (const char*)(base + (size_t)(kb * (32 * D_ * 2) + cb * 256));
      glds16<L_T>(voffT, src, lds0 + s * 2048);
      glds16<L_T + 1024>(voffT, src + 4 * D_ * 2, lds0 + s * 2048);
    };
    if (w == 4) { if (lane < 12) glds16<AUX_B1>(lane * 16, (const char*)(b1 + 96 * j), lds0); }
    sfor<0, 3>([&](auto P) {
      const char* xs = xbase + kst(P) * 128;
      const char* qs = qbase + (size_t)(kst(P) * (64 * NQKV * 2));
      sfor<0, 8>([&](auto E) { dmaP(P, E, xs, qs); });
    });
    if (w == 5 && lane < 16) ((LAS unsigned*)(L + AUX_C))[lane] = lane < 4 ? 0x3f803f80u : 0u;
    const unsigned tag = read_epoch(ws) + 1u;
    const unsigned cspub = WS_CSP + ((j * 16 + rb) * 224 + 32 * s + r32) * 8;
    const char* csgat = ws + WS_CSP + (size_t)(j * 16) * 224 * 8;
    const unsigned voffG = (8 * h * 224 + 32 * s + r32) * 8;
    u32x2 gr[8];
    auto gather_issue = [&](int off) {
#pragma unroll
      for (int i = 0; i < 8; i++)
        asm volatile("global_load_dwordx2 %0, %1, %2 sc1" : "=v"(gr[i]) : "v"(voffG), "s"(csgat + off + i * 224 * 8) : "memory");
    };
    auto gather_sum = [&](int off) -> float {
      for (int spin = 0; spin < (1 << 22); spin++) {
#pragma unroll
        for (int i = 0; i < 8; i++) asm volatile("" : "+v"(gr[i]));
        bool ok = true;
#pragma unroll
        for (int i = 0; i < 8; i++) ok &= gr[i].y == tag;
        if (__all(ok)) break;
        gather_issue(off);
        drain();
      }
      float a = 0.f;
#pragma unroll
      for (int i = 0; i < 8; i++) a += __uint_as_float(gr[i].x);
      return xhalf_add(a);
    };
    float cpart = 0.f;
    sfor<0, 10>([&](auto G) {
      constexpr int g = G;
      constexpr int NW[10] = {16, 18, 18, 17, 17, 18, 18, 24, 11, 9};
      wait_bar<NW[g]>();
      if constexpr (g == 1) { if (w == 7 && lane < 48) {
        const unsigned v = *(const LAS unsigned*)(L + AUX_B1 + lane * 4);
        *(LAS u32x2*)(L + AUX_BF + lane * 8) = u32x2{v << 16, v & 0xffff0000u};
      } }
      if constexpr (g == 0) dmaT(b);
      if constexpr (g == 4) dmaT(b < 84 ? 256 + b : b);
      if constexpr (g == 2) { cpart = xhalf_add(cpart); if (h == 0) granule_store(ws, cspub, cpart, tag); else asm volatile("" ::: "memory"); }
      if constexpr (g == 6) gather_issue(0);
      const char* xs = xbase + kst(g < 7 ? g + 3 : 0) * 128;
      const char* qs = qbase + (size_t)(kst(g < 7 ? g + 3 : 0) * (64 * NQKV * 2));
      if constexpr (g < 7) sfor<0, 8>([&](auto E) { dmaP(IC<g + 3>{}, E, xs, qs); });
      else if constexpr (g == 7) dmaF(IC<0>{});
      else if constexpr (g == 8) { dmaF(IC<1>{}); dmaF(IC<2>{}); }
      else dmaF(IC<3>{});
      if constexpr (g < 2) {
#pragma unroll
        for (int t = 0; t < 4; t++)
          if (4 * g + t >= kc0 && 4 * g + t < kc1) cpart += vsum8(tr_frag(bb ^ (64 * s), (g & 3) * 16384 + t * 4096, 1024));
      }
      if constexpr (g == 9) {
        const float cs = gather_sum(0);
        if (h == 0) ((LAS float*)(L + AUX_CS))[32 * s + r32] = cs;
      }
    });
    cpart = 0.f;
    const int uf = s < 3 ? s : 2;
    sfor<0, 10>([&](auto Fs) {
      constexpr int f = Fs;
      constexpr int NW[10] = {9, 9, 9, 9, 9, 9, 9, 6, 11, 0};
      wait_bar<NW[f]>();
      if constexpr (f == 5) { if (w == 4 && lane == 0) flag_store(ws, FL_1 + rb * 16 + j, tag); }
      if constexpr (f == 2) { cpart = xhalf_add(cpart); if (h == 0 && s < 3) granule_store(ws, cspub + 128 * 8, cpart, tag); }
      if constexpr (f < 6) dmaF(IC<(f < 6 ? f + 4 : 0)>{});
      if constexpr (f == 7) gather_issue(128 * 8);
      if constexpr (f < 2) {
#pragma unroll
        for (int t = 0; t < 4; t++)
          if (4 * f + t >= kc0 && 4 * f + t < kc1) cpart += vsum8(tr_frag(fa + uf * 64, FP[f % 5] + t * 3072, 768));
      }
    });
    if (s < 3) {
      const float cf = gather_sum(128 * 8);
      if (h == 0) ((LAS float*)(L + AUX_CF))[32 * s + r32] = cf;
      asm volatile("s_waitcnt lgkmcnt(0)" ::: "memory");
      if (lane == 0) *(volatile LAS unsigned*)(L + AUX_W + 4 * s) = 1u;
    }
    STAMP(1);
    {
      const int ty = lane >> 3, rbi = (lane >> 1) & 3, hi = lane & 1;
      const int c0 = ty == 2 ? 20 * hd : 8 * (odd ? cgX : cgA), c1 = ty == 2 ? 20 * hd + 19 : 8 * (odd ? cgA + 1 : cgX) + 7;
      const int tile = 5 * ty + (hi ? c1 : c0) / 128, rr = 2 * qi - 2 + rbi;
      flags_wait(ws, (lane < 24 && rr >= 0 && (ty > 0 || rbi >= 2)) ? FL_1 + rr * 16 + tile : -1, tag);
      if (lane == 0) *(volatile LAS unsigned*)(L + AUX_W + 16 + 4 * s) = 1u;
    }
    STAMP(2);
    {
      const int key0 = 256 * qi - 256;
      const char* kb = ws + WS_QK + (size_t)80 * S_ * 16;
      const u32x4 z = {0u, 0u, 0u, 0u};
#pragma unroll
      for (int i = 0; i < 2; i++) {
        const int pc = 2 * s + i, kk = key0 + 64 * pc;
        if (kk >= 0) {
          glds16c<L_K>(lane * 16, kb + ((size_t)cgA * S_ + kk) * 16, lds0 + pc * 1024);
          glds16c<L_K + 8192>(lane * 16, kb + ((size_t)(cgA + 1) * S_ + kk) * 16, lds0 + pc * 1024);
          glds16c<L_K + 16384>(lane * 16, kb + ((size_t)cgX * S_ + kk) * 16, lds0 + pc * 1024);
        } else {
#pragma unroll
          for (int c = 0; c < 3; c++) *(LAS u32x4*)(L + L_K + c * 8192 + pc * 1024 + lane * 16) = z;
        }
      }
      const char* vb = ws + WS_VT + ((long)hd * 128 + key0 / 16) * 640;
#pragma unroll
      for (int i = 0; i < 5; i++) {
        const int pc = 5 * s + i;
        if (qi > 0 || pc >= 10) glds16c<L_V>(lane * 16, vb + pc * 1024, lds0 + pc * 1024);
        else *(LAS u32x4*)(L + L_V + pc * 1024 + lane * 16) = z;
      }
    }
    u32x4 qf0, qf1;
    load_q(rws, w, lane, qi, cgA, cgX, odd, qf0, qf1);
    flags_wait(ws, FL_1 + 64 * s + lane, tag);
    wait_bar<0>();
    STAMP(3);
    if (w == 4 && lane == 0) flag_store(ws, FL_2 + rb * 16 + j, tag);
    unsigned voffX[4], voffW[5];
#pragma unroll
    for (int i = 0; i < 4; i++) voffX[i] = ((lane >> 4) * S_ + (lane & 15) + 16 * i) * 16;
#pragma unroll
    for (int i = 0; i < 5; i++) voffW[i] = ((lane >> 4) * D_ + (lane & 15) + 16 * i) * 16;
    const char* abase = ws + WS_ACT + (size_t)row3 * 16 + (size_t)s * 4 * S_ * 16;
    const char* wbase = ws + WS_WT + (size_t)n0 * 16 + (size_t)s * 4 * D_ * 16;
    const int rot3 = ((bi >> 2) + 3 * (bi & 3)) % 12, rot4 = ((bi >> 2) + (bi & 3)) % 5;
    auto kq = [&](int st) { int v; if (st < 12) { v = st + rot3; if (v >= 12) v -= 12; } else { v = st + rot4; if (v >= 17) v -= 5; } return v; };
    auto issueA = [&](int st) {
      const char* a = abase + (size_t)(kq(st) * (16 * S_ * 16));
      const unsigned d = lds0 + (st & 3) * P3_STAGE + s * 9216;
      sfor<0, 4>([&](auto I) { glds16c<I * 1024>(voffX[I], a, d); });
    };
    auto issueW = [&](int st) {
      const char* ww = wbase + (size_t)(kq(st) * (16 * D_ * 16));
      const unsigned d = lds0 + (st & 3) * P3_STAGE + s * 9216;
      sfor<0, 5>([&](auto I) { glds16c<4096 + I * 1024>(voffW[I], ww, d); });
    };
    issueW(0); issueW(1); issueW(2);
    attention_task(rws, lds0, w, lane, qi, cgA, cgX, odd, qf0, qf1);
    STAMP(4);
    flags_wait(ws, lane < 16 ? FL_2 + (rb3 >> 1) * 16 + lane : -1, tag);
    drain();
    STAMP(5);
    issueA(0); issueA(1); issueA(2);
    wait_bar<8>();
    STAMP(6);
    if (w == 4 && lane == 0) flag_store(ws, FL_3 + hd * 8 + qi, tag);
    issueA(3); issueW(3);
    wait_bar<13>();
    issueA(4); issueW(4);
    for (int st = 2; st < 14; st++) { wait_bar<18>(); issueA(st + 3); issueW(st + 3); }
    wait_bar<18>(); wait_bar<9>(); wait_bar<0>();
    STAMP(7);
    if (w == 4) { STAMP_DUMP(1); }
  }
}

extern "C" void kernel_launch(void* const* d_in, const int* in_sizes, int n_in, void* d_out, int out_size, void* d_ws, size_t ws_size,
                              hipStream_t stream) {
  (void)hipFuncSetAttribute((const void*)k_fused, hipFuncAttributeMaxDynamicSharedMemorySize, LDS_BYTES);
  k_fused<<<256, 512, LDS_BYTES, stream>>>((const u16*)d_in[0], (const u16*)d_in[1], (const u16*)d_in[2], (const u16*)d_in[4],
                                           (const u16*)d_in[5], (const u16*)d_in[6], (const u16*)d_in[7], (char*)d_ws, (u16*)d_out);
}
```

```cpp
#include <hip/hip_runtime.h>
#include <type_traits>

typedef unsigned short u16;
typedef unsigned u32x4 __attribute__((ext_vector_type(4)));
typedef unsigned u32x2 __attribute__((ext_vector_type(2)));
typedef float f32x4 __attribute__((ext_vector_type(4)));
typedef float f32x16 __attribute__((ext_vector_type(16)));
typedef short s16x8 __attribute__((ext_vector_type(8)));
typedef short s16x4 __attribute__((ext_vector_type(4)));
typedef __bf16 bf16x2_t __attribute__((ext_vector_type(2)));
#define LAS __attribute__((address_space(3)))
#define GAS __attribute__((address_space(1)))
#define DI __device__ __forceinline__
#define SB() __builtin_amdgcn_sched_barrier(0)
template <int N> using IC = std::integral_constant<int, N>;

constexpr int S_ = 2048, D_ = 640, F_ = 1536, NQKV = 1920;

constexpr unsigned WS_QK = 0;
constexpr unsigned WS_VT = 0x500000;
constexpr unsigned WS_ACT = 0x800000;
constexpr unsigned WS_WT = 0x1100000;
constexpr unsigned WS_FLAG = 0x1400000;
constexpr unsigned WS_STAT = 0x1418000;
constexpr unsigned WS_CSP = 0x1420000;
constexpr unsigned WS_BYTES = 0x14a0000;
constexpr int FL_EPOCH = 0, FL_1 = 16, FL_2 = FL_1 + 256, FL_3 = FL_2 + 256, FL_T = FL_3 + 256;

constexpr int L_B = 0;
constexpr int L_T = 65536;
constexpr int L_A = 78592;
constexpr int L_K = 115456;
constexpr int L_V = L_K + 24576;
constexpr int L_AUX = 160512;
constexpr int AUX_ST = L_AUX;
constexpr int AUX_CS = AUX_ST + 1024;
constexpr int AUX_CF = AUX_CS + 512;
constexpr int AUX_B1 = AUX_CF + 512;
constexpr int AUX_BF = AUX_B1 + 256;
constexpr int AUX_C = AUX_BF + 512;
constexpr int AUX_W = AUX_C + 32;
constexpr int LDS_BYTES = 163840;
constexpr int W3[7] = {73728, 94208, 0, 20480, 40960, 115456, 135936};
constexpr float QSCALE = 0.22360679774997896f * 1.4426950408889634f;

template <int I, int N, class Fn> DI void sfor(Fn&& f) {
  if constexpr (I < N) { f(IC<I>{}); sfor<I + 1, N>(f); }
}
template <int N> DI void wait_bar() { SB(); asm volatile("s_waitcnt vmcnt(%0) lgkmcnt(0)\n\ts_barrier" ::"n"(N) : "memory"); SB(); }
DI void lds_bar() { SB(); asm volatile("s_waitcnt lgkmcnt(0)\n\ts_barrier" ::: "memory"); SB(); }
DI void drain() { asm volatile("s_waitcnt vmcnt(0)" ::: "memory"); }
template <int IMM> DI void glds16(unsigned voff, const void* sbase, unsigned ldsbase) {
  asm volatile("s_add_i32 m0, %2, %3\n\ts_nop 0\n\tglobal_load_lds_dwordx4 %0, %1" ::"v"(voff), "s"(sbase), "s"(ldsbase), "n"(IMM) : "memory", "scc");
}
template <int IMM> DI void glds16c(unsigned voff, const void* sbase, unsigned ldsbase) {
  asm volatile("s_add_i32 m0, %2, %3\n\ts_nop 0\n\tglobal_load_lds_dwordx4 %0, %1 sc1" ::"v"(voff), "s"(sbase), "s"(ldsbase), "n"(IMM) : "memory", "scc");
}
typedef float f32x2 __attribute__((ext_vector_type(2)));
DI f32x2 fma2(f32x2 a, f32x2 b, f32x2 c) { return __builtin_elementwise_fma(a, b, c); }
DI unsigned pk2v(f32x2 v) { bf16x2_t p = {(__bf16)v.x, (__bf16)v.y}; return __builtin_bit_cast(unsigned, p); }
DI float max3(float a, float b, float c) { float r; asm("v_max3_f32 %0, %1, %2, %3" : "=v"(r) : "v"(a), "v"(b), "v"(c)); return r; }
DI float relu(float a) { float r; asm("v_max_f32 %0, 0, %1" : "=v"(r) : "v"(a)); return r; }
DI float bf2f(unsigned b) { return __uint_as_float(b << 16); }
DI unsigned pk2(float lo, float hi) { bf16x2_t p = {(__bf16)lo, (__bf16)hi}; return __builtin_bit_cast(unsigned, p); }
DI f32x16 mfma32(u32x4 a, u32x4 b, f32x16 c) {
  return __builtin_amdgcn_mfma_f32_32x32x16_bf16(__builtin_bit_cast(s16x8, a), __builtin_bit_cast(s16x8, b), c, 0, 0, 0);
}
DI f32x4 mfma16(u32x4 a, u32x4 b, f32x4 c) {
  return __builtin_amdgcn_mfma_f32_16x16x32_bf16(__builtin_bit_cast(s16x8, a), __builtin_bit_cast(s16x8, b), c, 0, 0, 0);
}
DI u32x4 lds128(unsigned a, int off = 0) { return *(const LAS u32x4*)((const LAS char*)(size_t)a + off); }
DI u32x4 tr_frag(unsigned a, int off, int second) {
  s16x4 lo = __builtin_amdgcn_ds_read_tr16_b64_v4i16((LAS s16x4*)((LAS char*)(size_t)a + off));
  s16x4 hi = __builtin_amdgcn_ds_read_tr16_b64_v4i16((LAS s16x4*)((LAS char*)(size_t)a + off + second));
  u32x2 x = __builtin_bit_cast(u32x2, lo), y = __builtin_bit_cast(u32x2, hi);
  return u32x4{x.x, x.y, y.x, y.y};
}
DI float dot2(unsigned a, unsigned b, float c) {
  return __builtin_amdgcn_fdot2_f32_bf16(__builtin_bit_cast(bf16x2_t, a), __builtin_bit_cast(bf16x2_t, b), c, false);
}
DI float sum8(u32x4 v, float c) {
  const unsigned one = 0x3f803f80u;
  c = dot2(v.x, one, c); c = dot2(v.y, one, c); c = dot2(v.z, one, c); return dot2(v.w, one, c);
}
DI float sq8(u32x4 v, float c) { c = dot2(v.x, v.x, c); c = dot2(v.y, v.y, c); c = dot2(v.z, v.z, c); return dot2(v.w, v.w, c); }
DI float vsum8(u32x4 v) {
  const float a = __uint_as_float(v.x << 16) + __uint_as_float(v.y << 16), b = __uint_as_float(v.x & 0xffff0000u) + __uint_as_float(v.y & 0xffff0000u);
  const float c = __uint_as_float(v.z << 16) + __uint_as_float(v.w << 16), d = __uint_as_float(v.z & 0xffff0000u) + __uint_as_float(v.w & 0xffff0000u);
  return (a + b) + (c + d);
}
DI void granule_store(char* ws, unsigned off, float v, unsigned tag) {
  __hip_atomic_store((GAS unsigned long long*)(ws + off), ((unsigned long long)tag << 32) | __float_as_uint(v), __ATOMIC_RELAXED, __HIP_MEMORY_SCOPE_AGENT);
}
DI unsigned long long granule_load(char* ws, unsigned off) {
  return __hip_atomic_load((GAS unsigned long long*)(ws + off), __ATOMIC_RELAXED, __HIP_MEMORY_SCOPE_AGENT);
}
DI float xhalf_add(float v) {
  auto r = __builtin_amdgcn_permlane32_swap(__float_as_uint(v), __float_as_uint(v), false, false);
  return __uint_as_float(r[0]) + __uint_as_float(r[1]);
}
DI float xhalf_max(float v) {
  auto r = __builtin_amdgcn_permlane32_swap(__float_as_uint(v), __float_as_uint(v), false, false);
  return fmaxf(__uint_as_float(r[0]), __uint_as_float(r[1]));
}
DI u32x4 pack_feat8(unsigned ax, unsigned ay, unsigned bx, unsigned by) {
  auto rx = __builtin_amdgcn_permlane32_swap(ax, bx, false, false);
  auto ry = __builtin_amdgcn_permlane32_swap(ay, by, false, false);
  return u32x4{rx[0], ry[0], rx[1], ry[1]};
}
DI f32x2 lo2(const f32x4& v) { return f32x2{v.x, v.y}; }
DI f32x2 hi2(const f32x4& v) { return f32x2{v.z, v.w}; }
template <int I> DI f32x2 pair(const f32x16& v) { return f32x2{v[2 * I], v[2 * I + 1]}; }
DI int fresh_lane() { int l; asm volatile("v_mbcnt_lo_u32_b32 %0, -1, 0\n\tv_mbcnt_hi_u32_b32 %0, -1, %0" : "=v"(l)); return l; }
typedef __amdgpu_buffer_rsrc_t rsrc_t;
DI void st128c(rsrc_t r, unsigned off, u32x4 v) { __builtin_amdgcn_raw_buffer_store_b128(v, r, off, 0, 16); }
DI void st64c(rsrc_t r, unsigned off, u32x2 v) { __builtin_amdgcn_raw_buffer_store_b64(v, r, off, 0, 16); }
DI u32x4 ld128c(rsrc_t r, unsigned off) { return __builtin_amdgcn_raw_buffer_load_b128(r, off, 0, 16); }
DI u32x2 ld64c(rsrc_t r, unsigned off) { return __builtin_amdgcn_raw_buffer_load_b64(r, off, 0, 16); }
DI unsigned flag_load(char* ws, int idx) {
  return __hip_atomic_load((GAS unsigned*)(ws + WS_FLAG) + idx * 16, __ATOMIC_RELAXED, __HIP_MEMORY_SCOPE_AGENT);
}
DI void flag_store(char* ws, int idx, unsigned v) {
  __hip_atomic_store((GAS unsigned*)(ws + WS_FLAG) + idx * 16, v, __ATOMIC_RELAXED, __HIP_MEMORY_SCOPE_AGENT);
}
DI unsigned read_epoch(const char* ws) {
  unsigned e;
  asm volatile("s_load_dword %0, %1, 0x0\n\ts_waitcnt lgkmcnt(0)" : "=s"(e) : "s"(ws + WS_FLAG + FL_EPOCH * 64) : "memory");
  return e;
}
DI void flags_wait(char* ws, int idx, unsigned tag) {
  for (int spin = 0; spin < (1 << 22); spin++) {
    const bool ok = idx < 0 || flag_load(ws, idx) == tag;
    if (__all(ok)) break;
    __builtin_amdgcn_s_sleep(1);
  }
  __builtin_amdgcn_fence(__ATOMIC_ACQUIRE, "wavefront");
}

#define STAMP_DECL
#define STAMP(i)
#define STAMP_DUMP(k)
#define STAMPC(i)

DI void attention_task(rsrc_t rws, unsigned lds0, int w, int lane, int qi, int cgA, int cgX, int odd, u32x4 qf0, u32x4 qf1) {
  const int r32 = lane & 31, h = lane >> 5;
  const int q0 = 256 * qi + 32 * w;
  const unsigned ka = lds0 + L_K + h * 8192 + (32 * w + r32) * 16;
  const unsigned kx = lds0 + L_K + 16384 + (32 * w + r32) * 16;
  f32x16 sc[9];
  const f32x16 zero = {};
  {
    u32x4 k0[9], k1[9];
#pragma unroll
    for (int t = 0; t < 9; t++) k0[t] = lds128(ka, t * 512);
#pragma unroll
    for (int t = 0; t < 9; t++) k1[t] = lds128(kx, t * 512);
    SB();
#pragma unroll
    for (int t = 0; t < 9; t++) sc[t] = mfma32(k0[t], qf0, zero);
#pragma unroll
    for (int t = 0; t < 9; t++) sc[t] = mfma32(k1[t], qf1, sc[t]);
  }
  const unsigned va = r32 < 20 ? lds0 + L_V + (2 * w) * 640 + h * 320 + r32 * 16 : lds0 + AUX_C + (r32 == 20 ? 0 : 16);
  const int vstep = r32 < 20 ? 640 : 0;
  u32x4 vf[18];
#pragma unroll
  for (int t = 0; t < 18; t++) vf[t] = lds128(va + t * vstep);
  if (qi == 0) {
    const int tmin = 8 - w;
    sfor<0, 8>([&](auto T) {
      constexpr int t = T;
      if (t < tmin) {
#pragma unroll
        for (int i = 0; i < 16; i++) sc[t][i] = -INFINITY;
      }
    });
  }
#pragma unroll
  for (int i = 0; i < 16; i++) {
    const int kr = (i & 3) + 8 * (i >> 2) + 4 * h;
    if (kr <= r32) sc[0][i] = -INFINITY;
    if (kr > r32) sc[8][i] = -INFINITY;
  }
  float mxa[4];
#pragma unroll
  for (int i = 0; i < 4; i++) mxa[i] = max3(sc[0][4 * i], sc[0][4 * i + 1], max3(sc[0][4 * i + 2], sc[0][4 * i + 3], sc[0][4 * i + 3]));
#pragma unroll
  for (int t = 1; t < 9; t++)
#pragma unroll
    for (int i = 0; i < 8; i++) mxa[i & 3] = max3(mxa[i & 3], sc[t][2 * i], sc[t][2 * i + 1]);
  float mx = max3(mxa[0], mxa[1], max3(mxa[2], mxa[3], mxa[3]));
  {
    auto r = __builtin_amdgcn_permlane32_swap(__float_as_uint(mx), __float_as_uint(mx), false, false);
    mx = max3(__uint_as_float(r[0]), __uint_as_float(r[1]), __uint_as_float(r[1]));
  }
  f32x16 o = {};
#pragma unroll
  for (int t = 0; t < 9; t++) {
    float p[16];
#pragma unroll
    for (int i = 0; i < 16; i++) p[i] = __builtin_amdgcn_exp2f(sc[t][i] - mx);
#pragma unroll
    for (int sp = 0; sp < 2; sp++) {
      const u32x4 pf = {pk2(p[8 * sp], p[8 * sp + 1]), pk2(p[8 * sp + 2], p[8 * sp + 3]), pk2(p[8 * sp + 4], p[8 * sp + 5]),
                        pk2(p[8 * sp + 6], p[8 * sp + 7])};
      o = mfma32(vf[2 * t + sp], pf, o);
    }
  }
  auto lr = __builtin_amdgcn_permlane32_swap(__float_as_uint(o[8]), __float_as_uint(o[8]), false, false);
  const float inv = 1.0f / __uint_as_float(lr[1]);
  const unsigned ob = WS_ACT + 192u * S_ * 16;
  st128c(rws, ob + ((cgA + h) * S_ + q0 + r32) * 16,
         u32x4{pk2(o[0] * inv, o[1] * inv), pk2(o[2] * inv, o[3] * inv), pk2(o[4] * inv, o[5] * inv), pk2(o[6] * inv, o[7] * inv)});
  if (h == 0) st64c(rws, ob + (cgX * S_ + q0 + r32) * 16 + odd * 8, u32x2{pk2(o[8] * inv, o[9] * inv), pk2(o[10] * inv, o[11] * inv)});
}
DI void load_q(rsrc_t rws, int w, int lane, int qi, int cgA, int cgX, int odd, u32x4& qf0, u32x4& qf1) {
  const int r32 = lane & 31, h = lane >> 5, q0 = 256 * qi + 32 * w;
  qf0 = ld128c(rws, WS_QK + ((cgA + h) * S_ + q0 + r32) * 16);
  const u32x2 xq = ld64c(rws, WS_QK + (cgX * S_ + q0 + r32) * 16 + odd * 8);
  const unsigned x0 = h ? 0u : xq.x, x1 = h ? 0u : xq.y;
  qf1 = odd ? u32x4{0u, 0u, x0, x1} : u32x4{x0, x1, 0u, 0u};
}

__global__ __launch_bounds__(512, 2) void k_fused(const u16* __restrict__ x, const u16* __restrict__ wqkv, const u16* __restrict__ wo,
                                                  const u16* __restrict__ w1, const u16* __restrict__ b1, const u16* __restrict__ w2,
                                                  const u16* __restrict__ b2, char* __restrict__ ws, u16* __restrict__ out) {
  extern __shared__ __attribute__((aligned(16))) char smem[];
  LAS char* L = (LAS char*)smem;
  const unsigned lds0 = (unsigned)(size_t)L;
  const int tid = threadIdx.x, lane = tid & 63, w = __builtin_amdgcn_readfirstlane(tid >> 6);
  const int b = blockIdx.x, xcd = b & 7, bi = b >> 3;
  const int s = w & 3;
  const rsrc_t rws = __builtin_amdgcn_make_buffer_rsrc(ws, 0, WS_BYTES, 0x00020000);
  const int rb = (xcd & 1) * 8 + (bi >> 2), j = (xcd >> 1) * 4 + (bi & 3);
  const int row0 = rb * 128;
  const int kc = (5 * rb) >> 1, rot = kc >> 2, kc0 = kc & 3, kc1 = kc0 + 2 + (rb & 1);
  const int hd = xcd * 4 + (bi & 3), qi = bi >> 2;
  const int odd = hd & 1, cgA = (hd >> 1) * 5 + (odd ? 3 : 0), cgX = (hd >> 1) * 5 + 2;
  const int rb3 = (xcd >> 1) * 8 + (bi >> 2), cgp = (xcd & 1) * 4 + (bi & 3);
  const int row3 = rb3 * 64, n0 = cgp * 80;
  STAMP_DECL;
  STAMP(0); STAMPC(38);
  const int r32 = lane & 31, h = lane >> 5;
  const int blk = (lane >> 4) & 1, q4 = (lane & 15) >> 2, p4 = lane & 3;
  const unsigned a0 = lds0 + L_A + (32 * s + r32) * 128 + ((h ^ ((r32 >> 1) & 7)) * 16);
  auto rdA = [&](int p, int t) -> u32x4 {
    if (p % 5 < 4) return lds128(a0 ^ (t * 32), (p % 5) * 16384);
    unsigned ad;
    asm volatile("v_add_u32 %0, 0x10000, %1" : "=v"(ad) : "v"(a0 ^ (t * 32)));
    return lds128(ad);
  };
  const unsigned bb = lds0 + L_B + (8 * h + q4) * 256 + (4 * q4 + 2 * blk + (p4 >> 1)) * 16 + 8 * (p4 & 1);
  const unsigned fa = lds0 + L_B + (8 * h + q4) * 192 + (2 * blk + (p4 >> 1)) * 16 + 8 * (p4 & 1);
  constexpr int FP[5] = {32768, 49152, 61440, 0, 16384};
  auto rdB = [&](int p, int t, int u) { return tr_frag(bb ^ (64 * u), (p & 3) * 16384 + t * 4096, 1024); };
  auto rdF = [&](int f, int t, int u) { return tr_frag(fa, FP[f % 5] + t * 3072 + u * 64, 768); };

  if (w < 4) {
    const int srow = row0 + 8 * j + 2 * s + h;
    u32x2 sx[5];
#pragma unroll
    for (int i = 0; i < 5; i++) sx[i] = *(const u32x2*)(x + (size_t)srow * D_ + 128 * i + 4 * r32);
    const unsigned tag = read_epoch(ws) + 1u;
    u32x4 af[20], at[4];
    auto getA = [&](auto P, auto T) -> u32x4& { if constexpr (P < 5) return af[4 * P + T]; else return at[T]; };
    auto row_stats = [&]() {
      float v[20];
#pragma unroll
      for (int i = 0; i < 5; i++) {
        v[4 * i] = bf2f(sx[i].x & 0xffffu); v[4 * i + 1] = bf2f(sx[i].x >> 16); v[4 * i + 2] = bf2f(sx[i].y & 0xffffu); v[4 * i + 3] = bf2f(sx[i].y >> 16);
      }
      float sm = 0.f;
#pragma unroll
      for (int i = 0; i < 20; i++) sm += v[i];
#pragma unroll
      for (int o = 16; o; o >>= 1) sm += __shfl_xor(sm, o, 32);
      const float mu = sm * (1.0f / D_);
      float sq = 0.f;
#pragma unroll
      for (int i = 0; i < 20; i++) sq = __builtin_fmaf(v[i] - mu, v[i] - mu, sq);
#pragma unroll
      for (int o = 16; o; o >>= 1) sq += __shfl_xor(sq, o, 32);
      const float r = 1.0f / sqrtf(sq * (1.0f / D_) + 1e-5f);
      const int lc = fresh_lane();
      if ((lc & 31) < 2) granule_store(ws, WS_STAT + (row0 + 8 * j + 2 * s + (lc >> 5)) * 16 + (lc & 1) * 8, (lc & 1) ? r : mu * r, tag);
    };
    unsigned long long stg = 0;
    auto stoff = [&]() { const int lc = fresh_lane(); return WS_STAT + (row0 + 32 * s + (lc & 31)) * 16 + (lc >> 5) * 8; };
    auto conv = [&](int hc) {
      const int kb = hc / 5, cb = hc % 5, lc = fresh_lane();
      const unsigned dst = WS_WT + ((kb * 4 + 2 * (s >> 1) + (lc >> 5)) * D_ + cb * 128 + 64 * (s & 1) + (lc & 31)) * 16;
      const unsigned src = (bb ^ (128 * (s & 1))) + (s >> 1) * 4096;
      st128c(rws, dst, tr_frag(src, L_T, 1024));
      st128c(rws, dst + 512, tr_frag(src ^ 64, L_T, 1024));
    };
    f32x16 acc[4] = {};
    auto body = [&](auto MODE) {
      u32x4 bs[3][4];
      auto mm = [&](auto N, int u) {
        constexpr int n = N;
        if constexpr (MODE != 2) {
          const u32x4 a = getA(IC<n / 4>{}, IC<n % 4>{});
          if constexpr (MODE == 0) acc[u] = mfma32(bs[n % 3][u], a, acc[u]); else acc[u] = mfma32(a, bs[n % 3][u], acc[u]);
        }
      };
      auto rB = [&](int g, int t, int u) { if constexpr (MODE != 2) bs[(4 * g + t) % 3][u] = rdB(g, t, u); };
      sfor<0, 10>([&](auto G) {
        constexpr int g = G;
        lds_bar();
        STAMP(1 + g);
#pragma unroll
        for (int u = 0; u < 4; u++) rB(g, 0, u);
        sfor<1, 4>([&](auto T) {
          constexpr int t = T;
          SB();
#pragma unroll
          for (int u = 0; u < 4; u++) {
            if constexpr (4 * g + t - 3 >= 0) mm(IC<(4 * g + t - 3 >= 0 ? 4 * g + t - 3 : 0)>{}, u);
            if (t == 1 && u == 3) getA(G, IC<0>{}) = rdA(g, 0);
            if (t == 1 && u == 3) getA(G, IC<1>{}) = rdA(g, 1);
            if (t == 2 && u == 3) getA(G, IC<2>{}) = rdA(g, 2);
            if (t == 3 && u == 0) getA(G, IC<3>{}) = rdA(g, 3);
            rB(g, t, u);
            SB();
          }
        });
#pragma unroll
        for (int u = 0; u < 4; u++) mm(IC<4 * g + 1>{}, u);
        if constexpr (g == 8) stg = granule_load(ws, stoff());
        if constexpr (g == 0) { SB(); row_stats(); }
        if constexpr (g == 3) conv(b);
        if constexpr (g == 7) { if (b < 84) conv(256 + b); }
      });
#pragma unroll
      for (int u = 0; u < 4; u++) mm(IC<38>{}, u);
#pragma unroll
      for (int u = 0; u < 4; u++) mm(IC<39>{}, u);
    };
    if (j < 10) body(IC<0>{}); else if (j < 15) body(IC<1>{}); else body(IC<2>{});
    STAMP(11);
    float mur, rs;
    {
      for (int spin = 0; spin < (1 << 22) && !__all((unsigned)(stg >> 32) == tag); spin++) stg = granule_load(ws, stoff());
      auto r = __builtin_amdgcn_permlane32_swap((unsigned)stg, (unsigned)stg, false, false);
      mur = __uint_as_float(r[0]); rs = __uint_as_float(r[1]);
    }
    const int l2 = fresh_lane(), r2 = l2 & 31, h2 = l2 >> 5;
    if (j >= 10 && h2 == 0) {
      ((LAS float*)(L + AUX_ST))[32 * s + r2] = mur;
      ((LAS float*)(L + AUX_ST))[128 + 32 * s + r2] = rs;
    }
    wait_bar<0>();
    if (j < 10) {
      const float sc = (j < 5) ? QSCALE : 1.0f;
      const f32x2 a2 = {rs * sc, rs * sc}, b2q = {-mur * sc, -mur * sc};
      const unsigned dst = WS_QK + ((16 * j + h2) * S_ + row0 + 32 * s + r2) * 16;
      const unsigned csa = lds0 + AUX_CS + 16 * h2;
      sfor<0, 2>([&](auto UU) {
        f32x4 cs[8];
#pragma unroll
        for (int i = 0; i < 8; i++) cs[i] = __builtin_bit_cast(f32x4, lds128(csa, (64 * UU + 8 * i) * 4));
        SB();
        sfor<0, 4>([&](auto C) {
          constexpr int u = 2 * UU + C / 2, k = C % 2, ci = 2 * C;
          const unsigned p0 = pk2v(fma2(pair<4 * k>(acc[u]), a2, lo2(cs[ci]) * b2q)), p1 = pk2v(fma2(pair<4 * k + 1>(acc[u]), a2, hi2(cs[ci]) * b2q));
          const unsigned p2 = pk2v(fma2(pair<4 * k + 2>(acc[u]), a2, lo2(cs[ci + 1]) * b2q)), p3 = pk2v(fma2(pair<4 * k + 3>(acc[u]), a2, hi2(cs[ci + 1]) * b2q));
          st128c(rws, dst + (4 * u + 2 * k) * (S_ * 16), pack_feat8(p0, p1, p2, p3));
        });
      });
    } else if (j < 15) {
      f32x4 murv[4], rsv[4];
      float csv[4];
#pragma unroll
      for (int g = 0; g < 4; g++) {
        murv[g] = *(const LAS f32x4*)(L + AUX_ST + (32 * s + 8 * g + 4 * h2) * 4);
        rsv[g] = *(const LAS f32x4*)(L + AUX_ST + 512 + (32 * s + 8 * g + 4 * h2) * 4);
        csv[g] = -*(const LAS float*)(L + AUX_CS + (32 * g + r2) * 4);
      }
      sfor<0, 4>([&](auto U) {
        constexpr int u = U;
        const f32x2 cs2 = {csv[u], csv[u]};
        const int colv = 128 * (j - 10) + 32 * u + r2;
        const int head = colv / 20, d = colv - head * 20;
        const int dd = (head & 1) ? (d < 4 ? d + 16 : d - 4) : d;
        const int slot = dd < 16 ? ((dd & 3) + 8 * ((dd >> 2) & 1) + 4 * (dd >> 3)) : dd;
        const unsigned dst = WS_VT + ((head * 128 + (row0 + 32 * s) / 16) * 2 + h2) * 320 + slot * 16;
        sfor<0, 2>([&](auto SP) {
          constexpr int sp = SP;
          const unsigned p0 = pk2v(fma2(pair<4 * sp>(acc[u]), lo2(rsv[2 * sp]), cs2 * lo2(murv[2 * sp])));
          const unsigned p1 = pk2v(fma2(pair<4 * sp + 1>(acc[u]), hi2(rsv[2 * sp]), cs2 * hi2(murv[2 * sp])));
          const unsigned p2 = pk2v(fma2(pair<4 * sp + 2>(acc[u]), lo2(rsv[2 * sp + 1]), cs2 * lo2(murv[2 * sp + 1])));
          const unsigned p3 = pk2v(fma2(pair<4 * sp + 3>(acc[u]), hi2(rsv[2 * sp + 1]), cs2 * hi2(murv[2 * sp + 1])));
          st128c(rws, dst + sp * 640, u32x4{p0, p1, p2, p3});
        });
      });
    }
    STAMP(12);
    f32x16 facc[3] = {};
    {
      u32x4 bs[3][3];
      auto pos = [](int f) { return (f + 5) % 10; };
      auto mm = [&](auto N, int u) {
        constexpr int n = N;
        facc[u] = mfma32(bs[n % 3][u], getA(IC<(n / 4 + 5) % 10>{}, IC<n % 4>{}), facc[u]);
      };
      sfor<0, 10>([&](auto Fs) {
        constexpr int f = Fs;
        if constexpr (f == 5) wait_bar<0>();
        else if constexpr (f > 0) lds_bar();
        STAMP(13 + f);
#pragma unroll
        for (int u = 0; u < 3; u++) bs[(4 * f) % 3][u] = rdF(f, 0, u);
        sfor<1, 4>([&](auto T) {
          constexpr int t = T;
          SB();
#pragma unroll
          for (int u = 0; u < 3; u++) {
            if constexpr (4 * f + t - 3 >= 0) mm(IC<(4 * f + t - 3 >= 0 ? 4 * f + t - 3 : 0)>{}, u);
            if constexpr (f < 5) {
              if (t == 1 && u == 2) at[0] = rdA(pos(f), 0);
              if (t == 1 && u == 2) at[1] = rdA(pos(f), 1);
              if (t == 2 && u == 2) at[2] = rdA(pos(f), 2);
              if (t == 3 && u == 0) at[3] = rdA(pos(f), 3);
            }
            bs[(4 * f + t) % 3][u] = rdF(f, t, u);
            SB();
          }
        });
#pragma unroll
        for (int u = 0; u < 3; u++) mm(IC<4 * f + 1>{}, u);
      });
#pragma unroll
      for (int u = 0; u < 3; u++) mm(IC<38>{}, u);
#pragma unroll
      for (int u = 0; u < 3; u++) mm(IC<39>{}, u);
    }
    STAMP(23);
    {
      const int l3 = fresh_lane(), r3 = l3 & 31, h3 = l3 >> 5;
      while (true) {
        const u32x4 rdy = *(const volatile LAS u32x4*)(L + AUX_W);
        if (__builtin_amdgcn_readfirstlane(rdy.x & rdy.y & rdy.z) == 1u) break;
      }
      asm volatile("" ::: "memory");
      const unsigned dst = WS_ACT + ((12 * j + h3) * S_ + row0 + 32 * s + r3) * 16;
      const unsigned ca = lds0 + AUX_CF + 16 * h3;
      const f32x2 rs2 = {rs, rs}, nm2 = {-mur, -mur};
      auto act = [&](f32x2 a, f32x2 cs, f32x2 bv) {
        f32x2 t = fma2(a, rs2, fma2(cs, nm2, bv));
        t = f32x2{relu(t.x), relu(t.y)};
        return pk2v(t * t);
      };
      sfor<0, 3>([&](auto U) {
        constexpr int u = U;
        f32x4 cs[4], bv[4];
#pragma unroll
        for (int i = 0; i < 4; i++) {
          cs[i] = __builtin_bit_cast(f32x4, lds128(ca, (32 * u + 8 * i) * 4));
          bv[i] = __builtin_bit_cast(f32x4, lds128(ca, AUX_BF - AUX_CF + (32 * u + 8 * i) * 4));
        }
        SB();
        sfor<0, 2>([&](auto K) {
          constexpr int k = K;
          const unsigned p0 = act(pair<4 * k>(facc[u]), lo2(cs[2 * k]), lo2(bv[2 * k])), p1 = act(pair<4 * k + 1>(facc[u]), hi2(cs[2 * k]), hi2(bv[2 * k]));
          const unsigned p2 = act(pair<4 * k + 2>(facc[u]), lo2(cs[2 * k + 1]), lo2(bv[2 * k + 1]));
          const unsigned p3 = act(pair<4 * k + 3>(facc[u]), hi2(cs[2 * k + 1]), hi2(bv[2 * k + 1]));
          st128c(rws, dst + (4 * u + 2 * k) * (S_ * 16), pack_feat8(p0, p1, p2, p3));
        });
      });
    }
    STAMP(24);
    while (__builtin_amdgcn_readfirstlane(*(const volatile LAS unsigned*)(L + AUX_W + 16 + 4 * s)) != 1u) __builtin_amdgcn_s_sleep(1);
    asm volatile("" ::: "memory");
    const int lanem = fresh_lane();
    u32x4 qf0, qf1;
    load_q(rws, w, lanem, qi, cgA, cgX, odd, qf0, qf1);
    wait_bar<0>();
    STAMP(25);
    attention_task(rws, lds0, w, lanem, qi, cgA, cgX, odd, qf0, qf1);
    STAMP(26);
    const int mh = s & 1, kh = s >> 1;
    const int rot3 = ((bi >> 2) + 3 * (bi & 3)) % 12, rot4 = ((bi >> 2) + (bi & 3)) % 5;
    auto kq = [&](int st) { int v; if (st < 12) { v = st + rot3; if (v >= 12) v -= 12; } else { v = st + rot4; if (v >= 17) v -= 5; } return v; };
    flags_wait(ws, lanem < 16 ? FL_2 + (rb3 >> 1) * 16 + lanem : -1, tag);
    drain();
    const unsigned avo = WS_ACT + ((4 * kh + (lanem >> 4)) * S_ + row3 + 32 * mh + (lanem & 15)) * 16;
    u32x4 ar[4][4];
    auto ldA = [&](auto ST) {
      constexpr int st = ST;
      const unsigned so = kq(st) * (16 * S_ * 16);
#pragma unroll
      for (int i = 0; i < 4; i++) ar[st & 3][i] = __builtin_amdgcn_raw_buffer_load_b128(rws, avo + (i >> 1) * (8 * S_ * 16) + (i & 1) * 256, so, 16);
    };
    ldA(IC<0>{}); ldA(IC<1>{}); ldA(IC<2>{});
    const int tok = row3 + 32 * mh + 16 * kh + (lanem & 15);
    u32x2 xv[5], bv[5];
#pragma unroll
    for (int i = 0; i < 5; i++) {
      xv[i] = *(const u32x2*)(x + (size_t)tok * D_ + n0 + 16 * i + 4 * (lanem >> 4));
      bv[i] = *(const u32x2*)(b2 + n0 + 16 * i + 4 * (lanem >> 4));
    }
    f32x4 accf[2][5] = {}, accs[2][5] = {};
    const unsigned wr = lds0 + kh * 5120 + lanem * 16;
    u32x4 fw[2][5];
    unsigned f3 = tag;
    auto mm3 = [&](auto ST, auto KK) {
      constexpr int st = ST, kk = KK;
#pragma unroll
      for (int i = 0; i < 5; i++)
#pragma unroll
        for (int r = 0; r < 2; r++) {
          if constexpr (st < 12) accf[r][i] = mfma16(fw[kk][i], ar[st & 3][2 * kk + r], accf[r][i]);
          else accs[r][i] = mfma16(fw[kk][i], ar[st & 3][2 * kk + r], accs[r][i]);
        }
    };
    sfor<0, 17>([&](auto ST) {
      constexpr int st = ST;
      lds_bar();
      if constexpr (st == 0) { STAMP(27); }
      if constexpr (st == 12) { STAMP(28); }
#pragma unroll
      for (int i = 0; i < 5; i++) fw[0][i] = lds128(wr, W3[st % 7] + i * 1024);
      SB();
      if constexpr (st > 0) mm3(IC<(st > 0 ? st - 1 : 0)>{}, IC<1>{});
      SB();
#pragma unroll
      for (int i = 0; i < 5; i++) fw[1][i] = lds128(wr, W3[st % 7] + 10240 + i * 1024);
      if constexpr (st == 7) { if (lanem < 32) f3 = flag_load(ws, FL_3 + lanem * 8 + (rb3 >> 2)); }
      if constexpr (st == 9) { if (!__all(f3 == tag)) flags_wait(ws, lanem < 32 ? FL_3 + lanem * 8 + (rb3 >> 2) : -1, tag); }
      if constexpr (st + 3 < 17) ldA(IC<(st + 3 < 17 ? st + 3 : 0)>{});
      SB();
      mm3(ST, IC<0>{});
    });
    mm3(IC<16>{}, IC<1>{});
    STAMP(29);
    f32x4 gf[5], gs[5];
    {
      LAS f32x4* ex = (LAS f32x4*)(L + W3[5]) + s * 640 + lanem;
#pragma unroll
      for (int i = 0; i < 5; i++) { ex[i * 64] = kh ? accf[0][i] : accf[1][i]; ex[320 + i * 64] = kh ? accs[0][i] : accs[1][i]; }
      lds_bar();
      const LAS f32x4* ey = (const LAS f32x4*)(L + W3[5]) + (s ^ 2) * 640 + lanem;
#pragma unroll
      for (int i = 0; i < 5; i++) { gf[i] = (kh ? accf[1][i] : accf[0][i]) + ey[i * 64]; gs[i] = (kh ? accs[1][i] : accs[0][i]) + ey[320 + i * 64]; }
    }
#pragma unroll
    for (int i = 0; i < 5; i++) {
      const float xf[4] = {bf2f(xv[i].x & 0xffffu), bf2f(xv[i].x >> 16), bf2f(xv[i].y & 0xffffu), bf2f(xv[i].y >> 16)};
      const float bf[4] = {bf2f(bv[i].x & 0xffffu), bf2f(bv[i].x >> 16), bf2f(bv[i].y & 0xffffu), bf2f(bv[i].y >> 16)};
      float r[4];
#pragma unroll
      for (int e = 0; e < 4; e++) {
        const float g = gf[i][e] + bf[e];
        const float sg = g * __builtin_amdgcn_rcpf(1.0f + __builtin_amdgcn_exp2f(-1.4426950408889634f * g));
        r[e] = __builtin_fmaf(gs[i][e], sg, xf[e]);
      }
      *(u32x2*)(out + (size_t)tok * D_ + n0 + 16 * i + 4 * (lanem >> 4)) = u32x2{pk2(r[0], r[1]), pk2(r[2], r[3])};
    }
    STAMP(30); STAMPC(39);
    if (b == 0 && w == 0) {
#pragma unroll
      for (int i = 0; i < 4; i++) flags_wait(ws, FL_2 + 64 * i + lanem, tag);
      if (lanem == 0) flag_store(ws, FL_EPOCH, tag);
    }
    if (w == 0) { STAMP_DUMP(0); }
  } else {
    const char* xbase = (const char*)(x + (size_t)row0 * D_);
    const char* fbase = (const char*)(w1 + 96 * j);
    const char* qbase = (const char*)(wqkv + 128 * (j < 15 ? j : 14));
    unsigned voffA[4], voffB[4], voffF[3];
#pragma unroll
    for (int e = 0; e < 4; e++) {
      voffA[e] = ((32 * s + 8 * e + (lane >> 3)) * D_ + (((lane & 7) ^ (4 * (e & 1) + (lane >> 4))) * 8)) * 2;
      voffB[e] = (16 * s + 4 * e + (lane >> 4)) * (NQKV * 2) + ((lane & 15) ^ ((lane >> 4) << 2)) * 16;
    }
    const unsigned voffT = (8 * s + (lane >> 4)) * (D_ * 2) + ((lane & 15) ^ ((lane >> 4) << 2)) * 16;
#pragma unroll
    for (int e = 0; e < 3; e++) { const int Lb = e * 1024 + lane * 16; voffF[e] = (16 * s + Lb / 192) * (F_ * 2) + Lb % 192; }
    const unsigned l4 = lds0 + s * 4096, l3k = lds0 + s * 3072;
    auto kst = [&](int p) { const int v = p + rot; return v >= 10 ? v - 10 : v; };
    auto dmaP = [&](auto P, auto E, const char* xs, const char* qs) {
      constexpr int p = P, e = E;
      if constexpr (e < 4) glds16<L_A + (p % 5) * 16384 + e * 1024>(voffA[e], xs, l4);
      else glds16<L_B + (p & 3) * 16384 + (e - 4) * 1024>(voffB[e - 4], qs, l4);
    };
    auto dmaF = [&](auto Fs) {
      constexpr int f = Fs;
      const char* src = fbase + (size_t)(kst((f + 5) % 10) * (64 * F_ * 2));
      glds16<L_B + FP[f % 5]>(voffF[0], src, l3k); glds16<L_B + FP[f % 5] + 1024>(voffF[1], src, l3k); glds16<L_B + FP[f % 5] + 2048>(voffF[2], src, l3k);
    };
    auto dmaT = [&](int hc) {
      const unsigned kb = (unsigned)hc / 5u, cb = hc - kb * 5;
      const size_t base = kb < 48 ? (size_t)w2 : (size_t)wo - (size_t)48 * 32 * D_ * 2;
      const char* src = (const char*)(base + (size_t)(kb * (32 * D_ * 2) + cb * 256));
      glds16<L_T>(voffT, src, lds0 + s * 2048);
      glds16<L_T + 1024>(voffT, src + 4 * D_ * 2, lds0 + s * 2048);
    };
    if (w == 4) { if (lane < 12) glds16<AUX_B1>(lane * 16, (const char*)(b1 + 96 * j), lds0); }
    sfor<0, 3>([&](auto P) {
      const char* xs = xbase + kst(P) * 128;
      const char* qs = qbase + (size_t)(kst(P) * (64 * NQKV * 2));
      sfor<0, 8>([&](auto E) { dmaP(P, E, xs, qs); });
    });
    if (w == 5 && lane < 16) ((LAS unsigned*)(L + AUX_C))[lane] = lane < 4 ? 0x3f803f80u : 0u;
    const unsigned tag = read_epoch(ws) + 1u;
    const unsigned cspub = WS_CSP + ((j * 16 + rb) * 224 + 32 * s + r32) * 8;
    const char* csgat = ws + WS_CSP + (size_t)(j * 16) * 224 * 8;
    const unsigned voffG = (8 * h * 224 + 32 * s + r32) * 8;
    u32x2 gr[8];
    auto gather_issue = [&](int off) {
#pragma unroll
      for (int i = 0; i < 8; i++)
        asm volatile("global_load_dwordx2 %0, %1, %2 sc1" : "=v"(gr[i]) : "v"(voffG), "s"(csgat + off + i * 224 * 8) : "memory");
    };
    auto gather_sum = [&](int off) -> float {
      for (int spin = 0; spin < (1 << 22); spin++) {
#pragma unroll
        for (int i = 0; i < 8; i++) asm volatile("" : "+v"(gr[i]));
        bool ok = true;
#pragma unroll
        for (int i = 0; i < 8; i++) ok &= gr[i].y == tag;
        if (__all(ok)) break;
        gather_issue(off);
        drain();
      }
      float a = 0.f;
#pragma unroll
      for (int i = 0; i < 8; i++) a += __uint_as_float(gr[i].x);
      return xhalf_add(a);
    };
    float cpart = 0.f;
    sfor<0, 10>([&](auto G) {
      constexpr int g = G;
      constexpr int NW[10] = {16, 18, 18, 17, 17, 18, 18, 24, 11, 9};
      wait_bar<NW[g]>();
      if constexpr (g == 1) { if (w == 7 && lane < 48) {
        const unsigned v = *(const LAS unsigned*)(L + AUX_B1 + lane * 4);
        *(LAS u32x2*)(L + AUX_BF + lane * 8) = u32x2{v << 16, v & 0xffff0000u};
      } }
      if constexpr (g == 0) dmaT(b);
      if constexpr (g == 4) dmaT(b < 84 ? 256 + b : b);
      if constexpr (g == 2) { cpart = xhalf_add(cpart); if (h == 0) granule_store(ws, cspub, cpart, tag); else asm volatile("" ::: "memory"); }
      if constexpr (g == 6) gather_issue(0);
      const char* xs = xbase + kst(g < 7 ? g + 3 : 0) * 128;
      const char* qs = qbase + (size_t)(kst(g < 7 ? g + 3 : 0) * (64 * NQKV * 2));
      if constexpr (g < 7) sfor<0, 8>([&](auto E) { dmaP(IC<g + 3>{}, E, xs, qs); });
      else if constexpr (g == 7) dmaF(IC<0>{});
      else if constexpr (g == 8) { dmaF(IC<1>{}); dmaF(IC<2>{}); }
      else dmaF(IC<3>{});
      if constexpr (g < 2) {
#pragma unroll
        for (int t = 0; t < 4; t++)
          if (4 * g + t >= kc0 && 4 * g + t < kc1) cpart += vsum8(tr_frag(bb ^ (64 * s), (g & 3) * 16384 + t * 4096, 1024));
      }
      if constexpr (g == 9) {
        const float cs = gather_sum(0);
        if (h == 0) ((LAS float*)(L + AUX_CS))[32 * s + r32] = cs;
      }
    });
    cpart = 0.f;
    const int uf = s < 3 ? s : 2;
    unsigned voffW[5];
#pragma unroll
    for (int i = 0; i < 5; i++) voffW[i] = ((lane >> 4) * D_ + (lane & 15) + 16 * i) * 16;
    const char* wbase = ws + WS_WT + (size_t)n0 * 16 + (size_t)s * 4 * D_ * 16;
    const int rot3 = ((bi >> 2) + 3 * (bi & 3)) % 12, rot4 = ((bi >> 2) + (bi & 3)) % 5;
    auto kq = [&](int st) { int v; if (st < 12) { v = st + rot3; if (v >= 12) v -= 12; } else { v = st + rot4; if (v >= 17) v -= 5; } return v; };
    const unsigned l5 = lds0 + s * 5120;
    auto issueW = [&](auto ST) {
      constexpr int st = ST;
      const char* ww = wbase + (size_t)(kq(st) * (16 * D_ * 16));
      sfor<0, 5>([&](auto I) { glds16c<W3[st % 7] + I * 1024>(voffW[I], ww, l5); });
    };
    int idxq;
    {
      const int ty = lane >> 3, rbi = (lane >> 1) & 3, hi = lane & 1;
      const int c0 = ty == 2 ? 20 * hd : 8 * (odd ? cgX : cgA), c1 = ty == 2 ? 20 * hd + 19 : 8 * (odd ? cgA + 1 : cgX) + 7;
      const int tile = 5 * ty + (hi ? c1 : c0) / 128, rr = 2 * qi - 2 + rbi;
      idxq = (lane < 24 && rr >= 0 && (ty > 0 || rbi >= 2)) ? FL_1 + rr * 16 + tile : FL_1 + rb * 16 + j;
    }
    unsigned pv[4];
    auto poll_issue = [&](unsigned& dst, int idx) {
      asm volatile("global_load_dword %0, %1, %2 sc1" : "=v"(dst) : "v"(idx * 64), "s"(ws + WS_FLAG) : "memory");
    };
    sfor<0, 10>([&](auto Fs) {
      constexpr int f = Fs;
      if constexpr (f <= 6) wait_bar<9>(); else if constexpr (f == 7) wait_bar<0>(); else lds_bar();
      if constexpr (f == 0) { if (w == 4 && lane == 0) flag_store(ws, FL_T + b, tag); }
      if constexpr (f == 5) { if (w == 4 && lane == 0) flag_store(ws, FL_1 + rb * 16 + j, tag); }
      if constexpr (f == 2) { cpart = xhalf_add(cpart); if (h == 0 && s < 3) granule_store(ws, cspub + 128 * 8, cpart, tag); }
      if constexpr (f < 6) dmaF(IC<(f < 6 ? f + 4 : 0)>{});
      if constexpr (f == 5) {
#pragma unroll
        for (int i = 0; i < 4; i++) poll_issue(pv[i], FL_T + 64 * i + lane);
      }
      if constexpr (f == 7) {
#pragma unroll
        for (int i = 0; i < 4; i++) asm volatile("" : "+v"(pv[i]));
        if (!__all(pv[0] == tag && pv[1] == tag && pv[2] == tag && pv[3] == tag)) {
#pragma unroll
          for (int i = 0; i < 4; i++) flags_wait(ws, FL_T + 64 * i + lane, tag);
        }
        gather_issue(128 * 8);
        issueW(IC<0>{}); issueW(IC<1>{});
      }
      if constexpr (f == 8) poll_issue(pv[0], idxq);
      if constexpr (f < 2) {
#pragma unroll
        for (int t = 0; t < 4; t++)
          if (4 * f + t >= kc0 && 4 * f + t < kc1) cpart += vsum8(tr_frag(fa + uf * 64, FP[f % 5] + t * 3072, 768));
      }
    });
    drain();
    if (s < 3) {
      const float cf = gather_sum(128 * 8);
      if (h == 0) ((LAS float*)(L + AUX_CF))[32 * s + r32] = cf;
      asm volatile("s_waitcnt lgkmcnt(0)" ::: "memory");
      if (lane == 0) *(volatile LAS unsigned*)(L + AUX_W + 4 * s) = 1u;
    }
    STAMP(1);
    asm volatile("" : "+v"(pv[0]));
    if (!__all(pv[0] == tag)) flags_wait(ws, idxq, tag);
    if (lane == 0) *(volatile LAS unsigned*)(L + AUX_W + 16 + 4 * s) = 1u;
    STAMP(2);
    {
      const int key0 = 256 * qi - 256;
      const char* kb = ws + WS_QK + (size_t)80 * S_ * 16;
      const u32x4 z = {0u, 0u, 0u, 0u};
#pragma unroll
      for (int i = 0; i < 2; i++) {
        const int pc = 2 * s + i, kk = key0 + 64 * pc;
        if (kk >= 0) {
          glds16c<L_K>(lane * 16, kb + ((size_t)cgA * S_ + kk) * 16, lds0 + pc * 1024);
          glds16c<L_K + 8192>(lane * 16, kb + ((size_t)(cgA + 1) * S_ + kk) * 16, lds0 + pc * 1024);
          glds16c<L_K + 16384>(lane * 16, kb + ((size_t)cgX * S_ + kk) * 16, lds0 + pc * 1024);
        } else {
#pragma unroll
          for (int c = 0; c < 3; c++) *(LAS u32x4*)(L + L_K + c * 8192 + pc * 1024 + lane * 16) = z;
        }
      }
      const char* vb = ws + WS_VT + ((long)hd * 128 + key0 / 16) * 640;
#pragma unroll
      for (int i = 0; i < 5; i++) {
        const int pc = 5 * s + i;
        if (qi > 0 || pc >= 10) glds16c<L_V>(lane * 16, vb + pc * 1024, lds0 + pc * 1024);
        else *(LAS u32x4*)(L + L_V + pc * 1024 + lane * 16) = z;
      }
    }
    u32x4 qf0, qf1;
    load_q(rws, w, lane, qi, cgA, cgX, odd, qf0, qf1);
    wait_bar<0>();
    STAMP(3);
    if (w == 4 && lane == 0) flag_store(ws, FL_2 + rb * 16 + j, tag);
    issueW(IC<2>{}); issueW(IC<3>{}); issueW(IC<4>{});
    attention_task(rws, lds0, w, lane, qi, cgA, cgX, odd, qf0, qf1);
    STAMP(4);
    wait_bar<0>();
    STAMP(6);
    if (w == 4 && lane == 0) flag_store(ws, FL_3 + hd * 8 + qi, tag);
    issueW(IC<5>{}); issueW(IC<6>{});
    sfor<1, 17>([&](auto ST) {
      constexpr int st = ST;
      wait_bar<(st <= 11) ? 25 : 5 * (16 - st)>();
      if constexpr (st + 6 < 17) issueW(IC<(st + 6 < 17 ? st + 6 : 0)>{});
    });
    lds_bar();
    STAMP(7);
    if (w == 4) { STAMP_DUMP(1); }
  }
}

extern "C" void kernel_launch(void* const* d_in, const int* in_sizes, int n_in, void* d_out, int out_size, void* d_ws, size_t ws_size,
                              hipStream_t stream) {
  (void)hipFuncSetAttribute((const void*)k_fused, hipFuncAttributeMaxDynamicSharedMemorySize, LDS_BYTES);
  k_fused<<<256, 512, LDS_BYTES, stream>>>((const u16*)d_in[0], (const u16*)d_in[1], (const u16*)d_in[2], (const u16*)d_in[4],
                                           (const u16*)d_in[5], (const u16*)d_in[6], (const u16*)d_in[7], (char*)d_ws, (u16*)d_out);
}
```

```cpp
#include <hip/hip_runtime.h>
#include <type_traits>

typedef unsigned short u16;
typedef unsigned u32x4 __attribute__((ext_vector_type(4)));
typedef unsigned u32x2 __attribute__((ext_vector_type(2)));
typedef float f32x4 __attribute__((ext_vector_type(4)));
typedef float f32x16 __attribute__((ext_vector_type(16)));
typedef short s16x8 __attribute__((ext_vector_type(8)));
typedef short s16x4 __attribute__((ext_vector_type(4)));
typedef __bf16 bf16x2_t __attribute__((ext_vector_type(2)));
#define LAS __attribute__((address_space(3)))
#define GAS __attribute__((address_space(1)))
#define DI __device__ __forceinline__
#define SB() __builtin_amdgcn_sched_barrier(0)
template <int N> using IC = std::integral_constant<int, N>;

constexpr int S_ = 2048, D_ = 640, F_ = 1536, NQKV = 1920;

constexpr unsigned WS_QK = 0;
constexpr unsigned WS_VT = 0x500000;
constexpr unsigned WS_ACT = 0x800000;
constexpr unsigned WS_WT = 0x1100000;
constexpr unsigned WS_FLAG = 0x1400000;
constexpr unsigned WS_STAT = 0x1418000;
constexpr unsigned WS_CSP = 0x1420000;
constexpr unsigned WS_BYTES = 0x14a0000;
constexpr int FL_EPOCH = 0, FL_1 = 16, FL_2 = FL_1 + 256, FL_3 = FL_2 + 256, FL_T = FL_3 + 256;

constexpr int L_B = 0;
constexpr int L_T = 65536;
constexpr int L_A = 78592;
constexpr int L_K = 115456;
constexpr int L_V = L_K + 24576;
constexpr int L_AUX = 160512;
constexpr int AUX_ST = L_AUX;
constexpr int AUX_CS = AUX_ST + 1024;
constexpr int AUX_CF = AUX_CS + 512;
constexpr int AUX_B1 = AUX_CF + 512;
constexpr int AUX_BF = AUX_B1 + 256;
constexpr int AUX_C = AUX_BF + 512;
constexpr int AUX_W = AUX_C + 32;
constexpr int LDS_BYTES = 163840;
constexpr int W3[7] = {73728, 94208, 0, 20480, 40960, 115456, 135936};
constexpr float QSCALE = 0.22360679774997896f * 1.4426950408889634f;

template <int I, int N, class Fn> DI void sfor(Fn&& f) {
  if constexpr (I < N) { f(IC<I>{}); sfor<I + 1, N>(f); }
}
template <int N> DI void wait_bar() { SB(); asm volatile("s_waitcnt vmcnt(%0) lgkmcnt(0)\n\ts_barrier" ::"n"(N) : "memory"); SB(); }
DI void lds_bar() { SB(); asm volatile("s_waitcnt lgkmcnt(0)\n\ts_barrier" ::: "memory"); SB(); }
DI void drain() { asm volatile("s_waitcnt vmcnt(0)" ::: "memory"); }
template <int IMM> DI void glds16(unsigned voff, const void* sbase, unsigned ldsbase) {
  asm volatile("s_add_i32 m0, %2, %3\n\ts_nop 0\n\tglobal_load_lds_dwordx4 %0, %1" ::"v"(voff), "s"(sbase), "s"(ldsbase), "n"(IMM) : "memory", "scc");
}
template <int IMM> DI void glds16c(unsigned voff, const void* sbase, unsigned ldsbase) {
  asm volatile("s_add_i32 m0, %2, %3\n\ts_nop 0\n\tglobal_load_lds_dwordx4 %0, %1 sc1" ::"v"(voff), "s"(sbase), "s"(ldsbase), "n"(IMM) : "memory", "scc");
}
typedef float f32x2 __attribute__((ext_vector_type(2)));
DI f32x2 fma2(f32x2 a, f32x2 b, f32x2 c) { return __builtin_elementwise_fma(a, b, c); }
DI unsigned pk2v(f32x2 v) { bf16x2_t p = {(__bf16)v.x, (__bf16)v.y}; return __builtin_bit_cast(unsigned, p); }
DI float max3(float a, float b, float c) { float r; asm("v_max3_f32 %0, %1, %2, %3" : "=v"(r) : "v"(a), "v"(b), "v"(c)); return r; }
DI float relu(float a) { float r; asm("v_max_f32 %0, 0, %1" : "=v"(r) : "v"(a)); return r; }
DI float bf2f(unsigned b) { return __uint_as_float(b << 16); }
DI unsigned pk2(float lo, float hi) { bf16x2_t p = {(__bf16)lo, (__bf16)hi}; return __builtin_bit_cast(unsigned, p); }
DI f32x16 mfma32(u32x4 a, u32x4 b, f32x16 c) {
  return __builtin_amdgcn_mfma_f32_32x32x16_bf16(__builtin_bit_cast(s16x8, a), __builtin_bit_cast(s16x8, b), c, 0, 0, 0);
}
DI f32x4 mfma16(u32x4 a, u32x4 b, f32x4 c) {
  return __builtin_amdgcn_mfma_f32_16x16x32_bf16(__builtin_bit_cast(s16x8, a), __builtin_bit_cast(s16x8, b), c, 0, 0, 0);
}
DI u32x4 lds128(unsigned a, int off = 0) { return *(const LAS u32x4*)((const LAS char*)(size_t)a + off); }
DI u32x4 tr_frag(unsigned a, int off, int second) {
  s16x4 lo = __builtin_amdgcn_ds_read_tr16_b64_v4i16((LAS s16x4*)((LAS char*)(size_t)a + off));
  s16x4 hi = __builtin_amdgcn_ds_read_tr16_b64_v4i16((LAS s16x4*)((LAS char*)(size_t)a + off + second));
  u32x2 x = __builtin_bit_cast(u32x2, lo), y = __builtin_bit_cast(u32x2, hi);
  return u32x4{x.x, x.y, y.x, y.y};
}
DI float dot2(unsigned a, unsigned b, float c) {
  return __builtin_amdgcn_fdot2_f32_bf16(__builtin_bit_cast(bf16x2_t, a), __builtin_bit_cast(bf16x2_t, b), c, false);
}
DI float sum8(u32x4 v, float c) {
  const unsigned one = 0x3f803f80u;
  c = dot2(v.x, one, c); c = dot2(v.y, one, c); c = dot2(v.z, one, c); return dot2(v.w, one, c);
}
DI float sq8(u32x4 v, float c) { c = dot2(v.x, v.x, c); c = dot2(v.y, v.y, c); c = dot2(v.z, v.z, c); return dot2(v.w, v.w, c); }
DI float vsum8(u32x4 v) {
  const float a = __uint_as_float(v.x << 16) + __uint_as_float(v.y << 16), b = __uint_as_float(v.x & 0xffff0000u) + __uint_as_float(v.y & 0xffff0000u);
  const float c = __uint_as_float(v.z << 16) + __uint_as_float(v.w << 16), d = __uint_as_float(v.z & 0xffff0000u) + __uint_as_float(v.w & 0xffff0000u);
  return (a + b) + (c + d);
}
DI void granule_store(char* ws, unsigned off, float v, unsigned tag) {
  __hip_atomic_store((GAS unsigned long long*)(ws + off), ((unsigned long long)tag << 32) | __float_as_uint(v), __ATOMIC_RELAXED, __HIP_MEMORY_SCOPE_AGENT);
}
DI unsigned long long granule_load(char* ws, unsigned off) {
  return __hip_atomic_load((GAS unsigned long long*)(ws + off), __ATOMIC_RELAXED, __HIP_MEMORY_SCOPE_AGENT);
}
template <int CTRL> DI float dpp_add(float v) {
  return v + __uint_as_float(__builtin_amdgcn_update_dpp(0u, __float_as_uint(v), CTRL, 0xf, 0xf, false));
}
DI float half_sum(float v) {
  v = dpp_add<0xB1>(v);
  v = dpp_add<0x4E>(v);
  v = dpp_add<0x141>(v);
  v = dpp_add<0x140>(v);
  auto r = __builtin_amdgcn_permlane16_swap(__float_as_uint(v), __float_as_uint(v), false, false);
  return __uint_as_float(r[0]) + __uint_as_float(r[1]);
}
DI float xhalf_add(float v) {
  auto r = __builtin_amdgcn_permlane32_swap(__float_as_uint(v), __float_as_uint(v), false, false);
  return __uint_as_float(r[0]) + __uint_as_float(r[1]);
}
DI float xhalf_max(float v) {
  auto r = __builtin_amdgcn_permlane32_swap(__float_as_uint(v), __float_as_uint(v), false, false);
  return fmaxf(__uint_as_float(r[0]), __uint_as_float(r[1]));
}
DI u32x4 pack_feat8(unsigned ax, unsigned ay, unsigned bx, unsigned by) {
  auto rx = __builtin_amdgcn_permlane32_swap(ax, bx, false, false);
  auto ry = __builtin_amdgcn_permlane32_swap(ay, by, false, false);
  return u32x4{rx[0], ry[0], rx[1], ry[1]};
}
DI f32x2 lo2(const f32x4& v) { return f32x2{v.x, v.y}; }
DI f32x2 hi2(const f32x4& v) { return f32x2{v.z, v.w}; }
template <int I> DI f32x2 pair(const f32x16& v) { return f32x2{v[2 * I], v[2 * I + 1]}; }
DI int fresh_lane() { int l; asm volatile("v_mbcnt_lo_u32_b32 %0, -1, 0\n\tv_mbcnt_hi_u32_b32 %0, -1, %0" : "=v"(l)); return l; }
typedef __amdgpu_buffer_rsrc_t rsrc_t;
DI void st128c(rsrc_t r, unsigned off, u32x4 v) { __builtin_amdgcn_raw_buffer_store_b128(v, r, off, 0, 16); }
DI void st64c(rsrc_t r, unsigned off, u32x2 v) { __builtin_amdgcn_raw_buffer_store_b64(v, r, off, 0, 16); }
DI u32x4 ld128c(rsrc_t r, unsigned off) { return __builtin_amdgcn_raw_buffer_load_b128(r, off, 0, 16); }
DI u32x2 ld64c(rsrc_t r, unsigned off) { return __builtin_amdgcn_raw_buffer_load_b64(r, off, 0, 16); }
DI unsigned flag_load(char* ws, int idx) {
  return __hip_atomic_load((GAS unsigned*)(ws + WS_FLAG) + idx * 16, __ATOMIC_RELAXED, __HIP_MEMORY_SCOPE_AGENT);
}
DI void flag_store(char* ws, int idx, unsigned v) {
  __hip_atomic_store((GAS unsigned*)(ws + WS_FLAG) + idx * 16, v, __ATOMIC_RELAXED, __HIP_MEMORY_SCOPE_AGENT);
}
DI unsigned read_epoch(const char* ws) {
  unsigned e;
  asm volatile("s_load_dword %0, %1, 0x0\n\ts_waitcnt lgkmcnt(0)" : "=s"(e) : "s"(ws + WS_FLAG + FL_EPOCH * 64) : "memory");
  return e;
}
DI void flags_wait(char* ws, int idx, unsigned tag) {
  for (int spin = 0; spin < (1 << 22); spin++) {
    const bool ok = idx < 0 || flag_load(ws, idx) == tag;
    if (__all(ok)) break;
    __builtin_amdgcn_s_sleep(1);
  }
  __builtin_amdgcn_fence(__ATOMIC_ACQUIRE, "wavefront");
}

#define STAMP_DECL
#define STAMP(i)
#define STAMP_DUMP(k)
#define STAMPC(i)

template <class Hook0, class Hook1>
DI void attention_task(rsrc_t rws, unsigned lds0, int w, int lane, int qi, int cgA, int cgX, int odd, u32x4 qf0, u32x4 qf1, Hook0&& mid, Hook1&& pre_store) {
  const int r32 = lane & 31, h = lane >> 5;
  const int q0 = 256 * qi + 32 * w;
  const unsigned ka = lds0 + L_K + h * 8192 + (32 * w + r32) * 16;
  const unsigned kx = lds0 + L_K + 16384 + (32 * w + r32) * 16;
  f32x16 sc[9];
  const f32x16 zero = {};
  {
    u32x4 k0[9], k1[9];
#pragma unroll
    for (int t = 0; t < 9; t++) k0[t] = lds128(ka, t * 512);
#pragma unroll
    for (int t = 0; t < 9; t++) k1[t] = lds128(kx, t * 512);
    SB();
#pragma unroll
    for (int t = 0; t < 9; t++) sc[t] = mfma32(k0[t], qf0, zero);
#pragma unroll
    for (int t = 0; t < 9; t++) sc[t] = mfma32(k1[t], qf1, sc[t]);
  }
  const unsigned va = r32 < 20 ? lds0 + L_V + (2 * w) * 640 + h * 320 + r32 * 16 : lds0 + AUX_C + (r32 == 20 ? 0 : 16);
  const int vstep = r32 < 20 ? 640 : 0;
  u32x4 vf[18];
#pragma unroll
  for (int t = 0; t < 18; t++) vf[t] = lds128(va + t * vstep);
  if (qi == 0) {
    const int tmin = 8 - w;
    sfor<0, 8>([&](auto T) {
      constexpr int t = T;
      if (t < tmin) {
#pragma unroll
        for (int i = 0; i < 16; i++) sc[t][i] = -INFINITY;
      }
    });
  }
#pragma unroll
  for (int i = 0; i < 16; i++) {
    const int kr = (i & 3) + 8 * (i >> 2) + 4 * h;
    if (kr <= r32) sc[0][i] = -INFINITY;
    if (kr > r32) sc[8][i] = -INFINITY;
  }
  float mxa[4];
#pragma unroll
  for (int i = 0; i < 4; i++) mxa[i] = max3(sc[0][4 * i], sc[0][4 * i + 1], max3(sc[0][4 * i + 2], sc[0][4 * i + 3], sc[0][4 * i + 3]));
#pragma unroll
  for (int t = 1; t < 9; t++)
#pragma unroll
    for (int i = 0; i < 8; i++) mxa[i & 3] = max3(mxa[i & 3], sc[t][2 * i], sc[t][2 * i + 1]);
  float mx = max3(mxa[0], mxa[1], max3(mxa[2], mxa[3], mxa[3]));
  {
    auto r = __builtin_amdgcn_permlane32_swap(__float_as_uint(mx), __float_as_uint(mx), false, false);
    mx = max3(__uint_as_float(r[0]), __uint_as_float(r[1]), __uint_as_float(r[1]));
  }
  mid();
  f32x16 o = {};
#pragma unroll
  for (int t = 0; t < 9; t++) {
    float p[16];
#pragma unroll
    for (int i = 0; i < 16; i++) p[i] = __builtin_amdgcn_exp2f(sc[t][i] - mx);
#pragma unroll
    for (int sp = 0; sp < 2; sp++) {
      const u32x4 pf = {pk2(p[8 * sp], p[8 * sp + 1]), pk2(p[8 * sp + 2], p[8 * sp + 3]), pk2(p[8 * sp + 4], p[8 * sp + 5]),
                        pk2(p[8 * sp + 6], p[8 * sp + 7])};
      o = mfma32(vf[2 * t + sp], pf, o);
    }
  }
  auto lr = __builtin_amdgcn_permlane32_swap(__float_as_uint(o[8]), __float_as_uint(o[8]), false, false);
  const float inv = __builtin_amdgcn_rcpf(__uint_as_float(lr[1]));
  const unsigned ob = WS_ACT + 192u * S_ * 16;
  SB();
  pre_store();
  SB();
  st128c(rws, ob + ((cgA + h) * S_ + q0 + r32) * 16,
         u32x4{pk2(o[0] * inv, o[1] * inv), pk2(o[2] * inv, o[3] * inv), pk2(o[4] * inv, o[5] * inv), pk2(o[6] * inv, o[7] * inv)});
  if (h == 0) st64c(rws, ob + (cgX * S_ + q0 + r32) * 16 + odd * 8, u32x2{pk2(o[8] * inv, o[9] * inv), pk2(o[10] * inv, o[11] * inv)});
}
DI void load_q(rsrc_t rws, int w, int lane, int qi, int cgA, int cgX, int odd, u32x4& qf0, u32x4& qf1) {
  const int r32 = lane & 31, h = lane >> 5, q0 = 256 * qi + 32 * w;
  qf0 = ld128c(rws, WS_QK + ((cgA + h) * S_ + q0 + r32) * 16);
  const u32x2 xq = ld64c(rws, WS_QK + (cgX * S_ + q0 + r32) * 16 + odd * 8);
  const unsigned x0 = h ? 0u : xq.x, x1 = h ? 0u : xq.y;
  qf1 = odd ? u32x4{0u, 0u, x0, x1} : u32x4{x0, x1, 0u, 0u};
}

__global__ __launch_bounds__(512, 2) void k_fused(const u16* __restrict__ x, const u16* __restrict__ wqkv, const u16* __restrict__ wo,
                                                  const u16* __restrict__ w1, const u16* __restrict__ b1, const u16* __restrict__ w2,
                                                  const u16* __restrict__ b2, char* __restrict__ ws, u16* __restrict__ out) {
  extern __shared__ __attribute__((aligned(16))) char smem[];
  LAS char* L = (LAS char*)smem;
  const unsigned lds0 = (unsigned)(size_t)L;
  const int tid = threadIdx.x, lane = tid & 63, w = __builtin_amdgcn_readfirstlane(tid >> 6);
  const int b = blockIdx.x, xcd = b & 7, bi = b >> 3;
  const int s = w & 3;
  const rsrc_t rws = __builtin_amdgcn_make_buffer_rsrc(ws, 0, WS_BYTES, 0x00020000);
  const int rb = (xcd & 1) * 8 + (bi >> 2), j = (xcd >> 1) * 4 + (bi & 3);
  const int row0 = rb * 128;
  const int kc = (5 * rb) >> 1, rot = kc >> 2, kc0 = kc & 3, kc1 = kc0 + 2 + (rb & 1);
  const int hd = xcd * 4 + (bi & 3), qi = bi >> 2;
  const int odd = hd & 1, cgA = (hd >> 1) * 5 + (odd ? 3 : 0), cgX = (hd >> 1) * 5 + 2;
  const int rb3 = (xcd >> 1) * 8 + (bi >> 2), cgp = (xcd & 1) * 4 + (bi & 3);
  const int row3 = rb3 * 64, n0 = cgp * 80;
  STAMP_DECL;
  STAMP(0); STAMPC(38);
  const int r32 = lane & 31, h = lane >> 5;
  const int blk = (lane >> 4) & 1, q4 = (lane & 15) >> 2, p4 = lane & 3;
  const unsigned a0 = lds0 + L_A + (32 * s + r32) * 128 + ((h ^ ((r32 >> 1) & 7)) * 16);
  auto rdA = [&](int p, int t) -> u32x4 {
    if (p % 5 < 4) return lds128(a0 ^ (t * 32), (p % 5) * 16384);
    unsigned ad;
    asm volatile("v_add_u32 %0, 0x10000, %1" : "=v"(ad) : "v"(a0 ^ (t * 32)));
    return lds128(ad);
  };
  const unsigned bb = lds0 + L_B + (8 * h + q4) * 256 + (4 * q4 + 2 * blk + (p4 >> 1)) * 16 + 8 * (p4 & 1);
  const unsigned fa = lds0 + L_B + (8 * h + q4) * 192 + (2 * blk + (p4 >> 1)) * 16 + 8 * (p4 & 1);
  constexpr int FP[5] = {32768, 49152, 61440, 0, 16384};
  auto rdB = [&](int p, int t, int u) { return tr_frag(bb ^ (64 * u), (p & 3) * 16384 + t * 4096, 1024); };
  auto rdF = [&](int f, int t, int u) { return tr_frag(fa, FP[f % 5] + t * 3072 + u * 64, 768); };

  if (w < 4) {
    const int srow = row0 + 8 * j + 2 * s + h;
    u32x2 sx[5];
#pragma unroll
    for (int i = 0; i < 5; i++) sx[i] = *(const u32x2*)(x + (size_t)srow * D_ + 128 * i + 4 * r32);
    const unsigned tag = read_epoch(ws) + 1u;
    u32x4 af[20], at[4];
    auto getA = [&](auto P, auto T) -> u32x4& { if constexpr (P < 5) return af[4 * P + T]; else return at[T]; };
    auto row_stats = [&]() {
      float v[20];
#pragma unroll
      for (int i = 0; i < 5; i++) {
        v[4 * i] = bf2f(sx[i].x & 0xffffu); v[4 * i + 1] = bf2f(sx[i].x >> 16); v[4 * i + 2] = bf2f(sx[i].y & 0xffffu); v[4 * i + 3] = bf2f(sx[i].y >> 16);
      }
      float sm = ((v[0] + v[1]) + (v[2] + v[3])) + ((v[4] + v[5]) + (v[6] + v[7])) + ((v[8] + v[9]) + (v[10] + v[11]));
      sm += ((v[12] + v[13]) + (v[14] + v[15])) + ((v[16] + v[17]) + (v[18] + v[19]));
      sm = half_sum(sm);
      const float mu = sm * (1.0f / D_);
      float q4[4] = {0.f, 0.f, 0.f, 0.f};
#pragma unroll
      for (int i = 0; i < 20; i++) q4[i & 3] = __builtin_fmaf(v[i] - mu, v[i] - mu, q4[i & 3]);
      float sq = (q4[0] + q4[1]) + (q4[2] + q4[3]);
      sq = half_sum(sq);
      const float r = __builtin_amdgcn_rsqf(sq * (1.0f / D_) + 1e-5f);
      const int lc = fresh_lane();
      if ((lc & 31) < 2) granule_store(ws, WS_STAT + (row0 + 8 * j + 2 * s + (lc >> 5)) * 16 + (lc & 1) * 8, (lc & 1) ? r : mu * r, tag);
    };
    unsigned long long stg = 0;
    auto stoff = [&]() { const int lc = fresh_lane(); return WS_STAT + (row0 + 32 * s + (lc & 31)) * 16 + (lc >> 5) * 8; };
    auto conv = [&](int hc) {
      const int kb = hc / 5, cb = hc % 5, lc = fresh_lane();
      const unsigned dst = WS_WT + ((kb * 4 + 2 * (s >> 1) + (lc >> 5)) * D_ + cb * 128 + 64 * (s & 1) + (lc & 31)) * 16;
      const unsigned src = (bb ^ (128 * (s & 1))) + (s >> 1) * 4096;
      st128c(rws, dst, tr_frag(src, L_T, 1024));
      st128c(rws, dst + 512, tr_frag(src ^ 64, L_T, 1024));
    };
    f32x16 acc[4] = {};
    auto body = [&](auto MODE) {
      u32x4 bs[3][4];
      auto mm = [&](auto N, int u) {
        constexpr int n = N;
        if constexpr (MODE != 2) {
          const u32x4 a = getA(IC<n / 4>{}, IC<n % 4>{});
          if constexpr (MODE == 0) acc[u] = mfma32(bs[n % 3][u], a, acc[u]); else acc[u] = mfma32(a, bs[n % 3][u], acc[u]);
        }
      };
      auto rB = [&](int g, int t, int u) { if constexpr (MODE != 2) bs[(4 * g + t) % 3][u] = rdB(g, t, u); };
      sfor<0, 10>([&](auto G) {
        constexpr int g = G;
        lds_bar();
        STAMP(1 + g);
#pragma unroll
        for (int u = 0; u < 4; u++) rB(g, 0, u);
        sfor<1, 4>([&](auto T) {
          constexpr int t = T;
          SB();
#pragma unroll
          for (int u = 0; u < 4; u++) {
            if constexpr (4 * g + t - 3 >= 0) mm(IC<(4 * g + t - 3 >= 0 ? 4 * g + t - 3 : 0)>{}, u);
            if (t == 1 && u == 3) getA(G, IC<0>{}) = rdA(g, 0);
            if (t == 1 && u == 3) getA(G, IC<1>{}) = rdA(g, 1);
            if (t == 2 && u == 3) getA(G, IC<2>{}) = rdA(g, 2);
            if (t == 3 && u == 0) getA(G, IC<3>{}) = rdA(g, 3);
            rB(g, t, u);
            SB();
          }
        });
#pragma unroll
        for (int u = 0; u < 4; u++) mm(IC<4 * g + 1>{}, u);
        if constexpr (g == 8) stg = granule_load(ws, stoff());
        if constexpr (g == 0) { SB(); row_stats(); }
        if constexpr (g == 3) conv(b);
        if constexpr (g == 7) { if (b < 84) conv(256 + b); }
      });
#pragma unroll
      for (int u = 0; u < 4; u++) mm(IC<38>{}, u);
#pragma unroll
      for (int u = 0; u < 4; u++) mm(IC<39>{}, u);
    };
    if (j < 10) body(IC<0>{}); else if (j < 15) body(IC<1>{}); else body(IC<2>{});
    STAMP(11);
    float mur, rs;
    {
      for (int spin = 0; spin < (1 << 22) && !__all((unsigned)(stg >> 32) == tag); spin++) stg = granule_load(ws, stoff());
      auto r = __builtin_amdgcn_permlane32_swap((unsigned)stg, (unsigned)stg, false, false);
      mur = __uint_as_float(r[0]); rs = __uint_as_float(r[1]);
    }
    const int l2 = fresh_lane(), r2 = l2 & 31, h2 = l2 >> 5;
    if (j >= 10 && h2 == 0) {
      ((LAS float*)(L + AUX_ST))[32 * s + r2] = mur;
      ((LAS float*)(L + AUX_ST))[128 + 32 * s + r2] = rs;
    }
    wait_bar<0>();
    if (j < 10) {
      const float sc = (j < 5) ? QSCALE : 1.0f;
      const f32x2 a2 = {rs * sc, rs * sc}, b2q = {-mur * sc, -mur * sc};
      const unsigned dst = WS_QK + ((16 * j + h2) * S_ + row0 + 32 * s + r2) * 16;
      const unsigned csa = lds0 + AUX_CS + 16 * h2;
      sfor<0, 2>([&](auto UU) {
        f32x4 cs[8];
#pragma unroll
        for (int i = 0; i < 8; i++) cs[i] = __builtin_bit_cast(f32x4, lds128(csa, (64 * UU + 8 * i) * 4));
        SB();
        sfor<0, 4>([&](auto C) {
          constexpr int u = 2 * UU + C / 2, k = C % 2, ci = 2 * C;
          const unsigned p0 = pk2v(fma2(pair<4 * k>(acc[u]), a2, lo2(cs[ci]) * b2q)), p1 = pk2v(fma2(pair<4 * k + 1>(acc[u]), a2, hi2(cs[ci]) * b2q));
          const unsigned p2 = pk2v(fma2(pair<4 * k + 2>(acc[u]), a2, lo2(cs[ci + 1]) * b2q)), p3 = pk2v(fma2(pair<4 * k + 3>(acc[u]), a2, hi2(cs[ci + 1]) * b2q));
          st128c(rws, dst + (4 * u + 2 * k) * (S_ * 16), pack_feat8(p0, p1, p2, p3));
        });
      });
    } else if (j < 15) {
      f32x4 murv[4], rsv[4];
      float csv[4];
#pragma unroll
      for (int g = 0; g < 4; g++) {
        murv[g] = *(const LAS f32x4*)(L + AUX_ST + (32 * s + 8 * g + 4 * h2) * 4);
        rsv[g] = *(const LAS f32x4*)(L + AUX_ST + 512 + (32 * s + 8 * g + 4 * h2) * 4);
        csv[g] = -*(const LAS float*)(L + AUX_CS + (32 * g + r2) * 4);
      }
      sfor<0, 4>([&](auto U) {
        constexpr int u = U;
        const f32x2 cs2 = {csv[u], csv[u]};
        const int colv = 128 * (j - 10) + 32 * u + r2;
        const int head = colv / 20, d = colv - head * 20;
        const int dd = (head & 1) ? (d < 4 ? d + 16 : d - 4) : d;
        const int slot = dd < 16 ? ((dd & 3) + 8 * ((dd >> 2) & 1) + 4 * (dd >> 3)) : dd;
        const unsigned dst = WS_VT + ((head * 128 + (row0 + 32 * s) / 16) * 2 + h2) * 320 + slot * 16;
        sfor<0, 2>([&](auto SP) {
          constexpr int sp = SP;
          const unsigned p0 = pk2v(fma2(pair<4 * sp>(acc[u]), lo2(rsv[2 * sp]), cs2 * lo2(murv[2 * sp])));
          const unsigned p1 = pk2v(fma2(pair<4 * sp + 1>(acc[u]), hi2(rsv[2 * sp]), cs2 * hi2(murv[2 * sp])));
          const unsigned p2 = pk2v(fma2(pair<4 * sp + 2>(acc[u]), lo2(rsv[2 * sp + 1]), cs2 * lo2(murv[2 * sp + 1])));
          const unsigned p3 = pk2v(fma2(pair<4 * sp + 3>(acc[u]), hi2(rsv[2 * sp + 1]), cs2 * hi2(murv[2 * sp + 1])));
          st128c(rws, dst + sp * 640, u32x4{p0, p1, p2, p3});
        });
      });
    }
    STAMP(12);
    f32x16 facc[3] = {};
    {
      u32x4 bs[3][3];
      auto pos = [](int f) { return (f + 5) % 10; };
      auto mm = [&](auto N, int u) {
        constexpr int n = N;
        facc[u] = mfma32(bs[n % 3][u], getA(IC<(n / 4 + 5) % 10>{}, IC<n % 4>{}), facc[u]);
      };
      sfor<0, 10>([&](auto Fs) {
        constexpr int f = Fs;
        if constexpr (f == 3) wait_bar<0>();
        else if constexpr (f > 0 && f < 9) lds_bar();
        STAMP(13 + f);
#pragma unroll
        for (int u = 0; u < 3; u++) bs[(4 * f) % 3][u] = rdF(f, 0, u);
        sfor<1, 4>([&](auto T) {
          constexpr int t = T;
          SB();
#pragma unroll
          for (int u = 0; u < 3; u++) {
            if constexpr (4 * f + t - 3 >= 0) mm(IC<(4 * f + t - 3 >= 0 ? 4 * f + t - 3 : 0)>{}, u);
            if constexpr (f < 5) {
              if (t == 1 && u == 2) at[0] = rdA(pos(f), 0);
              if (t == 1 && u == 2) at[1] = rdA(pos(f), 1);
              if (t == 2 && u == 2) at[2] = rdA(pos(f), 2);
              if (t == 3 && u == 0) at[3] = rdA(pos(f), 3);
            }
            bs[(4 * f + t) % 3][u] = rdF(f, t, u);
            SB();
          }
        });
#pragma unroll
        for (int u = 0; u < 3; u++) mm(IC<4 * f + 1>{}, u);
      });
#pragma unroll
      for (int u = 0; u < 3; u++) mm(IC<38>{}, u);
#pragma unroll
      for (int u = 0; u < 3; u++) mm(IC<39>{}, u);
    }
    STAMP(23);
    {
      const int l3 = fresh_lane(), r3 = l3 & 31, h3 = l3 >> 5;
      while (true) {
        const u32x4 rdy = *(const volatile LAS u32x4*)(L + AUX_W);
        if (__builtin_amdgcn_readfirstlane(rdy.x & rdy.y & rdy.z) == 1u) break;
      }
      asm volatile("" ::: "memory");
      const unsigned dst = WS_ACT + ((12 * j + h3) * S_ + row0 + 32 * s + r3) * 16;
      const unsigned ca = lds0 + AUX_CF + 16 * h3;
      const f32x2 rs2 = {rs, rs}, nm2 = {-mur, -mur};
      auto act = [&](f32x2 a, f32x2 cs, f32x2 bv) {
        f32x2 t = fma2(a, rs2, fma2(cs, nm2, bv));
        t = f32x2{relu(t.x), relu(t.y)};
        return pk2v(t * t);
      };
      sfor<0, 3>([&](auto U) {
        constexpr int u = U;
        f32x4 cs[4], bv[4];
#pragma unroll
        for (int i = 0; i < 4; i++) {
          cs[i] = __builtin_bit_cast(f32x4, lds128(ca, (32 * u + 8 * i) * 4));
          bv[i] = __builtin_bit_cast(f32x4, lds128(ca, AUX_BF - AUX_CF + (32 * u + 8 * i) * 4));
        }
        SB();
        sfor<0, 2>([&](auto K) {
          constexpr int k = K;
          const unsigned p0 = act(pair<4 * k>(facc[u]), lo2(cs[2 * k]), lo2(bv[2 * k])), p1 = act(pair<4 * k + 1>(facc[u]), hi2(cs[2 * k]), hi2(bv[2 * k]));
          const unsigned p2 = act(pair<4 * k + 2>(facc[u]), lo2(cs[2 * k + 1]), lo2(bv[2 * k + 1]));
          const unsigned p3 = act(pair<4 * k + 3>(facc[u]), hi2(cs[2 * k + 1]), hi2(bv[2 * k + 1]));
          st128c(rws, dst + (4 * u + 2 * k) * (S_ * 16), pack_feat8(p0, p1, p2, p3));
        });
      });
    }
    STAMP(24);
    while (__builtin_amdgcn_readfirstlane(*(const volatile LAS unsigned*)(L + AUX_W + 16 + 4 * s)) != 1u) __builtin_amdgcn_s_sleep(1);
    asm volatile("" ::: "memory");
    const int lanem = fresh_lane();
    u32x4 qf0, qf1;
    load_q(rws, w, lanem, qi, cgA, cgX, odd, qf0, qf1);
    wait_bar<0>();
    STAMP(25);
    const int mh = s & 1, kh = s >> 1;
    const int rot3 = ((bi >> 2) + 3 * (bi & 3)) % 12, rot4 = ((bi >> 2) + (bi & 3)) % 5;
    auto kq = [&](int st) { int v; if (st < 12) { v = st + rot3; if (v >= 12) v -= 12; } else { v = st + rot4; if (v >= 17) v -= 5; } return v; };
    u32x4 ar[4][4];
    unsigned avo;
    auto ldA = [&](auto ST) {
      constexpr int st = ST;
      const unsigned so = kq(st) * (16 * S_ * 16);
#pragma unroll
      for (int i = 0; i < 4; i++) ar[st & 3][i] = __builtin_amdgcn_raw_buffer_load_b128(rws, avo + (i >> 1) * (8 * S_ * 16) + (i & 1) * 256, so, 16);
    };
    unsigned f2 = tag;
    attention_task(rws, lds0, w, lanem, qi, cgA, cgX, odd, qf0, qf1,
                   [&]() { if (lanem < 16) f2 = flag_load(ws, FL_2 + (rb3 >> 1) * 16 + lanem); },
                   [&]() {
                     if (!__all(f2 == tag)) flags_wait(ws, lanem < 16 ? FL_2 + (rb3 >> 1) * 16 + lanem : -1, tag);
                     avo = WS_ACT + ((4 * kh + (lanem >> 4)) * S_ + row3 + 32 * mh + (lanem & 15)) * 16;
                     ldA(IC<0>{}); ldA(IC<1>{}); ldA(IC<2>{});
                   });
    STAMP(26);
    const int tok = row3 + 32 * mh + 16 * kh + (lanem & 15);
    u32x2 xv[5], bv[5];
    f32x4 accf[2][5] = {}, accs[2][5] = {};
    const unsigned wr = lds0 + kh * 5120 + lanem * 16;
    u32x4 fw[2][5];
    unsigned f3 = tag;
    auto mm3 = [&](auto ST, auto KK) {
      constexpr int st = ST, kk = KK;
#pragma unroll
      for (int i = 0; i < 5; i++)
#pragma unroll
        for (int r = 0; r < 2; r++) {
          if constexpr (st < 12) accf[r][i] = mfma16(fw[kk][i], ar[st & 3][2 * kk + r], accf[r][i]);
          else accs[r][i] = mfma16(fw[kk][i], ar[st & 3][2 * kk + r], accs[r][i]);
        }
    };
    sfor<0, 17>([&](auto ST) {
      constexpr int st = ST;
      if constexpr (st == 3) wait_bar<12>(); else lds_bar();
      if constexpr (st == 0) { STAMP(27); }
      if constexpr (st == 12) { STAMP(28); }
#pragma unroll
      for (int i = 0; i < 5; i++) fw[0][i] = lds128(wr, W3[st % 7] + i * 1024);
      SB();
      if constexpr (st > 0) mm3(IC<(st > 0 ? st - 1 : 0)>{}, IC<1>{});
      SB();
#pragma unroll
      for (int i = 0; i < 5; i++) fw[1][i] = lds128(wr, W3[st % 7] + 10240 + i * 1024);
      if constexpr (st == 7) { if (lanem < 32) f3 = flag_load(ws, FL_3 + lanem * 8 + (rb3 >> 2)); }
      if constexpr (st == 9) { if (!__all(f3 == tag)) flags_wait(ws, lanem < 32 ? FL_3 + lanem * 8 + (rb3 >> 2) : -1, tag); }
      if constexpr (st + 3 < 17) ldA(IC<(st + 3 < 17 ? st + 3 : 0)>{});
      if constexpr (st == 14) {
#pragma unroll
        for (int i = 0; i < 5; i++) {
          xv[i] = *(const u32x2*)(x + (size_t)tok * D_ + n0 + 16 * i + 4 * (lanem >> 4));
          bv[i] = *(const u32x2*)(b2 + n0 + 16 * i + 4 * (lanem >> 4));
        }
      }
      SB();
      mm3(ST, IC<0>{});
    });
    mm3(IC<16>{}, IC<1>{});
    STAMP(29);
    f32x4 gf[5], gs[5];
    {
      LAS f32x4* ex = (LAS f32x4*)(L + W3[5]) + s * 640 + lanem;
#pragma unroll
      for (int i = 0; i < 5; i++) { ex[i * 64] = kh ? accf[0][i] : accf[1][i]; ex[320 + i * 64] = kh ? accs[0][i] : accs[1][i]; }
      lds_bar();
      const LAS f32x4* ey = (const LAS f32x4*)(L + W3[5]) + (s ^ 2) * 640 + lanem;
#pragma unroll
      for (int i = 0; i < 5; i++) { gf[i] = (kh ? accf[1][i] : accf[0][i]) + ey[i * 64]; gs[i] = (kh ? accs[1][i] : accs[0][i]) + ey[320 + i * 64]; }
    }
#pragma unroll
    for (int i = 0; i < 5; i++) {
      const float xf[4] = {bf2f(xv[i].x & 0xffffu), bf2f(xv[i].x >> 16), bf2f(xv[i].y & 0xffffu), bf2f(xv[i].y >> 16)};
      const float bf[4] = {bf2f(bv[i].x & 0xffffu), bf2f(bv[i].x >> 16), bf2f(bv[i].y & 0xffffu), bf2f(bv[i].y >> 16)};
      float r[4];
#pragma unroll
      for (int e = 0; e < 4; e++) {
        const float g = gf[i][e] + bf[e];
        const float sg = g * __builtin_amdgcn_rcpf(1.0f + __builtin_amdgcn_exp2f(-1.4426950408889634f * g));
        r[e] = __builtin_fmaf(gs[i][e], sg, xf[e]);
      }
      *(u32x2*)(out + (size_t)tok * D_ + n0 + 16 * i + 4 * (lanem >> 4)) = u32x2{pk2(r[0], r[1]), pk2(r[2], r[3])};
    }
    STAMP(30); STAMPC(39);
    if (b == 0 && w == 0) {
#pragma unroll
      for (int i = 0; i < 4; i++) flags_wait(ws, FL_2 + 64 * i + lanem, tag);
      if (lanem == 0) flag_store(ws, FL_EPOCH, tag);
    }
    if (w == 0) { STAMP_DUMP(0); }
  } else {
    const char* xbase = (const char*)(x + (size_t)row0 * D_);
    const char* fbase = (const char*)(w1 + 96 * j);
    const char* qbase = (const char*)(wqkv + 128 * (j < 15 ? j : 14));
    unsigned voffA[4], voffB[4], voffF[3];
#pragma unroll
    for (int e = 0; e < 4; e++) {
      voffA[e] = ((32 * s + 8 * e + (lane >> 3)) * D_ + (((lane & 7) ^ (4 * (e & 1) + (lane >> 4))) * 8)) * 2;
      voffB[e] = (16 * s + 4 * e + (lane >> 4)) * (NQKV * 2) + ((lane & 15) ^ ((lane >> 4) << 2)) * 16;
    }
    const unsigned voffT = (8 * s + (lane >> 4)) * (D_ * 2) + ((lane & 15) ^ ((lane >> 4) << 2)) * 16;
#pragma unroll
    for (int e = 0; e < 3; e++) { const int Lb = e * 1024 + lane * 16; voffF[e] = (16 * s + Lb / 192) * (F_ * 2) + Lb % 192; }
    const unsigned l4 = lds0 + s * 4096, l3k = lds0 + s * 3072;
    auto kst = [&](int p) { const int v = p + rot; return v >= 10 ? v - 10 : v; };
    auto dmaP = [&](auto P, auto E, const char* xs, const char* qs) {
      constexpr int p = P, e = E;
      if constexpr (e < 4) glds16<L_A + (p % 5) * 16384 + e * 1024>(voffA[e], xs, l4);
      else glds16<L_B + (p & 3) * 16384 + (e - 4) * 1024>(voffB[e - 4], qs, l4);
    };
    auto dmaF = [&](auto Fs) {
      constexpr int f = Fs;
      const char* src = fbase + (size_t)(kst((f + 5) % 10) * (64 * F_ * 2));
      glds16<L_B + FP[f % 5]>(voffF[0], src, l3k); glds16<L_B + FP[f % 5] + 1024>(voffF[1], src, l3k); glds16<L_B + FP[f % 5] + 2048>(voffF[2], src, l3k);
    };
    auto dmaT = [&](int hc) {
      const unsigned kb = (unsigned)hc / 5u, cb = hc - kb * 5;
      const size_t base = kb < 48 ? (size_t)w2 : (size_t)wo - (size_t)48 * 32 * D_ * 2;
      const char* src = (const char*)(base + (size_t)(kb * (32 * D_ * 2) + cb * 256));
      glds16<L_T>(voffT, src, lds0 + s * 2048);
      glds16<L_T + 1024>(voffT, src + 4 * D_ * 2, lds0 + s * 2048);
    };
    if (w == 4) { if (lane < 12) glds16<AUX_B1>(lane * 16, (const char*)(b1 + 96 * j), lds0); }
    sfor<0, 3>([&](auto P) {
      const char* xs = xbase + kst(P) * 128;
      const char* qs = qbase + (size_t)(kst(P) * (64 * NQKV * 2));
      sfor<0, 8>([&](auto E) { dmaP(P, E, xs, qs); });
    });
    if (w == 5 && lane < 16) ((LAS unsigned*)(L + AUX_C))[lane] = lane < 4 ? 0x3f803f80u : 0u;
    const unsigned tag = read_epoch(ws) + 1u;
    const unsigned cspub = WS_CSP + ((j * 16 + rb) * 224 + 32 * s + r32) * 8;
    const char* csgat = ws + WS_CSP + (size_t)(j * 16) * 224 * 8;
    const unsigned voffG = (8 * h * 224 + 32 * s + r32) * 8;
    u32x2 gr[8];
    auto gather_issue = [&](int off) {
#pragma unroll
      for (int i = 0; i < 8; i++)
        asm volatile("global_load_dwordx2 %0, %1, %2 sc1" : "=v"(gr[i]) : "v"(voffG), "s"(csgat + off + i * 224 * 8) : "memory");
    };
    auto gather_sum = [&](int off) -> float {
      for (int spin = 0; spin < (1 << 22); spin++) {
#pragma unroll
        for (int i = 0; i < 8; i++) asm volatile("" : "+v"(gr[i]));
        bool ok = true;
#pragma unroll
        for (int i = 0; i < 8; i++) ok &= gr[i].y == tag;
        if (__all(ok)) break;
        gather_issue(off);
        drain();
      }
      float a = 0.f;
#pragma unroll
      for (int i = 0; i < 8; i++) a += __uint_as_float(gr[i].x);
      return xhalf_add(a);
    };
    float cpart = 0.f;
    sfor<0, 10>([&](auto G) {
      constexpr int g = G;
      constexpr int NW[10] = {16, 18, 18, 17, 17, 18, 18, 24, 11, 9};
      wait_bar<NW[g]>();
      if constexpr (g == 1) { if (w == 7 && lane < 48) {
        const unsigned v = *(const LAS unsigned*)(L + AUX_B1 + lane * 4);
        *(LAS u32x2*)(L + AUX_BF + lane * 8) = u32x2{v << 16, v & 0xffff0000u};
      } }
      if constexpr (g == 0) dmaT(b);
      if constexpr (g == 4) dmaT(b < 84 ? 256 + b : b);
      if constexpr (g == 2) { cpart = xhalf_add(cpart); if (h == 0) granule_store(ws, cspub, cpart, tag); else asm volatile("" ::: "memory"); }
      if constexpr (g == 6) gather_issue(0);
      const char* xs = xbase + kst(g < 7 ? g + 3 : 0) * 128;
      const char* qs = qbase + (size_t)(kst(g < 7 ? g + 3 : 0) * (64 * NQKV * 2));
      if constexpr (g < 7) sfor<0, 8>([&](auto E) { dmaP(IC<g + 3>{}, E, xs, qs); });
      else if constexpr (g == 7) dmaF(IC<0>{});
      else if constexpr (g == 8) { dmaF(IC<1>{}); dmaF(IC<2>{}); }
      else dmaF(IC<3>{});
      if constexpr (g < 2) {
#pragma unroll
        for (int t = 0; t < 4; t++)
          if (4 * g + t >= kc0 && 4 * g + t < kc1) cpart += vsum8(tr_frag(bb ^ (64 * s), (g & 3) * 16384 + t * 4096, 1024));
      }
      if constexpr (g == 9) {
        const float cs = gather_sum(0);
        if (h == 0) ((LAS float*)(L + AUX_CS))[32 * s + r32] = cs;
      }
    });
    cpart = 0.f;
    const int uf = s < 3 ? s : 2;
    unsigned voffW[5];
#pragma unroll
    for (int i = 0; i < 5; i++) voffW[i] = ((lane >> 4) * D_ + (lane & 15) + 16 * i) * 16;
    const char* wbase = ws + WS_WT + (size_t)n0 * 16 + (size_t)s * 4 * D_ * 16;
    const int rot3 = ((bi >> 2) + 3 * (bi & 3)) % 12, rot4 = ((bi >> 2) + (bi & 3)) % 5;
    auto kq = [&](int st) { int v; if (st < 12) { v = st + rot3; if (v >= 12) v -= 12; } else { v = st + rot4; if (v >= 17) v -= 5; } return v; };
    const unsigned l5 = lds0 + s * 5120;
    auto issueW = [&](auto ST) {
      constexpr int st = ST;
      const char* ww = wbase + (size_t)(kq(st) * (16 * D_ * 16));
      sfor<0, 5>([&](auto I) { glds16c<W3[st % 7] + I * 1024>(voffW[I], ww, l5); });
    };
    int idxq;
    {
      const int ty = lane >> 3, rbi = (lane >> 1) & 3, hi = lane & 1;
      const int c0 = ty == 2 ? 20 * hd : 8 * (odd ? cgX : cgA), c1 = ty == 2 ? 20 * hd + 19 : 8 * (odd ? cgA + 1 : cgX) + 7;
      const int tile = 5 * ty + (hi ? c1 : c0) / 128, rr = 2 * qi - 2 + rbi;
      idxq = (lane < 24 && rr >= 0 && (ty > 0 || rbi >= 2)) ? FL_1 + rr * 16 + tile : FL_1 + rb * 16 + j;
    }
    unsigned pv[4];
    auto poll_issue = [&](unsigned& dst, int idx) {
      asm volatile("global_load_dword %0, %1, %2 sc1" : "=v"(dst) : "v"(idx * 64), "s"(ws + WS_FLAG) : "memory");
    };
    sfor<0, 9>([&](auto Fs) {
      constexpr int f = Fs;
      if constexpr (f <= 5) wait_bar<9>(); else if constexpr (f == 6) wait_bar<6>(); else if constexpr (f == 7) lds_bar(); else wait_bar<19>();
      if constexpr (f == 0) { if (w == 4 && lane == 0) flag_store(ws, FL_T + b, tag); }
      if constexpr (f == 3) { if (w == 4 && lane == 0) flag_store(ws, FL_1 + rb * 16 + j, tag); }
      if constexpr (f == 2) { cpart = xhalf_add(cpart); if (h == 0 && s < 3) granule_store(ws, cspub + 128 * 8, cpart, tag); }
      if constexpr (f == 4) {
#pragma unroll
        for (int i = 0; i < 4; i++) poll_issue(pv[i], FL_T + 64 * i + lane);
      }
      if constexpr (f < 6) dmaF(IC<(f < 6 ? f + 4 : 0)>{});
      if constexpr (f == 6) {
#pragma unroll
        for (int i = 0; i < 4; i++) asm volatile("" : "+v"(pv[i]));
        if (!__all(pv[0] == tag && pv[1] == tag && pv[2] == tag && pv[3] == tag)) {
#pragma unroll
          for (int i = 0; i < 4; i++) flags_wait(ws, FL_T + 64 * i + lane, tag);
        }
        issueW(IC<0>{});
      }
      if constexpr (f == 7) { gather_issue(128 * 8); poll_issue(pv[0], idxq); issueW(IC<1>{}); }
      if constexpr (f < 2) {
#pragma unroll
        for (int t = 0; t < 4; t++)
          if (4 * f + t >= kc0 && 4 * f + t < kc1) cpart += vsum8(tr_frag(fa + uf * 64, FP[f % 5] + t * 3072, 768));
      }
    });
    asm volatile("s_waitcnt vmcnt(5)" ::: "memory");
    if (s < 3) {
      const float cf = gather_sum(128 * 8);
      if (h == 0) ((LAS float*)(L + AUX_CF))[32 * s + r32] = cf;
      asm volatile("s_waitcnt lgkmcnt(0)" ::: "memory");
      if (lane == 0) *(volatile LAS unsigned*)(L + AUX_W + 4 * s) = 1u;
    }
    STAMP(1);
    asm volatile("" : "+v"(pv[0]));
    if (!__all(pv[0] == tag)) flags_wait(ws, idxq, tag);
    if (lane == 0) *(volatile LAS unsigned*)(L + AUX_W + 16 + 4 * s) = 1u;
    STAMP(2);
    {
      const int key0 = 256 * qi - 256;
      const char* kb = ws + WS_QK + (size_t)80 * S_ * 16;
      const u32x4 z = {0u, 0u, 0u, 0u};
#pragma unroll
      for (int i = 0; i < 2; i++) {
        const int pc = 2 * s + i, kk = key0 + 64 * pc;
        if (kk >= 0) {
          glds16c<L_K>(lane * 16, kb + ((size_t)cgA * S_ + kk) * 16, lds0 + pc * 1024);
          glds16c<L_K + 8192>(lane * 16, kb + ((size_t)(cgA + 1) * S_ + kk) * 16, lds0 + pc * 1024);
          glds16c<L_K + 16384>(lane * 16, kb + ((size_t)cgX * S_ + kk) * 16, lds0 + pc * 1024);
        } else {
#pragma unroll
          for (int c = 0; c < 3; c++) *(LAS u32x4*)(L + L_K + c * 8192 + pc * 1024 + lane * 16) = z;
        }
      }
      const char* vb = ws + WS_VT + ((long)hd * 128 + key0 / 16) * 640;
#pragma unroll
      for (int i = 0; i < 5; i++) {
        const int pc = 5 * s + i;
        if (qi > 0 || pc >= 10) glds16c<L_V>(lane * 16, vb + pc * 1024, lds0 + pc * 1024);
        else *(LAS u32x4*)(L + L_V + pc * 1024 + lane * 16) = z;
      }
    }
    u32x4 qf0, qf1;
    load_q(rws, w, lane, qi, cgA, cgX, odd, qf0, qf1);
    wait_bar<0>();
    STAMP(3);
    if (w == 4 && lane == 0) flag_store(ws, FL_2 + rb * 16 + j, tag);
    issueW(IC<2>{}); issueW(IC<3>{}); issueW(IC<4>{});
    attention_task(rws, lds0, w, lane, qi, cgA, cgX, odd, qf0, qf1, []() {}, []() {});
    STAMP(4);
    lds_bar();
    STAMP(6);
    issueW(IC<5>{}); issueW(IC<6>{});
    sfor<1, 17>([&](auto ST) {
      constexpr int st = ST;
      wait_bar<(st == 3) ? 20 : ((st <= 11) ? 25 : 5 * (16 - st))>();
      if constexpr (st == 3) { if (w == 4 && lane == 0) flag_store(ws, FL_3 + hd * 8 + qi, tag); }
      if constexpr (st + 6 < 17) issueW(IC<(st + 6 < 17 ? st + 6 : 0)>{});
    });
    lds_bar();
    STAMP(7);
    if (w == 4) { STAMP_DUMP(1); }
  }
}

extern "C" void kernel_launch(void* const* d_in, const int* in_sizes, int n_in, void* d_out, int out_size, void* d_ws, size_t ws_size,
                              hipStream_t stream) {
  (void)hipFuncSetAttribute((const void*)k_fused, hipFuncAttributeMaxDynamicSharedMemorySize, LDS_BYTES);
  k_fused<<<256, 512, LDS_BYTES, stream>>>((const u16*)d_in[0], (const u16*)d_in[1], (const u16*)d_in[2], (const u16*)d_in[4],
                                           (const u16*)d_in[5], (const u16*)d_in[6], (const u16*)d_in[7], (char*)d_ws, (u16*)d_out);
}
```

```cpp
#include <hip/hip_runtime.h>
#include <type_traits>

typedef unsigned short u16;
typedef unsigned u32x4 __attribute__((ext_vector_type(4)));
typedef unsigned u32x2 __attribute__((ext_vector_type(2)));
typedef float f32x4 __attribute__((ext_vector_type(4)));
typedef float f32x16 __attribute__((ext_vector_type(16)));
typedef short s16x8 __attribute__((ext_vector_type(8)));
typedef short s16x4 __attribute__((ext_vector_type(4)));
typedef __bf16 bf16x2_t __attribute__((ext_vector_type(2)));
#define LAS __attribute__((address_space(3)))
#define GAS __attribute__((address_space(1)))
#define DI __device__ __forceinline__
#define SB() __builtin_amdgcn_sched_barrier(0)
template <int N> using IC = std::integral_constant<int, N>;

constexpr int S_ = 2048, D_ = 640, F_ = 1536, NQKV = 1920;

constexpr unsigned WS_QK = 0;
constexpr unsigned WS_VT = 0x500000;
constexpr unsigned WS_ACT = 0x800000;
constexpr unsigned WS_WT = 0x1100000;
constexpr unsigned WS_FLAG = 0x1400000;
constexpr unsigned WS_STAT = 0x1418000;
constexpr unsigned WS_CSP = 0x1420000;
constexpr unsigned WS_BYTES = 0x14a0000;
constexpr int FL_EPOCH = 0, FL_1 = 16, FL_2 = FL_1 + 256, FL_3 = FL_2 + 256, FL_T = FL_3 + 256;

constexpr int L_B = 0;
constexpr int L_T = 65536;
constexpr int L_A = 78592;
constexpr int L_K = 115456;
constexpr int L_V = L_K + 24576;
constexpr int L_AUX = 160512;
constexpr int AUX_ST = L_AUX;
constexpr int AUX_CS = AUX_ST + 1024;
constexpr int AUX_CF = AUX_CS + 512;
constexpr int AUX_B1 = AUX_CF + 512;
constexpr int AUX_BF = AUX_B1 + 256;
constexpr int AUX_C = AUX_BF + 512;
constexpr int AUX_W = AUX_C + 32;
constexpr int LDS_BYTES = 163840;
constexpr int W3[7] = {73728, 94208, 0, 20480, 40960, 115456, 135936};
constexpr float QSCALE = 0.22360679774997896f * 1.4426950408889634f;

template <int I, int N, class Fn> DI void sfor(Fn&& f) {
  if constexpr (I < N) { f(IC<I>{}); sfor<I + 1, N>(f); }
}
template <int N> DI void wait_bar() { SB(); asm volatile("s_waitcnt vmcnt(%0) lgkmcnt(0)\n\ts_barrier" ::"n"(N) : "memory"); SB(); }
DI void lds_bar() { SB(); asm volatile("s_waitcnt lgkmcnt(0)\n\ts_barrier" ::: "memory"); SB(); }
DI void drain() { asm volatile("s_waitcnt vmcnt(0)" ::: "memory"); }
template <int IMM> DI void glds16(unsigned voff, const void* sbase, unsigned ldsbase) {
  asm volatile("s_add_i32 m0, %2, %3\n\ts_nop 0\n\tglobal_load_lds_dwordx4 %0, %1" ::"v"(voff), "s"(sbase), "s"(ldsbase), "n"(IMM) : "memory", "scc");
}
template <int IMM> DI void glds16c(unsigned voff, const void* sbase, unsigned ldsbase) {
  asm volatile("s_add_i32 m0, %2, %3\n\ts_nop 0\n\tglobal_load_lds_dwordx4 %0, %1 sc1" ::"v"(voff), "s"(sbase), "s"(ldsbase), "n"(IMM) : "memory", "scc");
}
typedef float f32x2 __attribute__((ext_vector_type(2)));
DI f32x2 fma2(f32x2 a, f32x2 b, f32x2 c) { return __builtin_elementwise_fma(a, b, c); }
DI unsigned pk2v(f32x2 v) { bf16x2_t p = {(__bf16)v.x, (__bf16)v.y}; return __builtin_bit_cast(unsigned, p); }
DI float max3(float a, float b, float c) { float r; asm("v_max3_f32 %0, %1, %2, %3" : "=v"(r) : "v"(a), "v"(b), "v"(c)); return r; }
DI float relu(float a) { float r; asm("v_max_f32 %0, 0, %1" : "=v"(r) : "v"(a)); return r; }
DI float bf2f(unsigned b) { return __uint_as_float(b << 16); }
DI unsigned pk2(float lo, float hi) { bf16x2_t p = {(__bf16)lo, (__bf16)hi}; return __builtin_bit_cast(unsigned, p); }
DI f32x16 mfma32(u32x4 a, u32x4 b, f32x16 c) {
  return __builtin_amdgcn_mfma_f32_32x32x16_bf16(__builtin_bit_cast(s16x8, a), __builtin_bit_cast(s16x8, b), c, 0, 0, 0);
}
DI f32x4 mfma16(u32x4 a, u32x4 b, f32x4 c) {
  return __builtin_amdgcn_mfma_f32_16x16x32_bf16(__builtin_bit_cast(s16x8, a), __builtin_bit_cast(s16x8, b), c, 0, 0, 0);
}
DI u32x4 lds128(unsigned a, int off = 0) { return *(const LAS u32x4*)((const LAS char*)(size_t)a + off); }
DI u32x4 tr_frag(unsigned a, int off, int second) {
  s16x4 lo = __builtin_amdgcn_ds_read_tr16_b64_v4i16((LAS s16x4*)((LAS char*)(size_t)a + off));
  s16x4 hi = __builtin_amdgcn_ds_read_tr16_b64_v4i16((LAS s16x4*)((LAS char*)(size_t)a + off + second));
  u32x2 x = __builtin_bit_cast(u32x2, lo), y = __builtin_bit_cast(u32x2, hi);
  return u32x4{x.x, x.y, y.x, y.y};
}
DI float dot2(unsigned a, unsigned b, float c) {
  return __builtin_amdgcn_fdot2_f32_bf16(__builtin_bit_cast(bf16x2_t, a), __builtin_bit_cast(bf16x2_t, b), c, false);
}
DI float sum8(u32x4 v, float c) {
  const unsigned one = 0x3f803f80u;
  c = dot2(v.x, one, c); c = dot2(v.y, one, c); c = dot2(v.z, one, c); return dot2(v.w, one, c);
}
DI float sq8(u32x4 v, float c) { c = dot2(v.x, v.x, c); c = dot2(v.y, v.y, c); c = dot2(v.z, v.z, c); return dot2(v.w, v.w, c); }
DI float vsum8(u32x4 v) {
  const float a = __uint_as_float(v.x << 16) + __uint_as_float(v.y << 16), b = __uint_as_float(v.x & 0xffff0000u) + __uint_as_float(v.y & 0xffff0000u);
  const float c = __uint_as_float(v.z << 16) + __uint_as_float(v.w << 16), d = __uint_as_float(v.z & 0xffff0000u) + __uint_as_float(v.w & 0xffff0000u);
  return (a + b) + (c + d);
}
DI void granule_store(char* ws, unsigned off, float v, unsigned tag) {
  __hip_atomic_store((GAS unsigned long long*)(ws + off), ((unsigned long long)tag << 32) | __float_as_uint(v), __ATOMIC_RELAXED, __HIP_MEMORY_SCOPE_AGENT);
}
DI unsigned long long granule_load(char* ws, unsigned off) {
  return __hip_atomic_load((GAS unsigned long long*)(ws + off), __ATOMIC_RELAXED, __HIP_MEMORY_SCOPE_AGENT);
}
template <int CTRL> DI float dpp_add(float v) {
  return v + __uint_as_float(__builtin_amdgcn_update_dpp(0u, __float_as_uint(v), CTRL, 0xf, 0xf, false));
}
DI float half_sum(float v) {
  v = dpp_add<0xB1>(v);
  v = dpp_add<0x4E>(v);
  v = dpp_add<0x141>(v);
  v = dpp_add<0x140>(v);
  auto r = __builtin_amdgcn_permlane16_swap(__float_as_uint(v), __float_as_uint(v), false, false);
  return __uint_as_float(r[0]) + __uint_as_float(r[1]);
}
DI float xhalf_add(float v) {
  auto r = __builtin_amdgcn_permlane32_swap(__float_as_uint(v), __float_as_uint(v), false, false);
  return __uint_as_float(r[0]) + __uint_as_float(r[1]);
}
DI float xhalf_max(float v) {
  auto r = __builtin_amdgcn_permlane32_swap(__float_as_uint(v), __float_as_uint(v), false, false);
  return fmaxf(__uint_as_float(r[0]), __uint_as_float(r[1]));
}
DI u32x4 pack_feat8(unsigned ax, unsigned ay, unsigned bx, unsigned by) {
  auto rx = __builtin_amdgcn_permlane32_swap(ax, bx, false, false);
  auto ry = __builtin_amdgcn_permlane32_swap(ay, by, false, false);
  return u32x4{rx[0], ry[0], rx[1], ry[1]};
}
DI f32x2 lo2(const f32x4& v) { return f32x2{v.x, v.y}; }
DI f32x2 hi2(const f32x4& v) { return f32x2{v.z, v.w}; }
template <int I> DI f32x2 pair(const f32x16& v) { return f32x2{v[2 * I], v[2 * I + 1]}; }
DI int fresh_lane() { int l; asm volatile("v_mbcnt_lo_u32_b32 %0, -1, 0\n\tv_mbcnt_hi_u32_b32 %0, -1, %0" : "=v"(l)); return l; }
typedef __amdgpu_buffer_rsrc_t rsrc_t;
DI void st128c(rsrc_t r, unsigned off, u32x4 v) { __builtin_amdgcn_raw_buffer_store_b128(v, r, off, 0, 16); }
DI void st64c(rsrc_t r, unsigned off, u32x2 v) { __builtin_amdgcn_raw_buffer_store_b64(v, r, off, 0, 16); }
DI u32x4 ld128c(rsrc_t r, unsigned off) { return __builtin_amdgcn_raw_buffer_load_b128(r, off, 0, 16); }
DI u32x2 ld64c(rsrc_t r, unsigned off) { return __builtin_amdgcn_raw_buffer_load_b64(r, off, 0, 16); }
DI unsigned flag_load(char* ws, int idx) {
  return __hip_atomic_load((GAS unsigned*)(ws + WS_FLAG) + idx * 16, __ATOMIC_RELAXED, __HIP_MEMORY_SCOPE_AGENT);
}
DI void flag_store(char* ws, int idx, unsigned v) {
  __hip_atomic_store((GAS unsigned*)(ws + WS_FLAG) + idx * 16, v, __ATOMIC_RELAXED, __HIP_MEMORY_SCOPE_AGENT);
}
DI unsigned read_epoch(const char* ws) {
  unsigned e;
  asm volatile("s_load_dword %0, %1, 0x0\n\ts_waitcnt lgkmcnt(0)" : "=s"(e) : "s"(ws + WS_FLAG + FL_EPOCH * 64) : "memory");
  return e;
}
DI void arrive_publish(LAS char* L, int word, unsigned total, char* ws, int flag, unsigned tag, int lane) {
  if (lane == 0) {
    if (__hip_atomic_fetch_add((LAS unsigned*)(L + word), 1u, __ATOMIC_RELAXED, __HIP_MEMORY_SCOPE_WORKGROUP) == total - 1u) flag_store(ws, flag, tag);
  }
}
DI void flags_wait(char* ws, int idx, unsigned tag) {
  for (int spin = 0; spin < (1 << 22); spin++) {
    const bool ok = idx < 0 || flag_load(ws, idx) == tag;
    if (__all(ok)) break;
    __builtin_amdgcn_s_sleep(1);
  }
  __builtin_amdgcn_fence(__ATOMIC_ACQUIRE, "wavefront");
}

#define STAMP_DECL
#define STAMP(i)
#define STAMP_DUMP(k)
#define STAMPC(i)

template <class Hook0, class Hook1, class Hook2>
DI void attention_task(rsrc_t rws, unsigned lds0, int w, int lane, int qi, int cgA, int cgX, int odd, u32x4 qf0, u32x4 qf1, Hook0&& mid,
                       Hook1&& per_tile, Hook2&& pre_store) {
  const int r32 = lane & 31, h = lane >> 5;
  const int q0 = 256 * qi + 32 * w;
  const unsigned ka = lds0 + L_K + h * 8192 + (32 * w + r32) * 16;
  const unsigned kx = lds0 + L_K + 16384 + (32 * w + r32) * 16;
  f32x16 sc[9];
  const f32x16 zero = {};
  const unsigned va = r32 < 20 ? lds0 + L_V + (2 * w) * 640 + h * 320 + r32 * 16 : lds0 + AUX_C + (r32 == 20 ? 0 : 16);
  const int vstep = r32 < 20 ? 640 : 0;
  const int tmin = qi == 0 ? 8 - w : 0;
  u32x4 vf[18];
  unsigned dm[8];
  float mxa[4] = {-INFINITY, -INFINITY, -INFINITY, -INFINITY};
  {
    u32x4 k0[9], k1[9];
#pragma unroll
    for (int n = 0; n < 9; n++) { k0[n] = lds128(ka, ((n + 8) % 9) * 512); k1[n] = lds128(kx, ((n + 8) % 9) * 512); }
    sfor<0, 11>([&](auto N) {
      constexpr int n = N, t = (n + 8) % 9, u = n - 3;
      SB();
      if constexpr (n < 9) { sc[t] = mfma32(k0[n], qf0, zero); sc[t] = mfma32(k1[n], qf1, sc[t]); }
      if constexpr (n >= 2 && n < 11) { vf[2 * (n - 2)] = lds128(va + (2 * (n - 2)) * vstep); vf[2 * (n - 2) + 1] = lds128(va + (2 * (n - 2) + 1) * vstep); }
      if constexpr (u >= 0) {
        if (u < tmin) {
#pragma unroll
          for (int i = 0; i < 16; i++) sc[u][i] = -INFINITY;
        }
        if constexpr (u == 0) {
#pragma unroll
          for (int i = 0; i < 16; i += 2) {
            const int kr = (i & 3) + 8 * (i >> 2) + 4 * h;
            const bool d0 = kr <= r32, d1 = kr + 1 <= r32;
            sc[0][i] = d0 ? sc[8][i] : sc[0][i];
            sc[0][i + 1] = d1 ? sc[8][i + 1] : sc[0][i + 1];
            dm[i >> 1] = d1 ? 0xffffffffu : (d0 ? 0x0000ffffu : 0u);
          }
        }
#pragma unroll
        for (int i = 0; i < 8; i++) mxa[i & 3] = max3(mxa[i & 3], sc[u][2 * i], sc[u][2 * i + 1]);
      }
    });
  }
  SB();
  float mx = max3(mxa[0], mxa[1], max3(mxa[2], mxa[3], mxa[3]));
  {
    auto r = __builtin_amdgcn_permlane32_swap(__float_as_uint(mx), __float_as_uint(mx), false, false);
    mx = max3(__uint_as_float(r[0]), __uint_as_float(r[1]), __uint_as_float(r[1]));
  }
  u32x4 nm;
  {
    const unsigned m0 = __float_as_uint(mx) & 0xffff0000u;
    const float r1 = mx - __uint_as_float(m0);
    const unsigned m1 = __float_as_uint(r1) & 0xffff0000u;
    const unsigned m2 = __float_as_uint(r1 - __uint_as_float(m1)) & 0xffff0000u;
    const unsigned a = ((m0 >> 16) | m1) ^ 0x80008000u, c = (m2 >> 16) ^ 0x8000u;
    nm = u32x4{h ? 0u : a, h ? 0u : c, 0u, 0u};
  }
  const u32x4 ones3 = {0x3f803f80u, 0x00003f80u, 0u, 0u};
  mid();
  SB();
  sc[1] = mfma32(ones3, nm, sc[1]);
  sc[2] = mfma32(ones3, nm, sc[2]);
  f32x16 o = {};
  sfor<1, 9>([&](auto T) {
    constexpr int tt = T, t = tt & 7;
    SB();
    if constexpr (tt + 2 < 9) sc[(tt + 2) & 7] = mfma32(ones3, nm, sc[(tt + 2) & 7]);
    per_tile(T);
    unsigned pk[8];
#pragma unroll
    for (int i = 0; i < 8; i++) pk[i] = pk2(__builtin_amdgcn_exp2f(sc[t][2 * i]), __builtin_amdgcn_exp2f(sc[t][2 * i + 1]));
    if constexpr (t != 0) {
      o = mfma32(vf[2 * t], u32x4{pk[0], pk[1], pk[2], pk[3]}, o);
      o = mfma32(vf[2 * t + 1], u32x4{pk[4], pk[5], pk[6], pk[7]}, o);
    } else {
      o = mfma32(vf[16], u32x4{pk[0] & dm[0], pk[1] & dm[1], pk[2] & dm[2], pk[3] & dm[3]}, o);
      o = mfma32(vf[17], u32x4{pk[4] & dm[4], pk[5] & dm[5], pk[6] & dm[6], pk[7] & dm[7]}, o);
      o = mfma32(vf[0], u32x4{pk[0] & ~dm[0], pk[1] & ~dm[1], pk[2] & ~dm[2], pk[3] & ~dm[3]}, o);
      o = mfma32(vf[1], u32x4{pk[4] & ~dm[4], pk[5] & ~dm[5], pk[6] & ~dm[6], pk[7] & ~dm[7]}, o);
    }
  });
  SB();
  pre_store();
  SB();
  auto lr = __builtin_amdgcn_permlane32_swap(__float_as_uint(o[8]), __float_as_uint(o[8]), false, false);
  const float inv = __builtin_amdgcn_rcpf(__uint_as_float(lr[1]));
  const unsigned ob = WS_ACT + 192u * S_ * 16;
  st128c(rws, ob + ((cgA + h) * S_ + q0 + r32) * 16,
         u32x4{pk2(o[0] * inv, o[1] * inv), pk2(o[2] * inv, o[3] * inv), pk2(o[4] * inv, o[5] * inv), pk2(o[6] * inv, o[7] * inv)});
  if (h == 0) st64c(rws, ob + (cgX * S_ + q0 + r32) * 16 + odd * 8, u32x2{pk2(o[8] * inv, o[9] * inv), pk2(o[10] * inv, o[11] * inv)});
}
DI void load_q(rsrc_t rws, int w, int lane, int qi, int cgA, int cgX, int odd, u32x4& qf0, u32x4& qf1) {
  const int r32 = lane & 31, h = lane >> 5, q0 = 256 * qi + 32 * w;
  qf0 = ld128c(rws, WS_QK + ((cgA + h) * S_ + q0 + r32) * 16);
  const u32x2 xq = ld64c(rws, WS_QK + (cgX * S_ + q0 + r32) * 16 + odd * 8);
  const unsigned x0 = h ? 0u : xq.x, x1 = h ? 0u : xq.y;
  qf1 = odd ? u32x4{0u, 0u, x0, x1} : u32x4{x0, x1, 0u, 0u};
}

__global__ __launch_bounds__(512, 2) void k_fused(const u16* __restrict__ x, const u16* __restrict__ wqkv, const u16* __restrict__ wo,
                                                  const u16* __restrict__ w1, const u16* __restrict__ b1, const u16* __restrict__ w2,
                                                  const u16* __restrict__ b2, char* __restrict__ ws, u16* __restrict__ out) {
  extern __shared__ __attribute__((aligned(16))) char smem[];
  LAS char* L = (LAS char*)smem;
  const unsigned lds0 = (unsigned)(size_t)L;
  const int tid = threadIdx.x, lane = tid & 63, w = __builtin_amdgcn_readfirstlane(tid >> 6);
  const int b = blockIdx.x, xcd = b & 7, bi = b >> 3;
  const int s = w & 3;
  const rsrc_t rws = __builtin_amdgcn_make_buffer_rsrc(ws, 0, WS_BYTES, 0x00020000);
  const int rb = (xcd & 1) * 8 + (bi >> 2), j = (xcd >> 1) * 4 + (bi & 3);
  const int row0 = rb * 128;
  const int kc = (5 * rb) >> 1, rot = kc >> 2, kc0 = kc & 3, kc1 = kc0 + 2 + (rb & 1);
  const int hd = xcd * 4 + (bi & 3), qi = bi >> 2;
  const int odd = hd & 1, cgA = (hd >> 1) * 5 + (odd ? 3 : 0), cgX = (hd >> 1) * 5 + 2;
  const int rb3 = (xcd >> 1) * 8 + (bi >> 2), cgp = (xcd & 1) * 4 + (bi & 3);
  const int row3 = rb3 * 64, n0 = cgp * 80;
  STAMP_DECL;
  STAMP(0); STAMPC(38);
  const int r32 = lane & 31, h = lane >> 5;
  const int blk = (lane >> 4) & 1, q4 = (lane & 15) >> 2, p4 = lane & 3;
  const unsigned a0 = lds0 + L_A + (32 * s + r32) * 128 + ((h ^ ((r32 >> 1) & 7)) * 16);
  auto rdA = [&](int p, int t) -> u32x4 {
    if (p % 5 < 4) return lds128(a0 ^ (t * 32), (p % 5) * 16384);
    unsigned ad;
    asm volatile("v_add_u32 %0, 0x10000, %1" : "=v"(ad) : "v"(a0 ^ (t * 32)));
    return lds128(ad);
  };
  const unsigned bb = lds0 + L_B + (8 * h + q4) * 256 + (4 * q4 + 2 * blk + (p4 >> 1)) * 16 + 8 * (p4 & 1);
  const unsigned fa = lds0 + L_B + (8 * h + q4) * 192 + (2 * blk + (p4 >> 1)) * 16 + 8 * (p4 & 1);
  constexpr int FP[6] = {36864, 49152, 61440, 0, 12288, 24576};
  auto rdB = [&](int p, int t, int u) { return tr_frag(bb ^ (64 * u), (p & 3) * 16384 + t * 4096, 1024); };
  auto rdF = [&](int f, int t, int u) { return tr_frag(fa, FP[f % 6] + t * 3072 + u * 64, 768); };

  if (w < 4) {
    const unsigned tag = read_epoch(ws) + 1u;
    u32x4 af[20], at[4];
    auto getA = [&](auto P, auto T) -> u32x4& { if constexpr (P < 5) return af[4 * P + T]; else return at[T]; };
    unsigned long long stg = 0;
    auto stoff = [&]() { const int lc = fresh_lane(); return WS_STAT + (row0 + 32 * s + (lc & 31)) * 16 + (lc >> 5) * 8; };
    f32x16 acc[4] = {};
    auto body = [&](auto MODE) {
      u32x4 bs[3][4];
      auto mm = [&](auto N, int u) {
        constexpr int n = N;
        if constexpr (MODE != 2) {
          const u32x4 a = getA(IC<n / 4>{}, IC<n % 4>{});
          if constexpr (MODE == 0) acc[u] = mfma32(bs[n % 3][u], a, acc[u]); else acc[u] = mfma32(a, bs[n % 3][u], acc[u]);
        }
      };
      auto rB = [&](int g, int t, int u) { if constexpr (MODE != 2) bs[(4 * g + t) % 3][u] = rdB(g, t, u); };
      sfor<0, 10>([&](auto G) {
        constexpr int g = G;
        lds_bar();
        if constexpr (g == 0 || g == 5) { STAMP(1 + g); }
#pragma unroll
        for (int u = 0; u < 4; u++) rB(g, 0, u);
        sfor<1, 4>([&](auto T) {
          constexpr int t = T;
          SB();
#pragma unroll
          for (int u = 0; u < 4; u++) {
            if constexpr (4 * g + t - 3 >= 0) mm(IC<(4 * g + t - 3 >= 0 ? 4 * g + t - 3 : 0)>{}, u);
            if (t == 1 && u == 3) getA(G, IC<0>{}) = rdA(g, 0);
            if (t == 1 && u == 3) getA(G, IC<1>{}) = rdA(g, 1);
            if (t == 2 && u == 3) getA(G, IC<2>{}) = rdA(g, 2);
            if (t == 3 && u == 0) getA(G, IC<3>{}) = rdA(g, 3);
            rB(g, t, u);
            SB();
          }
        });
#pragma unroll
        for (int u = 0; u < 4; u++) mm(IC<4 * g + 1>{}, u);
        if constexpr (g == 8) stg = granule_load(ws, stoff());
      });
#pragma unroll
      for (int u = 0; u < 4; u++) mm(IC<38>{}, u);
#pragma unroll
      for (int u = 0; u < 4; u++) mm(IC<39>{}, u);
    };
    if (j < 10) body(IC<0>{}); else if (j < 15) body(IC<1>{}); else body(IC<2>{});
    STAMP(11);
    float mur, rs;
    {
      for (int spin = 0; spin < (1 << 22) && !__all((unsigned)(stg >> 32) == tag); spin++) stg = granule_load(ws, stoff());
      auto r = __builtin_amdgcn_permlane32_swap((unsigned)stg, (unsigned)stg, false, false);
      mur = __uint_as_float(r[0]); rs = __uint_as_float(r[1]);
    }
    const int l2 = fresh_lane(), r2 = l2 & 31, h2 = l2 >> 5;
    if (j >= 10 && h2 == 0) {
      ((LAS float*)(L + AUX_ST))[32 * s + r2] = mur;
      ((LAS float*)(L + AUX_ST))[128 + 32 * s + r2] = rs;
    }
    lds_bar();
    if (j < 10) {
      const float sc = (j < 5) ? QSCALE : 1.0f;
      const f32x2 a2 = {rs * sc, rs * sc}, b2q = {-mur * sc, -mur * sc};
      const unsigned dst = WS_QK + ((16 * j + h2) * S_ + row0 + 32 * s + r2) * 16;
      const unsigned csa = lds0 + AUX_CS + 16 * h2;
      sfor<0, 2>([&](auto UU) {
        f32x4 cs[8];
#pragma unroll
        for (int i = 0; i < 8; i++) cs[i] = __builtin_bit_cast(f32x4, lds128(csa, (64 * UU + 8 * i) * 4));
        SB();
        sfor<0, 4>([&](auto C) {
          constexpr int u = 2 * UU + C / 2, k = C % 2, ci = 2 * C;
          const unsigned p0 = pk2v(fma2(pair<4 * k>(acc[u]), a2, lo2(cs[ci]) * b2q)), p1 = pk2v(fma2(pair<4 * k + 1>(acc[u]), a2, hi2(cs[ci]) * b2q));
          const unsigned p2 = pk2v(fma2(pair<4 * k + 2>(acc[u]), a2, lo2(cs[ci + 1]) * b2q)), p3 = pk2v(fma2(pair<4 * k + 3>(acc[u]), a2, hi2(cs[ci + 1]) * b2q));
          st128c(rws, dst + (4 * u + 2 * k) * (S_ * 16), pack_feat8(p0, p1, p2, p3));
        });
      });
    } else if (j < 15) {
      f32x4 murv[4], rsv[4];
      float csv[4];
#pragma unroll
      for (int g = 0; g < 4; g++) {
        murv[g] = *(const LAS f32x4*)(L + AUX_ST + (32 * s + 8 * g + 4 * h2) * 4);
        rsv[g] = *(const LAS f32x4*)(L + AUX_ST + 512 + (32 * s + 8 * g + 4 * h2) * 4);
        csv[g] = -*(const LAS float*)(L + AUX_CS + (32 * g + r2) * 4);
      }
      sfor<0, 4>([&](auto U) {
        constexpr int u = U;
        const f32x2 cs2 = {csv[u], csv[u]};
        const int colv = 128 * (j - 10) + 32 * u + r2;
        const int head = colv / 20, d = colv - head * 20;
        const int dd = (head & 1) ? (d < 4 ? d + 16 : d - 4) : d;
        const int slot = dd < 16 ? ((dd & 3) + 8 * ((dd >> 2) & 1) + 4 * (dd >> 3)) : dd;
        const unsigned dst = WS_VT + ((head * 128 + (row0 + 32 * s) / 16) * 2 + h2) * 320 + slot * 16;
        sfor<0, 2>([&](auto SP) {
          constexpr int sp = SP;
          const unsigned p0 = pk2v(fma2(pair<4 * sp>(acc[u]), lo2(rsv[2 * sp]), cs2 * lo2(murv[2 * sp])));
          const unsigned p1 = pk2v(fma2(pair<4 * sp + 1>(acc[u]), hi2(rsv[2 * sp]), cs2 * hi2(murv[2 * sp])));
          const unsigned p2 = pk2v(fma2(pair<4 * sp + 2>(acc[u]), lo2(rsv[2 * sp + 1]), cs2 * lo2(murv[2 * sp + 1])));
          const unsigned p3 = pk2v(fma2(pair<4 * sp + 3>(acc[u]), hi2(rsv[2 * sp + 1]), cs2 * hi2(murv[2 * sp + 1])));
          st128c(rws, dst + sp * 640, u32x4{p0, p1, p2, p3});
        });
      });
    }
    STAMP(12);
    f32x16 facc[3] = {};
    {
      u32x4 bs[3][3];
      auto pos = [](int f) { return (f + 5) % 10; };
      auto mm = [&](auto N, int u) {
        constexpr int n = N;
        facc[u] = mfma32(bs[n % 3][u], getA(IC<(n / 4 + 5) % 10>{}, IC<n % 4>{}), facc[u]);
      };
      sfor<0, 10>([&](auto Fs) {
        constexpr int f = Fs;
        if constexpr (f == 2 || f == 4 || f == 6) lds_bar();
        if constexpr (f == 3) { SB(); drain(); arrive_publish(L, AUX_W + 12, 4, ws, FL_1 + rb * 16 + j, tag, fresh_lane()); SB(); }
        if constexpr (f == 5) { STAMP(13 + f); }
#pragma unroll
        for (int u = 0; u < 3; u++) bs[(4 * f) % 3][u] = rdF(f, 0, u);
        sfor<1, 4>([&](auto T) {
          constexpr int t = T;
          SB();
#pragma unroll
          for (int u = 0; u < 3; u++) {
            if constexpr (4 * f + t - 3 >= 0) mm(IC<(4 * f + t - 3 >= 0 ? 4 * f + t - 3 : 0)>{}, u);
            if constexpr (f < 5) {
              if (t == 1 && u == 2) at[0] = rdA(pos(f), 0);
              if (t == 1 && u == 2) at[1] = rdA(pos(f), 1);
              if (t == 2 && u == 2) at[2] = rdA(pos(f), 2);
              if (t == 3 && u == 0) at[3] = rdA(pos(f), 3);
            }
            bs[(4 * f + t) % 3][u] = rdF(f, t, u);
            SB();
          }
        });
#pragma unroll
        for (int u = 0; u < 3; u++) mm(IC<4 * f + 1>{}, u);
      });
#pragma unroll
      for (int u = 0; u < 3; u++) mm(IC<38>{}, u);
#pragma unroll
      for (int u = 0; u < 3; u++) mm(IC<39>{}, u);
    }
    STAMP(23);
    {
      const int l3 = fresh_lane(), r3 = l3 & 31, h3 = l3 >> 5;
      while (true) {
        const u32x4 rdy = *(const volatile LAS u32x4*)(L + AUX_W);
        if (__builtin_amdgcn_readfirstlane(rdy.x & rdy.y & rdy.z) == 1u) break;
      }
      asm volatile("" ::: "memory");
      const unsigned dst = WS_ACT + ((12 * j + h3) * S_ + row0 + 32 * s + r3) * 16;
      const unsigned ca = lds0 + AUX_CF + 16 * h3;
      const f32x2 rs2 = {rs, rs}, nm2 = {-mur, -mur};
      auto act = [&](f32x2 a, f32x2 cs, f32x2 bv) {
        f32x2 t = fma2(a, rs2, fma2(cs, nm2, bv));
        t = f32x2{relu(t.x), relu(t.y)};
        return pk2v(t * t);
      };
      sfor<0, 3>([&](auto U) {
        constexpr int u = U;
        f32x4 cs[4], bv[4];
#pragma unroll
        for (int i = 0; i < 4; i++) {
          cs[i] = __builtin_bit_cast(f32x4, lds128(ca, (32 * u + 8 * i) * 4));
          bv[i] = __builtin_bit_cast(f32x4, lds128(ca, AUX_BF - AUX_CF + (32 * u + 8 * i) * 4));
        }
        SB();
        sfor<0, 2>([&](auto K) {
          constexpr int k = K;
          const unsigned p0 = act(pair<4 * k>(facc[u]), lo2(cs[2 * k]), lo2(bv[2 * k])), p1 = act(pair<4 * k + 1>(facc[u]), hi2(cs[2 * k]), hi2(bv[2 * k]));
          const unsigned p2 = act(pair<4 * k + 2>(facc[u]), lo2(cs[2 * k + 1]), lo2(bv[2 * k + 1]));
          const unsigned p3 = act(pair<4 * k + 3>(facc[u]), hi2(cs[2 * k + 1]), hi2(bv[2 * k + 1]));
          st128c(rws, dst + (4 * u + 2 * k) * (S_ * 16), pack_feat8(p0, p1, p2, p3));
        });
      });
    }
    STAMP(24);
    while (__builtin_amdgcn_readfirstlane(*(const volatile LAS unsigned*)(L + AUX_W + 16 + 4 * s)) != 1u) __builtin_amdgcn_s_sleep(1);
    asm volatile("" ::: "memory");
    const int lanem = fresh_lane();
    u32x4 qf0, qf1;
    load_q(rws, w, lanem, qi, cgA, cgX, odd, qf0, qf1);
    wait_bar<0>();
    STAMP(25);
    const int mh = s & 1, kh = s >> 1;
    const int rot3 = ((bi >> 2) + 3 * (bi & 3)) % 12, rot4 = ((bi >> 2) + (bi & 3)) % 5;
    auto kq = [&](int st) { int v; if (st < 12) { v = st + rot3; if (v >= 12) v -= 12; } else { v = st + rot4; if (v >= 17) v -= 5; } return v; };
    u32x4 ar[6][4];
    unsigned avo;
    auto ldA = [&](auto ST) {
      constexpr int st = ST;
      const unsigned so = kq(st) * (16 * S_ * 16);
#pragma unroll
      for (int i = 0; i < 4; i++) ar[st % 6][i] = __builtin_amdgcn_raw_buffer_load_b128(rws, (avo ^ ((i & 1) * 256)) + (i >> 1) * (8 * S_ * 16), so, 16);
    };
    unsigned f2 = tag;
    attention_task(rws, lds0, w, lanem, qi, cgA, cgX, odd, qf0, qf1,
                   [&]() {}, [&](auto T) { if constexpr (T == 5) { if (lanem < 16) f2 = flag_load(ws, FL_2 + (rb3 >> 1) * 16 + lanem); } },
                   [&]() {
                     if (!__all(f2 == tag)) flags_wait(ws, lanem < 16 ? FL_2 + (rb3 >> 1) * 16 + lanem : -1, tag);
                     avo = WS_ACT + ((4 * kh + (lanem >> 4)) * S_ + row3 + 32 * mh + 16 * kh + (lanem & 15)) * 16;
                     ldA(IC<0>{}); ldA(IC<1>{}); ldA(IC<2>{}); ldA(IC<3>{});
                   });
    STAMP(26);
    const int tok = row3 + 32 * mh + 16 * kh + (lanem & 15);
    u32x2 xv[5], bv[5];
    f32x4 accf[2][5] = {}, accs[2][5] = {};
    const unsigned wr = lds0 + kh * 5120 + lanem * 16;
    u32x4 fw[2][5];
    unsigned f3 = tag;
    auto mm1 = [&](auto ST, auto KK, auto X) {
      constexpr int st = ST, kk = KK, i = X / 2, r = X % 2;
      if constexpr (st < 12) accf[r][i] = mfma16(fw[kk][i], ar[st % 6][2 * kk + r], accf[r][i]);
      else accs[r][i] = mfma16(fw[kk][i], ar[st % 6][2 * kk + r], accs[r][i]);
    };
    auto ldA1 = [&](auto ST, auto I) {
      constexpr int st = ST, i = I;
      ar[st % 6][i] = __builtin_amdgcn_raw_buffer_load_b128(rws, (avo ^ ((i & 1) * 256)) + (i >> 1) * (8 * S_ * 16), kq(st) * (16 * S_ * 16), 16);
    };
    sfor<0, 17>([&](auto ST) {
      constexpr int st = ST;
      if constexpr (st == 4) wait_bar<16>(); else if constexpr (st >= 2 && st % 2 == 0) lds_bar();
      if constexpr (st == 2) { STAMP(27); }
      if constexpr (st == 12) { STAMP(28); }
      if constexpr (st == 9) { if (!__all(f3 == tag)) flags_wait(ws, lanem < 32 ? FL_3 + lanem * 8 + (rb3 >> 2) : -1, tag); }
      sfor<0, 10>([&](auto X) {
        constexpr int xx = X;
        if constexpr (xx < 5) fw[0][xx] = lds128(wr, W3[st % 7] + xx * 1024);
        if constexpr (st > 0) mm1(IC<(st > 0 ? st - 1 : 0)>{}, IC<1>{}, X);
        SB();
      });
      sfor<0, 10>([&](auto X) {
        constexpr int xx = X;
        if constexpr (xx < 5) fw[1][xx] = lds128(wr, W3[st % 7] + 10240 + xx * 1024);
        mm1(ST, IC<0>{}, X);
        if constexpr (xx >= 6 && st <= 7) ldA1(IC<(st <= 7 ? st + 4 : 0)>{}, IC<(xx >= 6 ? xx - 6 : 0)>{});
        if constexpr (xx >= 6 && st >= 9 && st <= 11) ldA1(IC<(st >= 9 && st <= 11 ? 2 * st - 6 : 0)>{}, IC<(xx >= 6 ? xx - 6 : 0)>{});
        if constexpr (xx >= 2 && xx < 6 && (st == 9 || st == 10)) ldA1(IC<(st == 9 || st == 10 ? 2 * st - 5 : 0)>{}, IC<(xx >= 2 ? (xx - 2) & 3 : 0)>{});
        SB();
      });
      if constexpr (st == 7) { if (lanem < 32) f3 = flag_load(ws, FL_3 + lanem * 8 + (rb3 >> 2)); }
      if constexpr (st == 12) {
#pragma unroll
        for (int i = 0; i < 5; i++) {
          xv[i] = *(const u32x2*)(x + (size_t)tok * D_ + n0 + 16 * i + 4 * (lanem >> 4));
          bv[i] = *(const u32x2*)(b2 + n0 + 16 * i + 4 * (lanem >> 4));
        }
      }
    });
    sfor<0, 10>([&](auto X) { mm1(IC<16>{}, IC<1>{}, X); });
    STAMP(29);
    f32x4 gf[5], gs[5];
    {
      LAS f32x4* ex = (LAS f32x4*)(L + W3[5]) + s * 640 + lanem;
#pragma unroll
      for (int i = 0; i < 5; i++) { ex[i * 64] = accf[1][i]; ex[320 + i * 64] = accs[1][i]; }
      lds_bar();
      const LAS f32x4* ey = (const LAS f32x4*)(L + W3[5]) + (s ^ 2) * 640 + lanem;
#pragma unroll
      for (int i = 0; i < 5; i++) { gf[i] = accf[0][i] + ey[i * 64]; gs[i] = accs[0][i] + ey[320 + i * 64]; }
    }
#pragma unroll
    for (int i = 0; i < 5; i++) {
      const float xf[4] = {bf2f(xv[i].x & 0xffffu), bf2f(xv[i].x >> 16), bf2f(xv[i].y & 0xffffu), bf2f(xv[i].y >> 16)};
      const float bf[4] = {bf2f(bv[i].x & 0xffffu), bf2f(bv[i].x >> 16), bf2f(bv[i].y & 0xffffu), bf2f(bv[i].y >> 16)};
      float r[4];
#pragma unroll
      for (int e = 0; e < 4; e++) {
        const float g = gf[i][e] + bf[e];
        const float sg = g * __builtin_amdgcn_rcpf(1.0f + __builtin_amdgcn_exp2f(-1.4426950408889634f * g));
        r[e] = __builtin_fmaf(gs[i][e], sg, xf[e]);
      }
      *(u32x2*)(out + (size_t)tok * D_ + n0 + 16 * i + 4 * (lanem >> 4)) = u32x2{pk2(r[0], r[1]), pk2(r[2], r[3])};
    }
    STAMP(30); STAMPC(39);
    if (b == 0 && w == 0) {
#pragma unroll
      for (int i = 0; i < 4; i++) flags_wait(ws, FL_2 + 64 * i + lanem, tag);
      if (lanem == 0) flag_store(ws, FL_EPOCH, tag);
    }
    if (w == 0) { STAMP_DUMP(0); }
  } else {
    const char* xbase = (const char*)(x + (size_t)row0 * D_);
    const char* fbase = (const char*)(w1 + 96 * j);
    const char* qbase = (const char*)(wqkv + 128 * (j < 15 ? j : 14));
    unsigned voffA[4], voffB[4], voffF[3];
#pragma unroll
    for (int e = 0; e < 4; e++) {
      voffA[e] = ((32 * s + 8 * e + (lane >> 3)) * D_ + (((lane & 7) ^ (4 * (e & 1) + (lane >> 4))) * 8)) * 2;
      voffB[e] = (16 * s + 4 * e + (lane >> 4)) * (NQKV * 2) + ((lane & 15) ^ ((lane >> 4) << 2)) * 16;
    }
    const unsigned voffT = (8 * s + (lane >> 4)) * (D_ * 2) + ((lane & 15) ^ ((lane >> 4) << 2)) * 16;
#pragma unroll
    for (int e = 0; e < 3; e++) { const int Lb = e * 1024 + lane * 16; voffF[e] = (16 * s + Lb / 192) * (F_ * 2) + Lb % 192; }
    const unsigned l4 = lds0 + s * 4096, l3k = lds0 + s * 3072;
    auto kst = [&](int p) { const int v = p + rot; return v >= 10 ? v - 10 : v; };
    auto dmaP = [&](auto P, auto E, const char* xs, const char* qs) {
      constexpr int p = P, e = E;
      if constexpr (e < 4) glds16<L_A + (p % 5) * 16384 + e * 1024>(voffA[e], xs, l4);
      else glds16<L_B + (p & 3) * 16384 + (e - 4) * 1024>(voffB[e - 4], qs, l4);
    };
    auto dmaF = [&](auto Fs) {
      constexpr int f = Fs;
      const char* src = fbase + (size_t)(kst((f + 5) % 10) * (64 * F_ * 2));
      glds16<L_B + FP[f % 6]>(voffF[0], src, l3k); glds16<L_B + FP[f % 6] + 1024>(voffF[1], src, l3k); glds16<L_B + FP[f % 6] + 2048>(voffF[2], src, l3k);
    };
    auto dmaT = [&](int hc) {
      const unsigned kb = (unsigned)hc / 5u, cb = hc - kb * 5;
      const size_t base = kb < 48 ? (size_t)w2 : (size_t)wo - (size_t)48 * 32 * D_ * 2;
      const char* src = (const char*)(base + (size_t)(kb * (32 * D_ * 2) + cb * 256));
      glds16<L_T>(voffT, src, lds0 + s * 2048);
      glds16<L_T + 1024>(voffT, src + 4 * D_ * 2, lds0 + s * 2048);
    };
    u32x2 sx[5];
    {
      const unsigned so = ((8 * j + 2 * s + h) * D_ + 4 * r32) * 2;
#pragma unroll
      for (int i = 0; i < 5; i++) asm volatile("global_load_dwordx2 %0, %1, %2 offset:%3" : "=v"(sx[i]) : "v"(so), "s"(xbase), "n"(256 * i) : "memory");
    }
    if (w == 4) { if (lane < 12) glds16<AUX_B1>(lane * 16, (const char*)(b1 + 96 * j), lds0); }
    sfor<0, 3>([&](auto P) {
      const char* xs = xbase + kst(P) * 128;
      const char* qs = qbase + (size_t)(kst(P) * (64 * NQKV * 2));
      sfor<0, 8>([&](auto E) { dmaP(P, E, xs, qs); });
    });
    if (w == 5 && lane < 16) ((LAS unsigned*)(L + AUX_C))[lane] = lane < 4 ? 0x3f803f80u : 0u;
    const unsigned tag = read_epoch(ws) + 1u;
    {
      asm volatile("s_waitcnt vmcnt(24)" : "+v"(sx[0]), "+v"(sx[1]), "+v"(sx[2]), "+v"(sx[3]), "+v"(sx[4])::"memory");
      float v[20];
#pragma unroll
      for (int i = 0; i < 5; i++) {
        v[4 * i] = bf2f(sx[i].x & 0xffffu); v[4 * i + 1] = bf2f(sx[i].x >> 16); v[4 * i + 2] = bf2f(sx[i].y & 0xffffu); v[4 * i + 3] = bf2f(sx[i].y >> 16);
      }
      float sm = ((v[0] + v[1]) + (v[2] + v[3])) + ((v[4] + v[5]) + (v[6] + v[7])) + ((v[8] + v[9]) + (v[10] + v[11]));
      sm += ((v[12] + v[13]) + (v[14] + v[15])) + ((v[16] + v[17]) + (v[18] + v[19]));
      sm = half_sum(sm);
      const float mu = sm * (1.0f / D_);
      float qq[4] = {0.f, 0.f, 0.f, 0.f};
#pragma unroll
      for (int i = 0; i < 20; i++) qq[i & 3] = __builtin_fmaf(v[i] - mu, v[i] - mu, qq[i & 3]);
      float sq = (qq[0] + qq[1]) + (qq[2] + qq[3]);
      sq = half_sum(sq);
      const float r = __builtin_amdgcn_rsqf(sq * (1.0f / D_) + 1e-5f);
      if (r32 < 2) granule_store(ws, WS_STAT + (row0 + 8 * j + 2 * s + h) * 16 + r32 * 8, r32 ? r : mu * r, tag);
    }
    auto conv = [&](int hc) {
      const int kb = hc / 5, cb = hc % 5, lc = fresh_lane();
      const unsigned dst = WS_WT + ((kb * 4 + 2 * (s >> 1) + (lc >> 5)) * D_ + cb * 128 + 64 * (s & 1) + (lc & 31)) * 16;
      const unsigned src = (bb ^ (128 * (s & 1))) + (s >> 1) * 4096;
      st128c(rws, dst, tr_frag(src, L_T, 1024));
      st128c(rws, dst + 512, tr_frag(src ^ 64, L_T, 1024));
    };
    const unsigned cspub = WS_CSP + ((j * 16 + rb) * 224 + 32 * s + r32) * 8;
    const char* csgat = ws + WS_CSP + (size_t)(j * 16) * 224 * 8;
    const unsigned voffG = (8 * h * 224 + 32 * s + r32) * 8;
    u32x2 gr[8];
    auto gather_issue = [&](int off) {
#pragma unroll
      for (int i = 0; i < 8; i++)
        asm volatile("global_load_dwordx2 %0, %1, %2 sc1" : "=v"(gr[i]) : "v"(voffG), "s"(csgat + off + i * 224 * 8) : "memory");
    };
    auto gather_sum = [&](int off) -> float {
      for (int spin = 0; spin < (1 << 22); spin++) {
#pragma unroll
        for (int i = 0; i < 8; i++) asm volatile("" : "+v"(gr[i]));
        bool ok = true;
#pragma unroll
        for (int i = 0; i < 8; i++) ok &= gr[i].y == tag;
        if (__all(ok)) break;
        gather_issue(off);
        drain();
      }
      float a = 0.f;
#pragma unroll
      for (int i = 0; i < 8; i++) a += __uint_as_float(gr[i].x);
      return xhalf_add(a);
    };
    float cpart = 0.f;
    sfor<0, 10>([&](auto G) {
      constexpr int g = G;
      constexpr int NW[10] = {17, 19, 18, 17, 19, 20, 18, 24, 21, 11};
      wait_bar<NW[g]>();
      if constexpr (g == 1) { if (w == 7 && lane < 48) {
        const unsigned v = *(const LAS unsigned*)(L + AUX_B1 + lane * 4);
        *(LAS u32x2*)(L + AUX_BF + lane * 8) = u32x2{v << 16, v & 0xffff0000u};
      } }
      if constexpr (g == 0) dmaT(b);
      if constexpr (g == 4) dmaT(b < 84 ? 256 + b : b);
      if constexpr (g == 2) { cpart = xhalf_add(cpart); if (h == 0) granule_store(ws, cspub, cpart, tag); else asm volatile("" ::: "memory"); }
      if constexpr (g == 3) conv(b);
      if constexpr (g == 7) conv(b < 84 ? 256 + b : b);
      if constexpr (g == 6) gather_issue(0);
      const char* xs = xbase + kst(g < 7 ? g + 3 : 0) * 128;
      const char* qs = qbase + (size_t)(kst(g < 7 ? g + 3 : 0) * (64 * NQKV * 2));
      if constexpr (g < 7) sfor<0, 8>([&](auto E) { dmaP(IC<g + 3>{}, E, xs, qs); });
      else if constexpr (g == 7) dmaF(IC<0>{});
      else if constexpr (g == 8) { dmaF(IC<1>{}); dmaF(IC<2>{}); }
      else dmaF(IC<3>{});
      if constexpr (g < 2) {
#pragma unroll
        for (int t = 0; t < 4; t++)
          if (4 * g + t >= kc0 && 4 * g + t < kc1) cpart += vsum8(tr_frag(bb ^ (64 * s), (g & 3) * 16384 + t * 4096, 1024));
      }
      if constexpr (g == 9) {
        const float cs = gather_sum(0);
        if (h == 0) ((LAS float*)(L + AUX_CS))[32 * s + r32] = cs;
      }
    });
    cpart = 0.f;
    const int uf = s < 3 ? s : 2;
    unsigned voffW[5];
#pragma unroll
    for (int i = 0; i < 5; i++) voffW[i] = ((lane >> 4) * D_ + (lane & 15) + 16 * i) * 16;
    const char* wbase = ws + WS_WT + (size_t)n0 * 16 + (size_t)s * 4 * D_ * 16;
    const int rot3 = ((bi >> 2) + 3 * (bi & 3)) % 12, rot4 = ((bi >> 2) + (bi & 3)) % 5;
    auto kq = [&](int st) { int v; if (st < 12) { v = st + rot3; if (v >= 12) v -= 12; } else { v = st + rot4; if (v >= 17) v -= 5; } return v; };
    const unsigned l5 = lds0 + s * 5120;
    auto issueW = [&](auto ST) {
      constexpr int st = ST;
      const char* ww = wbase + (size_t)(kq(st) * (16 * D_ * 16));
      sfor<0, 5>([&](auto I) { glds16c<W3[st % 7] + I * 1024>(voffW[I], ww, l5); });
    };
    int idxq;
    {
      const int ty = lane >> 3, rbi = (lane >> 1) & 3, hi = lane & 1;
      const int c0 = ty == 2 ? 20 * hd : 8 * (odd ? cgX : cgA), c1 = ty == 2 ? 20 * hd + 19 : 8 * (odd ? cgA + 1 : cgX) + 7;
      const int tile = 5 * ty + (hi ? c1 : c0) / 128, rr = 2 * qi - 2 + rbi;
      idxq = (lane < 24 && rr >= 0 && (ty > 0 || rbi >= 2)) ? FL_1 + rr * 16 + tile : FL_1 + rb * 16 + j;
    }
    unsigned pv[5];
    auto poll_issue = [&](unsigned& dst, int idx) {
      asm volatile("global_load_dword %0, %1, %2 sc1" : "=v"(dst) : "v"(idx * 64), "s"(ws + WS_FLAG) : "memory");
    };
    wait_bar<6>();
    if (w == 4 && lane == 0) flag_store(ws, FL_T + b, tag);
    dmaF(IC<4>{}); dmaF(IC<5>{});
#pragma unroll
    for (int t = 0; t < 8; t++)
      if (t >= kc0 && t < kc1) cpart += vsum8(tr_frag(fa + uf * 64, FP[t >> 2] + (t & 3) * 3072, 768));
    cpart = xhalf_add(cpart);
    if (h == 0 && s < 3) granule_store(ws, cspub + 128 * 8, cpart, tag);
    wait_bar<6>();
    dmaF(IC<6>{}); dmaF(IC<7>{});
#pragma unroll
    for (int i = 0; i < 4; i++) poll_issue(pv[i], FL_T + 64 * i + lane);
    wait_bar<10>();
    dmaF(IC<8>{}); dmaF(IC<9>{});
    wait_bar<0>();
    gather_issue(128 * 8); poll_issue(pv[0 + 4], idxq);
#pragma unroll
    for (int i = 0; i < 4; i++) asm volatile("" : "+v"(pv[i]));
    if (!__all(pv[0] == tag && pv[1] == tag && pv[2] == tag && pv[3] == tag)) {
#pragma unroll
      for (int i = 0; i < 4; i++) flags_wait(ws, FL_T + 64 * i + lane, tag);
    }
    issueW(IC<0>{}); issueW(IC<1>{});
    asm volatile("s_waitcnt vmcnt(10)" ::: "memory");
    if (s < 3) {
      const float cf = gather_sum(128 * 8);
      if (h == 0) ((LAS float*)(L + AUX_CF))[32 * s + r32] = cf;
      asm volatile("s_waitcnt lgkmcnt(0)" ::: "memory");
      if (lane == 0) *(volatile LAS unsigned*)(L + AUX_W + 4 * s) = 1u;
    }
    STAMP(1);
    asm volatile("" : "+v"(pv[4]));
    if (!__all(pv[4] == tag)) flags_wait(ws, idxq, tag);
    if (lane == 0) *(volatile LAS unsigned*)(L + AUX_W + 16 + 4 * s) = 1u;
    STAMP(2);
    {
      const int key0 = 256 * qi - 256;
      const char* kb = ws + WS_QK + (size_t)80 * S_ * 16;
      const u32x4 z = {0u, 0u, 0u, 0u};
#pragma unroll
      for (int i = 0; i < 2; i++) {
        const int pc = 2 * s + i, kk = key0 + 64 * pc;
        if (kk >= 0) {
          glds16c<L_K>(lane * 16, kb + ((size_t)cgA * S_ + kk) * 16, lds0 + pc * 1024);
          glds16c<L_K + 8192>(lane * 16, kb + ((size_t)(cgA + 1) * S_ + kk) * 16, lds0 + pc * 1024);
          glds16c<L_K + 16384>(lane * 16, kb + ((size_t)cgX * S_ + kk) * 16, lds0 + pc * 1024);
        } else {
#pragma unroll
          for (int c = 0; c < 3; c++) *(LAS u32x4*)(L + L_K + c * 8192 + pc * 1024 + lane * 16) = z;
        }
      }
      const char* vb = ws + WS_VT + ((long)hd * 128 + key0 / 16) * 640;
#pragma unroll
      for (int i = 0; i < 5; i++) {
        const int pc = 5 * s + i;
        if (qi > 0 || pc >= 10) glds16c<L_V>(lane * 16, vb + pc * 1024, lds0 + pc * 1024);
        else *(LAS u32x4*)(L + L_V + pc * 1024 + lane * 16) = z;
      }
    }
    u32x4 qf0, qf1;
    load_q(rws, w, lane, qi, cgA, cgX, odd, qf0, qf1);
    wait_bar<0>();
    STAMP(3);
    if (w == 4 && lane == 0) flag_store(ws, FL_2 + rb * 16 + j, tag);
    issueW(IC<2>{}); issueW(IC<3>{}); issueW(IC<4>{});
    attention_task(rws, lds0, w, lane, qi, cgA, cgX, odd, qf0, qf1, []() {}, [&](auto T) {}, []() {});
    STAMP(4);
    sfor<1, 9>([&](auto SP) {
      constexpr int st = 2 * SP;
      wait_bar<(st == 2) ? 7 : (st == 4 ? 10 : ((st <= 12) ? 15 : (st == 14 ? 5 : 0)))>();
      if constexpr (st == 2) { STAMP(6); issueW(IC<5>{}); issueW(IC<6>{}); issueW(IC<7>{}); }
      if constexpr (st == 4) { if (w == 4 && lane == 0) flag_store(ws, FL_3 + hd * 8 + qi, tag); issueW(IC<8>{}); }
      if constexpr (st >= 4 && st + 6 < 17) { issueW(IC<(st + 6 < 17 ? st + 5 : 0)>{}); issueW(IC<(st + 6 < 17 ? st + 6 : 0)>{}); }
    });
    lds_bar();
    STAMP(7);
    if (w == 4) { STAMP_DUMP(1); }
  }
}

extern "C" void kernel_launch(void* const* d_in, const int* in_sizes, int n_in, void* d_out, int out_size, void* d_ws, size_t ws_size,
                              hipStream_t stream) {
  (void)hipFuncSetAttribute((const void*)k_fused, hipFuncAttributeMaxDynamicSharedMemorySize, LDS_BYTES);
  k_fused<<<256, 512, LDS_BYTES, stream>>>((const u16*)d_in[0], (const u16*)d_in[1], (const u16*)d_in[2], (const u16*)d_in[4],
                                           (const u16*)d_in[5], (const u16*)d_in[6], (const u16*)d_in[7], (char*)d_ws, (u16*)d_out);
}
```

```cpp
#include <hip/hip_runtime.h>
#include <type_traits>

typedef unsigned short u16;
typedef unsigned u32x4 __attribute__((ext_vector_type(4)));
typedef unsigned u32x2 __attribute__((ext_vector_type(2)));
typedef float f32x4 __attribute__((ext_vector_type(4)));
typedef float f32x16 __attribute__((ext_vector_type(16)));
typedef short s16x8 __attribute__((ext_vector_type(8)));
typedef short s16x4 __attribute__((ext_vector_type(4)));
typedef __bf16 bf16x2_t __attribute__((ext_vector_type(2)));
#define LAS __attribute__((address_space(3)))
#define GAS __attribute__((address_space(1)))
#define DI __device__ __forceinline__
#define SB() __builtin_amdgcn_sched_barrier(0)
template <int N> using IC = std::integral_constant<int, N>;

constexpr int S_ = 2048, D_ = 640, F_ = 1536, NQKV = 1920;

constexpr unsigned WS_QK = 0;
constexpr unsigned WS_VT = 0x500000;
constexpr unsigned WS_ACT = 0x800000;
constexpr unsigned WS_WT = 0x1100000;
constexpr unsigned WS_FLAG = 0x1400000;
constexpr unsigned WS_STAT = 0x1418000;
constexpr unsigned WS_CSP = 0x1420000;
constexpr unsigned WS_BYTES = 0x14a0000;
constexpr int FL_EPOCH = 0, FL_1 = 16, FL_2 = FL_1 + 256, FL_3 = FL_2 + 256, FL_T = FL_3 + 256;

constexpr int L_B = 0;
constexpr int L_T = 65536;
constexpr int L_A = 78592;
constexpr int L_K = 115456;
constexpr int L_V = L_K + 24576;
constexpr int L_AUX = 160512;
constexpr int AUX_ST = L_AUX;
constexpr int AUX_CS = AUX_ST + 1024;
constexpr int AUX_CF = AUX_CS + 512;
constexpr int AUX_B1 = AUX_CF + 512;
constexpr int AUX_BF = AUX_B1 + 256;
constexpr int AUX_C = AUX_BF + 512;
constexpr int AUX_W = AUX_C + 32;
constexpr int LDS_BYTES = 163840;
constexpr int W3[7] = {73728, 94208, 0, 20480, 40960, 115456, 135936};
constexpr float QSCALE = 0.22360679774997896f * 1.4426950408889634f;

template <int I, int N, class Fn> DI void sfor(Fn&& f) {
  if constexpr (I < N) { f(IC<I>{}); sfor<I + 1, N>(f); }
}
template <int N> DI void wait_bar() { SB(); asm volatile("s_waitcnt vmcnt(%0) lgkmcnt(0)\n\ts_barrier" ::"n"(N) : "memory"); SB(); }
DI void lds_bar() { SB(); asm volatile("s_waitcnt lgkmcnt(0)\n\ts_barrier" ::: "memory"); SB(); }
DI void drain() { asm volatile("s_waitcnt vmcnt(0)" ::: "memory"); }
template <int IMM> DI void glds16(unsigned voff, const void* sbase, unsigned ldsbase) {
  asm volatile("s_add_i32 m0, %2, %3\n\ts_nop 0\n\tglobal_load_lds_dwordx4 %0, %1" ::"v"(voff), "s"(sbase), "s"(ldsbase), "n"(IMM) : "memory", "scc");
}
template <int IMM> DI void glds16c(unsigned voff, const void* sbase, unsigned ldsbase) {
  asm volatile("s_add_i32 m0, %2, %3\n\ts_nop 0\n\tglobal_load_lds_dwordx4 %0, %1 sc1" ::"v"(voff), "s"(sbase), "s"(ldsbase), "n"(IMM) : "memory", "scc");
}
typedef float f32x2 __attribute__((ext_vector_type(2)));
DI f32x2 fma2(f32x2 a, f32x2 b, f32x2 c) { return __builtin_elementwise_fma(a, b, c); }
DI unsigned pk2v(f32x2 v) { bf16x2_t p = {(__bf16)v.x, (__bf16)v.y}; return __builtin_bit_cast(unsigned, p); }
DI float max3(float a, float b, float c) { float r; asm("v_max3_f32 %0, %1, %2, %3" : "=v"(r) : "v"(a), "v"(b), "v"(c)); return r; }
DI float relu(float a) { float r; asm("v_max_f32 %0, 0, %1" : "=v"(r) : "v"(a)); return r; }
DI float bf2f(unsigned b) { return __uint_as_float(b << 16); }
DI unsigned pk2(float lo, float hi) { bf16x2_t p = {(__bf16)lo, (__bf16)hi}; return __builtin_bit_cast(unsigned, p); }
DI f32x16 mfma32(u32x4 a, u32x4 b, f32x16 c) {
  return __builtin_amdgcn_mfma_f32_32x32x16_bf16(__builtin_bit_cast(s16x8, a), __builtin_bit_cast(s16x8, b), c, 0, 0, 0);
}
DI f32x4 mfma16(u32x4 a, u32x4 b, f32x4 c) {
  return __builtin_amdgcn_mfma_f32_16x16x32_bf16(__builtin_bit_cast(s16x8, a), __builtin_bit_cast(s16x8, b), c, 0, 0, 0);
}
DI u32x4 lds128(unsigned a, int off = 0) { return *(const LAS u32x4*)((const LAS char*)(size_t)a + off); }
DI u32x4 tr_frag(unsigned a, int off, int second) {
  s16x4 lo = __builtin_amdgcn_ds_read_tr16_b64_v4i16((LAS s16x4*)((LAS char*)(size_t)a + off));
  s16x4 hi = __builtin_amdgcn_ds_read_tr16_b64_v4i16((LAS s16x4*)((LAS char*)(size_t)a + off + second));
  u32x2 x = __builtin_bit_cast(u32x2, lo), y = __builtin_bit_cast(u32x2, hi);
  return u32x4{x.x, x.y, y.x, y.y};
}
DI float dot2(unsigned a, unsigned b, float c) {
  return __builtin_amdgcn_fdot2_f32_bf16(__builtin_bit_cast(bf16x2_t, a), __builtin_bit_cast(bf16x2_t, b), c, false);
}
DI float sum8(u32x4 v, float c) {
  const unsigned one = 0x3f803f80u;
  c = dot2(v.x, one, c); c = dot2(v.y, one, c); c = dot2(v.z, one, c); return dot2(v.w, one, c);
}
DI float sq8(u32x4 v, float c) { c = dot2(v.x, v.x, c); c = dot2(v.y, v.y, c); c = dot2(v.z, v.z, c); return dot2(v.w, v.w, c); }
DI float vsum8(u32x4 v) {
  const float a = __uint_as_float(v.x << 16) + __uint_as_float(v.y << 16), b = __uint_as_float(v.x & 0xffff0000u) + __uint_as_float(v.y & 0xffff0000u);
  const float c = __uint_as_float(v.z << 16) + __uint_as_float(v.w << 16), d = __uint_as_float(v.z & 0xffff0000u) + __uint_as_float(v.w & 0xffff0000u);
  return (a + b) + (c + d);
}
DI void granule_store(char* ws, unsigned off, float v, unsigned tag) {
  __hip_atomic_store((GAS unsigned long long*)(ws + off), ((unsigned long long)tag << 32) | __float_as_uint(v), __ATOMIC_RELAXED, __HIP_MEMORY_SCOPE_AGENT);
}
DI unsigned long long granule_load(char* ws, unsigned off) {
  return __hip_atomic_load((GAS unsigned long long*)(ws + off), __ATOMIC_RELAXED, __HIP_MEMORY_SCOPE_AGENT);
}
template <int CTRL> DI float dpp_add(float v) {
  return v + __uint_as_float(__builtin_amdgcn_update_dpp(0u, __float_as_uint(v), CTRL, 0xf, 0xf, false));
}
DI float half_sum(float v) {
  v = dpp_add<0xB1>(v);
  v = dpp_add<0x4E>(v);
  v = dpp_add<0x141>(v);
  v = dpp_add<0x140>(v);
  auto r = __builtin_amdgcn_permlane16_swap(__float_as_uint(v), __float_as_uint(v), false, false);
  return __uint_as_float(r[0]) + __uint_as_float(r[1]);
}
DI float xhalf_add(float v) {
  auto r = __builtin_amdgcn_permlane32_swap(__float_as_uint(v), __float_as_uint(v), false, false);
  return __uint_as_float(r[0]) + __uint_as_float(r[1]);
}
DI float xhalf_max(float v) {
  auto r = __builtin_amdgcn_permlane32_swap(__float_as_uint(v), __float_as_uint(v), false, false);
  return fmaxf(__uint_as_float(r[0]), __uint_as_float(r[1]));
}
DI u32x4 pack_feat8(unsigned ax, unsigned ay, unsigned bx, unsigned by) {
  auto rx = __builtin_amdgcn_permlane32_swap(ax, bx, false, false);
  auto ry = __builtin_amdgcn_permlane32_swap(ay, by, false, false);
  return u32x4{rx[0], ry[0], rx[1], ry[1]};
}
DI f32x2 lo2(const f32x4& v) { return f32x2{v.x, v.y}; }
DI f32x2 hi2(const f32x4& v) { return f32x2{v.z, v.w}; }
template <int I> DI f32x2 pair(const f32x16& v) { return f32x2{v[2 * I], v[2 * I + 1]}; }
DI int fresh_lane() { int l; asm volatile("v_mbcnt_lo_u32_b32 %0, -1, 0\n\tv_mbcnt_hi_u32_b32 %0, -1, %0" : "=v"(l)); return l; }
typedef __amdgpu_buffer_rsrc_t rsrc_t;
DI void st128c(rsrc_t r, unsigned off, u32x4 v) { __builtin_amdgcn_raw_buffer_store_b128(v, r, off, 0, 16); }
DI void st64c(rsrc_t r, unsigned off, u32x2 v) { __builtin_amdgcn_raw_buffer_store_b64(v, r, off, 0, 16); }
DI u32x4 ld128c(rsrc_t r, unsigned off) { return __builtin_amdgcn_raw_buffer_load_b128(r, off, 0, 16); }
DI u32x2 ld64c(rsrc_t r, unsigned off) { return __builtin_amdgcn_raw_buffer_load_b64(r, off, 0, 16); }
DI unsigned flag_load(char* ws, int idx) {
  return __hip_atomic_load((GAS unsigned*)(ws + WS_FLAG) + idx * 16, __ATOMIC_RELAXED, __HIP_MEMORY_SCOPE_AGENT);
}
DI void flag_store(char* ws, int idx, unsigned v) {
  __hip_atomic_store((GAS unsigned*)(ws + WS_FLAG) + idx * 16, v, __ATOMIC_RELAXED, __HIP_MEMORY_SCOPE_AGENT);
}
DI unsigned read_epoch(const char* ws) {
  unsigned e;
  asm volatile("s_load_dword %0, %1, 0x0\n\ts_waitcnt lgkmcnt(0)" : "=s"(e) : "s"(ws + WS_FLAG + FL_EPOCH * 64) : "memory");
  return e;
}
DI void arrive_publish(LAS char* L, int word, unsigned total, char* ws, int flag, unsigned tag, int lane) {
  if (lane == 0) {
    if (__hip_atomic_fetch_add((LAS unsigned*)(L + word), 1u, __ATOMIC_RELAXED, __HIP_MEMORY_SCOPE_WORKGROUP) == total - 1u) flag_store(ws, flag, tag);
  }
}
DI void flags_wait(char* ws, int idx, unsigned tag) {
  for (int spin = 0; spin < (1 << 22); spin++) {
    const bool ok = idx < 0 || flag_load(ws, idx) == tag;
    if (__all(ok)) break;
    __builtin_amdgcn_s_sleep(1);
  }
  __builtin_amdgcn_fence(__ATOMIC_ACQUIRE, "wavefront");
}

#define STAMP_DECL
#define STAMP(i)
#define STAMP_DUMP(k)
#define STAMPC(i)

template <class Hook0, class Hook1, class Hook2>
DI void attention_task(rsrc_t rws, unsigned lds0, int w, int lane, int qi, int cgA, int cgX, int odd, u32x4 qf0, u32x4 qf1, Hook0&& mid,
                       Hook1&& per_tile, Hook2&& pre_store) {
  const int r32 = lane & 31, h = lane >> 5;
  const int q0 = 256 * qi + 32 * w;
  const unsigned ka = lds0 + L_K + h * 8192 + (32 * w + r32) * 16;
  const unsigned kx = lds0 + L_K + 16384 + (32 * w + r32) * 16;
  f32x16 sc[9];
  const f32x16 zero = {};
  const unsigned va = r32 < 20 ? lds0 + L_V + (2 * w) * 640 + h * 320 + r32 * 16 : lds0 + AUX_C + (r32 == 20 ? 0 : 16);
  const int vstep = r32 < 20 ? 640 : 0;
  const int tmin = qi == 0 ? 8 - w : 0;
  u32x4 vf[18];
  unsigned dm[8];
  float mxa[4] = {-INFINITY, -INFINITY, -INFINITY, -INFINITY};
  {
    u32x4 k0[9], k1[9];
#pragma unroll
    for (int n = 0; n < 9; n++) { k0[n] = lds128(ka, ((n + 8) % 9) * 512); k1[n] = lds128(kx, ((n + 8) % 9) * 512); }
    sfor<0, 11>([&](auto N) {
      constexpr int n = N, t = (n + 8) % 9, u = n - 3;
      SB();
      if constexpr (n < 9) { sc[t] = mfma32(k0[n], qf0, zero); sc[t] = mfma32(k1[n], qf1, sc[t]); }
      if constexpr (n >= 2 && n < 11) { vf[2 * (n - 2)] = lds128(va + (2 * (n - 2)) * vstep); vf[2 * (n - 2) + 1] = lds128(va + (2 * (n - 2) + 1) * vstep); }
      if constexpr (u >= 0) {
        if (u < tmin) {
#pragma unroll
          for (int i = 0; i < 16; i++) sc[u][i] = -INFINITY;
        }
        if constexpr (u == 0) {
#pragma unroll
          for (int i = 0; i < 16; i += 2) {
            const int kr = (i & 3) + 8 * (i >> 2) + 4 * h;
            const bool d0 = kr <= r32, d1 = kr + 1 <= r32;
            sc[0][i] = d0 ? sc[8][i] : sc[0][i];
            sc[0][i + 1] = d1 ? sc[8][i + 1] : sc[0][i + 1];
            dm[i >> 1] = d1 ? 0xffffffffu : (d0 ? 0x0000ffffu : 0u);
          }
        }
#pragma unroll
        for (int i = 0; i < 8; i++) mxa[i & 3] = max3(mxa[i & 3], sc[u][2 * i], sc[u][2 * i + 1]);
      }
    });
  }
  SB();
  float mx = max3(mxa[0], mxa[1], max3(mxa[2], mxa[3], mxa[3]));
  {
    auto r = __builtin_amdgcn_permlane32_swap(__float_as_uint(mx), __float_as_uint(mx), false, false);
    mx = max3(__uint_as_float(r[0]), __uint_as_float(r[1]), __uint_as_float(r[1]));
  }
  u32x4 nm;
  {
    const unsigned m0 = __float_as_uint(mx) & 0xffff0000u;
    const float r1 = mx - __uint_as_float(m0);
    const unsigned m1 = __float_as_uint(r1) & 0xffff0000u;
    const unsigned m2 = __float_as_uint(r1 - __uint_as_float(m1)) & 0xffff0000u;
    const unsigned a = ((m0 >> 16) | m1) ^ 0x80008000u, c = (m2 >> 16) ^ 0x8000u;
    nm = u32x4{h ? 0u : a, h ? 0u : c, 0u, 0u};
  }
  const u32x4 ones3 = {0x3f803f80u, 0x00003f80u, 0u, 0u};
  mid();
  SB();
  sc[1] = mfma32(ones3, nm, sc[1]);
  sc[2] = mfma32(ones3, nm, sc[2]);
  f32x16 o = {};
  sfor<1, 9>([&](auto T) {
    constexpr int tt = T, t = tt & 7;
    SB();
    if constexpr (tt + 2 < 9) sc[(tt + 2) & 7] = mfma32(ones3, nm, sc[(tt + 2) & 7]);
    per_tile(T);
    unsigned pk[8];
#pragma unroll
    for (int i = 0; i < 8; i++) pk[i] = pk2(__builtin_amdgcn_exp2f(sc[t][2 * i]), __builtin_amdgcn_exp2f(sc[t][2 * i + 1]));
    if constexpr (t != 0) {
      o = mfma32(vf[2 * t], u32x4{pk[0], pk[1], pk[2], pk[3]}, o);
      o = mfma32(vf[2 * t + 1], u32x4{pk[4], pk[5], pk[6], pk[7]}, o);
    } else {
      o = mfma32(vf[16], u32x4{pk[0] & dm[0], pk[1] & dm[1], pk[2] & dm[2], pk[3] & dm[3]}, o);
      o = mfma32(vf[17], u32x4{pk[4] & dm[4], pk[5] & dm[5], pk[6] & dm[6], pk[7] & dm[7]}, o);
      o = mfma32(vf[0], u32x4{pk[0] & ~dm[0], pk[1] & ~dm[1], pk[2] & ~dm[2], pk[3] & ~dm[3]}, o);
      o = mfma32(vf[1], u32x4{pk[4] & ~dm[4], pk[5] & ~dm[5], pk[6] & ~dm[6], pk[7] & ~dm[7]}, o);
    }
  });
  SB();
  pre_store();
  SB();
  auto lr = __builtin_amdgcn_permlane32_swap(__float_as_uint(o[8]), __float_as_uint(o[8]), false, false);
  const float inv = __builtin_amdgcn_rcpf(__uint_as_float(lr[1]));
  const unsigned ob = WS_ACT + 192u * S_ * 16;
  st128c(rws, ob + ((cgA + h) * S_ + q0 + r32) * 16,
         u32x4{pk2(o[0] * inv, o[1] * inv), pk2(o[2] * inv, o[3] * inv), pk2(o[4] * inv, o[5] * inv), pk2(o[6] * inv, o[7] * inv)});
  if (h == 0) st64c(rws, ob + (cgX * S_ + q0 + r32) * 16 + odd * 8, u32x2{pk2(o[8] * inv, o[9] * inv), pk2(o[10] * inv, o[11] * inv)});
}
DI void load_q(rsrc_t rws, int w, int lane, int qi, int cgA, int cgX, int odd, u32x4& qf0, u32x4& qf1) {
  const int r32 = lane & 31, h = lane >> 5, q0 = 256 * qi + 32 * w;
  qf0 = ld128c(rws, WS_QK + ((cgA + h) * S_ + q0 + r32) * 16);
  const u32x2 xq = ld64c(rws, WS_QK + (cgX * S_ + q0 + r32) * 16 + odd * 8);
  const unsigned x0 = h ? 0u : xq.x, x1 = h ? 0u : xq.y;
  qf1 = odd ? u32x4{0u, 0u, x0, x1} : u32x4{x0, x1, 0u, 0u};
}

__global__ __launch_bounds__(512, 2) void k_fused(const u16* __restrict__ x, const u16* __restrict__ wqkv, const u16* __restrict__ wo,
                                                  const u16* __restrict__ w1, const u16* __restrict__ b1, const u16* __restrict__ w2,
                                                  const u16* __restrict__ b2, char* __restrict__ ws, u16* __restrict__ out) {
  extern __shared__ __attribute__((aligned(16))) char smem[];
  LAS char* L = (LAS char*)smem;
  const unsigned lds0 = (unsigned)(size_t)L;
  const int tid = threadIdx.x, lane = tid & 63, w = __builtin_amdgcn_readfirstlane(tid >> 6);
  const int b = blockIdx.x, xcd = b & 7, bi = b >> 3;
  const int s = w & 3;
  const rsrc_t rws = __builtin_amdgcn_make_buffer_rsrc(ws, 0, WS_BYTES, 0x00020000);
  const int rb = (xcd & 1) * 8 + (bi >> 2), j = (xcd >> 1) * 4 + (bi & 3);
  const int row0 = rb * 128;
  const int kc = (5 * rb) >> 1, rot = kc >> 2, kc0 = kc & 3, kc1 = kc0 + 2 + (rb & 1);
  const int hd = xcd * 4 + (bi & 3), qi = bi >> 2;
  const int odd = hd & 1, cgA = (hd >> 1) * 5 + (odd ? 3 : 0), cgX = (hd >> 1) * 5 + 2;
  const int rb3 = (xcd >> 1) * 8 + (bi >> 2), cgp = (xcd & 1) * 4 + (bi & 3);
  const int row3 = rb3 * 64, n0 = cgp * 80;
  STAMP_DECL;
  STAMP(0); STAMPC(38);
  const int r32 = lane & 31, h = lane >> 5;
  const int blk = (lane >> 4) & 1, q4 = (lane & 15) >> 2, p4 = lane & 3;
  const unsigned a0 = lds0 + L_A + (32 * s + r32) * 128 + ((h ^ ((r32 >> 1) & 7)) * 16);
  auto rdA = [&](int p, int t) -> u32x4 {
    if (p % 5 < 4) return lds128(a0 ^ (t * 32), (p % 5) * 16384);
    unsigned ad;
    asm volatile("v_add_u32 %0, 0x10000, %1" : "=v"(ad) : "v"(a0 ^ (t * 32)));
    return lds128(ad);
  };
  const unsigned bb = lds0 + L_B + (8 * h + q4) * 256 + (4 * q4 + 2 * blk + (p4 >> 1)) * 16 + 8 * (p4 & 1);
  const unsigned fa = lds0 + L_B + (8 * h + q4) * 192 + (2 * blk + (p4 >> 1)) * 16 + 8 * (p4 & 1);
  constexpr int FP[6] = {36864, 49152, 61440, 0, 12288, 24576};
  auto rdB = [&](int p, int t, int u) { return tr_frag(bb ^ (64 * u), (p & 3) * 16384 + t * 4096, 1024); };
  auto rdF = [&](int f, int t, int u) { return tr_frag(fa, FP[f % 6] + t * 3072 + u * 64, 768); };

  if (w < 4) {
    const unsigned tag = read_epoch(ws) + 1u;
    u32x4 af[20], at[4];
    auto getA = [&](auto P, auto T) -> u32x4& { if constexpr (P < 5) return af[4 * P + T]; else return at[T]; };
    unsigned long long stg = 0;
    auto stoff = [&]() { const int lc = fresh_lane(); return WS_STAT + (row0 + 32 * s + (lc & 31)) * 16 + (lc >> 5) * 8; };
    f32x16 acc[4] = {};
    auto body = [&](auto MODE) {
      u32x4 bs[3][4];
      auto mm = [&](auto N, int u) {
        constexpr int n = N;
        if constexpr (MODE != 2) {
          const u32x4 a = getA(IC<n / 4>{}, IC<n % 4>{});
          if constexpr (MODE == 0) acc[u] = mfma32(bs[n % 3][u], a, acc[u]); else acc[u] = mfma32(a, bs[n % 3][u], acc[u]);
        }
      };
      auto rB = [&](int g, int t, int u) { if constexpr (MODE != 2) bs[(4 * g + t) % 3][u] = rdB(g, t, u); };
      sfor<0, 10>([&](auto G) {
        constexpr int g = G;
        lds_bar();
        if constexpr (g == 0 || g == 5) { STAMP(1 + g); }
#pragma unroll
        for (int u = 0; u < 4; u++) rB(g, 0, u);
        sfor<1, 4>([&](auto T) {
          constexpr int t = T;
          SB();
#pragma unroll
          for (int u = 0; u < 4; u++) {
            if constexpr (4 * g + t - 3 >= 0) mm(IC<(4 * g + t - 3 >= 0 ? 4 * g + t - 3 : 0)>{}, u);
            if (t == 1 && u == 3) getA(G, IC<0>{}) = rdA(g, 0);
            if (t == 1 && u == 3) getA(G, IC<1>{}) = rdA(g, 1);
            if (t == 2 && u == 3) getA(G, IC<2>{}) = rdA(g, 2);
            if (t == 3 && u == 0) getA(G, IC<3>{}) = rdA(g, 3);
            rB(g, t, u);
            SB();
          }
        });
#pragma unroll
        for (int u = 0; u < 4; u++) mm(IC<4 * g + 1>{}, u);
        if constexpr (g == 8) stg = granule_load(ws, stoff());
      });
#pragma unroll
      for (int u = 0; u < 4; u++) mm(IC<38>{}, u);
#pragma unroll
      for (int u = 0; u < 4; u++) mm(IC<39>{}, u);
    };
    if (j < 10) body(IC<0>{}); else if (j < 15) body(IC<1>{}); else body(IC<2>{});
    STAMP(11);
    float mur, rs;
    {
      for (int spin = 0; spin < (1 << 22) && !__all((unsigned)(stg >> 32) == tag); spin++) stg = granule_load(ws, stoff());
      auto r = __builtin_amdgcn_permlane32_swap((unsigned)stg, (unsigned)stg, false, false);
      mur = __uint_as_float(r[0]); rs = __uint_as_float(r[1]);
    }
    const int l2 = fresh_lane(), r2 = l2 & 31, h2 = l2 >> 5;
    if (j >= 10 && h2 == 0) {
      ((LAS float*)(L + AUX_ST))[32 * s + r2] = mur;
      ((LAS float*)(L + AUX_ST))[128 + 32 * s + r2] = rs;
    }
    lds_bar();
    if (j < 10) {
      const float sc = (j < 5) ? QSCALE : 1.0f;
      const f32x2 a2 = {rs * sc, rs * sc}, b2q = {-mur * sc, -mur * sc};
      const unsigned dst = WS_QK + ((16 * j + h2) * S_ + row0 + 32 * s + r2) * 16;
      const unsigned csa = lds0 + AUX_CS + 16 * h2;
      sfor<0, 2>([&](auto UU) {
        f32x4 cs[8];
#pragma unroll
        for (int i = 0; i < 8; i++) cs[i] = __builtin_bit_cast(f32x4, lds128(csa, (64 * UU + 8 * i) * 4));
        SB();
        sfor<0, 4>([&](auto C) {
          constexpr int u = 2 * UU + C / 2, k = C % 2, ci = 2 * C;
          const unsigned p0 = pk2v(fma2(pair<4 * k>(acc[u]), a2, lo2(cs[ci]) * b2q)), p1 = pk2v(fma2(pair<4 * k + 1>(acc[u]), a2, hi2(cs[ci]) * b2q));
          const unsigned p2 = pk2v(fma2(pair<4 * k + 2>(acc[u]), a2, lo2(cs[ci + 1]) * b2q)), p3 = pk2v(fma2(pair<4 * k + 3>(acc[u]), a2, hi2(cs[ci + 1]) * b2q));
          st128c(rws, dst + (4 * u + 2 * k) * (S_ * 16), pack_feat8(p0, p1, p2, p3));
        });
      });
    } else if (j < 15) {
      f32x4 murv[4], rsv[4];
      float csv[4];
#pragma unroll
      for (int g = 0; g < 4; g++) {
        murv[g] = *(const LAS f32x4*)(L + AUX_ST + (32 * s + 8 * g + 4 * h2) * 4);
        rsv[g] = *(const LAS f32x4*)(L + AUX_ST + 512 + (32 * s + 8 * g + 4 * h2) * 4);
        csv[g] = -*(const LAS float*)(L + AUX_CS + (32 * g + r2) * 4);
      }
      sfor<0, 4>([&](auto U) {
        constexpr int u = U;
        const f32x2 cs2 = {csv[u], csv[u]};
        const int colv = 128 * (j - 10) + 32 * u + r2;
        const int head = colv / 20, d = colv - head * 20;
        const int dd = (head & 1) ? (d < 4 ? d + 16 : d - 4) : d;
        const int slot = dd < 16 ? ((dd & 3) + 8 * ((dd >> 2) & 1) + 4 * (dd >> 3)) : dd;
        const unsigned dst = WS_VT + ((head * 128 + (row0 + 32 * s) / 16) * 2 + h2) * 320 + slot * 16;
        sfor<0, 2>([&](auto SP) {
          constexpr int sp = SP;
          const unsigned p0 = pk2v(fma2(pair<4 * sp>(acc[u]), lo2(rsv[2 * sp]), cs2 * lo2(murv[2 * sp])));
          const unsigned p1 = pk2v(fma2(pair<4 * sp + 1>(acc[u]), hi2(rsv[2 * sp]), cs2 * hi2(murv[2 * sp])));
          const unsigned p2 = pk2v(fma2(pair<4 * sp + 2>(acc[u]), lo2(rsv[2 * sp + 1]), cs2 * lo2(murv[2 * sp + 1])));
          const unsigned p3 = pk2v(fma2(pair<4 * sp + 3>(acc[u]), hi2(rsv[2 * sp + 1]), cs2 * hi2(murv[2 * sp + 1])));
          st128c(rws, dst + sp * 640, u32x4{p0, p1, p2, p3});
        });
      });
    }
    STAMP(12);
    f32x16 facc[3] = {};
    {
      u32x4 bs[3][3];
      auto pos = [](int f) { return (f + 5) % 10; };
      auto mm = [&](auto N, int u) {
        constexpr int n = N;
        facc[u] = mfma32(bs[n % 3][u], getA(IC<(n / 4 + 5) % 10>{}, IC<n % 4>{}), facc[u]);
      };
      sfor<0, 10>([&](auto Fs) {
        constexpr int f = Fs;
        if constexpr (f == 2 || f == 4 || f == 6) lds_bar();
        if constexpr (f == 3) { SB(); drain(); arrive_publish(L, AUX_W + 12, 4, ws, FL_1 + rb * 16 + j, tag, fresh_lane()); SB(); }
        if constexpr (f == 5) { STAMP(13 + f); }
#pragma unroll
        for (int u = 0; u < 3; u++) bs[(4 * f) % 3][u] = rdF(f, 0, u);
        sfor<1, 4>([&](auto T) {
          constexpr int t = T;
          SB();
#pragma unroll
          for (int u = 0; u < 3; u++) {
            if constexpr (4 * f + t - 3 >= 0) mm(IC<(4 * f + t - 3 >= 0 ? 4 * f + t - 3 : 0)>{}, u);
            if constexpr (f < 5) {
              if (t == 1 && u == 2) at[0] = rdA(pos(f), 0);
              if (t == 1 && u == 2) at[1] = rdA(pos(f), 1);
              if (t == 2 && u == 2) at[2] = rdA(pos(f), 2);
              if (t == 3 && u == 0) at[3] = rdA(pos(f), 3);
            }
            bs[(4 * f + t) % 3][u] = rdF(f, t, u);
            SB();
          }
        });
#pragma unroll
        for (int u = 0; u < 3; u++) mm(IC<4 * f + 1>{}, u);
      });
#pragma unroll
      for (int u = 0; u < 3; u++) mm(IC<38>{}, u);
#pragma unroll
      for (int u = 0; u < 3; u++) mm(IC<39>{}, u);
    }
    STAMP(23);
    {
      const int l3 = fresh_lane(), r3 = l3 & 31, h3 = l3 >> 5;
      while (true) {
        const u32x4 rdy = *(const volatile LAS u32x4*)(L + AUX_W);
        if (__builtin_amdgcn_readfirstlane(rdy.x & rdy.y & rdy.z) == 1u) break;
      }
      asm volatile("" ::: "memory");
      const unsigned dst = WS_ACT + ((12 * j + h3) * S_ + row0 + 32 * s + r3) * 16;
      const unsigned ca = lds0 + AUX_CF + 16 * h3;
      const f32x2 rs2 = {rs, rs}, nm2 = {-mur, -mur};
      auto act = [&](f32x2 a, f32x2 cs, f32x2 bv) {
        f32x2 t = fma2(a, rs2, fma2(cs, nm2, bv));
        t = f32x2{relu(t.x), relu(t.y)};
        return pk2v(t * t);
      };
      sfor<0, 3>([&](auto U) {
        constexpr int u = U;
        f32x4 cs[4], bv[4];
#pragma unroll
        for (int i = 0; i < 4; i++) {
          cs[i] = __builtin_bit_cast(f32x4, lds128(ca, (32 * u + 8 * i) * 4));
          bv[i] = __builtin_bit_cast(f32x4, lds128(ca, AUX_BF - AUX_CF + (32 * u + 8 * i) * 4));
        }
        SB();
        sfor<0, 2>([&](auto K) {
          constexpr int k = K;
          const unsigned p0 = act(pair<4 * k>(facc[u]), lo2(cs[2 * k]), lo2(bv[2 * k])), p1 = act(pair<4 * k + 1>(facc[u]), hi2(cs[2 * k]), hi2(bv[2 * k]));
          const unsigned p2 = act(pair<4 * k + 2>(facc[u]), lo2(cs[2 * k + 1]), lo2(bv[2 * k + 1]));
          const unsigned p3 = act(pair<4 * k + 3>(facc[u]), hi2(cs[2 * k + 1]), hi2(bv[2 * k + 1]));
          st128c(rws, dst + (4 * u + 2 * k) * (S_ * 16), pack_feat8(p0, p1, p2, p3));
        });
      });
    }
    STAMP(24);
    while (__builtin_amdgcn_readfirstlane(*(const volatile LAS unsigned*)(L + AUX_W + 16 + 4 * s)) != 1u) __builtin_amdgcn_s_sleep(1);
    asm volatile("" ::: "memory");
    const int lanem = fresh_lane();
    u32x4 qf0, qf1;
    load_q(rws, w, lanem, qi, cgA, cgX, odd, qf0, qf1);
    wait_bar<0>();
    STAMP(25);
    const int mh = s & 1, kh = s >> 1;
    const int rot3 = ((bi >> 2) + 3 * (bi & 3)) % 12, rot4 = ((bi >> 2) + (bi & 3)) % 5;
    auto kq = [&](int st) { int v; if (st < 12) { v = st + rot3; if (v >= 12) v -= 12; } else { v = st + rot4; if (v >= 17) v -= 5; } return v; };
    u32x4 ar[6][4];
    unsigned avo;
    auto ldA = [&](auto ST) {
      constexpr int st = ST;
      const unsigned so = kq(st) * (16 * S_ * 16);
#pragma unroll
      for (int i = 0; i < 4; i++) ar[st % 6][i] = __builtin_amdgcn_raw_buffer_load_b128(rws, (avo ^ ((i & 1) * 256)) + (i >> 1) * (8 * S_ * 16), so, 16);
    };
    unsigned f2 = tag;
    attention_task(rws, lds0, w, lanem, qi, cgA, cgX, odd, qf0, qf1,
                   [&]() {}, [&](auto T) { if constexpr (T == 5) { if (lanem < 16) f2 = flag_load(ws, FL_2 + (rb3 >> 1) * 16 + lanem); } },
                   [&]() {
                     if (!__all(f2 == tag)) flags_wait(ws, lanem < 16 ? FL_2 + (rb3 >> 1) * 16 + lanem : -1, tag);
                     avo = WS_ACT + ((4 * kh + (lanem >> 4)) * S_ + row3 + 32 * mh + 16 * kh + (lanem & 15)) * 16;
                     ldA(IC<0>{}); ldA(IC<1>{}); ldA(IC<2>{}); ldA(IC<3>{});
                   });
    STAMP(26);
    const int tok = row3 + 32 * mh + 16 * kh + (lanem & 15);
    u32x2 xv[5], bv[5];
    f32x4 accf[2][5] = {}, accs[2][5] = {};
    const unsigned wr = lds0 + kh * 5120 + lanem * 16;
    u32x4 fw[2][5];
    unsigned f3 = tag;
    auto mm1 = [&](auto ST, auto KK, auto X) {
      constexpr int st = ST, kk = KK, i = X / 2, r = X % 2;
      if constexpr (st < 12) accf[r][i] = mfma16(fw[kk][i], ar[st % 6][2 * kk + r], accf[r][i]);
      else accs[r][i] = mfma16(fw[kk][i], ar[st % 6][2 * kk + r], accs[r][i]);
    };
    auto ldA1 = [&](auto ST, auto I) {
      constexpr int st = ST, i = I;
      ar[st % 6][i] = __builtin_amdgcn_raw_buffer_load_b128(rws, (avo ^ ((i & 1) * 256)) + (i >> 1) * (8 * S_ * 16), kq(st) * (16 * S_ * 16), 16);
    };
    sfor<0, 17>([&](auto ST) {
      constexpr int st = ST;
      if constexpr (st == 4) wait_bar<16>(); else if constexpr (st >= 2 && st % 2 == 0) lds_bar();
      if constexpr (st == 2) { STAMP(27); }
      if constexpr (st == 12) { STAMP(28); }
      if constexpr (st == 9) { if (!__all(f3 == tag)) flags_wait(ws, lanem < 32 ? FL_3 + lanem * 8 + (rb3 >> 2) : -1, tag); }
      sfor<0, 10>([&](auto X) {
        constexpr int xx = X;
        if constexpr (xx < 5) fw[0][xx] = lds128(wr, W3[st % 7] + xx * 1024);
        if constexpr (st > 0) mm1(IC<(st > 0 ? st - 1 : 0)>{}, IC<1>{}, X);
        SB();
      });
      sfor<0, 10>([&](auto X) {
        constexpr int xx = X;
        if constexpr (xx < 5) fw[1][xx] = lds128(wr, W3[st % 7] + 10240 + xx * 1024);
        mm1(ST, IC<0>{}, X);
        if constexpr (xx >= 6 && st <= 7) ldA1(IC<(st <= 7 ? st + 4 : 0)>{}, IC<(xx >= 6 ? xx - 6 : 0)>{});
        if constexpr (xx >= 6 && st >= 9 && st <= 11) ldA1(IC<(st >= 9 && st <= 11 ? 2 * st - 6 : 0)>{}, IC<(xx >= 6 ? xx - 6 : 0)>{});
        if constexpr (xx >= 2 && xx < 6 && (st == 9 || st == 10)) ldA1(IC<(st == 9 || st == 10 ? 2 * st - 5 : 0)>{}, IC<(xx >= 2 ? (xx - 2) & 3 : 0)>{});
        SB();
      });
      if constexpr (st == 7) { if (lanem < 32) f3 = flag_load(ws, FL_3 + lanem * 8 + (rb3 >> 2)); }
      if constexpr (st == 12) {
#pragma unroll
        for (int i = 0; i < 5; i++) {
          xv[i] = *(const u32x2*)(x + (size_t)tok * D_ + n0 + 16 * i + 4 * (lanem >> 4));
          bv[i] = *(const u32x2*)(b2 + n0 + 16 * i + 4 * (lanem >> 4));
        }
      }
    });
    sfor<0, 10>([&](auto X) { mm1(IC<16>{}, IC<1>{}, X); });
    STAMP(29);
    f32x4 gf[5], gs[5];
    {
      LAS f32x4* ex = (LAS f32x4*)(L + W3[5]) + s * 640 + lanem;
#pragma unroll
      for (int i = 0; i < 5; i++) { ex[i * 64] = accf[1][i]; ex[320 + i * 64] = accs[1][i]; }
      lds_bar();
      const LAS f32x4* ey = (const LAS f32x4*)(L + W3[5]) + (s ^ 2) * 640 + lanem;
#pragma unroll
      for (int i = 0; i < 5; i++) { gf[i] = accf[0][i] + ey[i * 64]; gs[i] = accs[0][i] + ey[320 + i * 64]; }
    }
#pragma unroll
    for (int i = 0; i < 5; i++) {
      const float xf[4] = {bf2f(xv[i].x & 0xffffu), bf2f(xv[i].x >> 16), bf2f(xv[i].y & 0xffffu), bf2f(xv[i].y >> 16)};
      const float bf[4] = {bf2f(bv[i].x & 0xffffu), bf2f(bv[i].x >> 16), bf2f(bv[i].y & 0xffffu), bf2f(bv[i].y >> 16)};
      float r[4];
#pragma unroll
      for (int e = 0; e < 4; e++) {
        const float g = gf[i][e] + bf[e];
        const float sg = g * __builtin_amdgcn_rcpf(1.0f + __builtin_amdgcn_exp2f(-1.4426950408889634f * g));
        r[e] = __builtin_fmaf(gs[i][e], sg, xf[e]);
      }
      *(u32x2*)(out + (size_t)tok * D_ + n0 + 16 * i + 4 * (lanem >> 4)) = u32x2{pk2(r[0], r[1]), pk2(r[2], r[3])};
    }
    STAMP(30); STAMPC(39);
    if (w == 0) { STAMP_DUMP(0); }
  } else {
    const char* xbase = (const char*)(x + (size_t)row0 * D_);
    const char* fbase = (const char*)(w1 + 96 * j);
    const char* qbase = (const char*)(wqkv + 128 * (j < 15 ? j : 14));
    unsigned voffA[4], voffB[4], voffF[3];
#pragma unroll
    for (int e = 0; e < 4; e++) {
      voffA[e] = ((32 * s + 8 * e + (lane >> 3)) * D_ + (((lane & 7) ^ (4 * (e & 1) + (lane >> 4))) * 8)) * 2;
      voffB[e] = (16 * s + 4 * e + (lane >> 4)) * (NQKV * 2) + ((lane & 15) ^ ((lane >> 4) << 2)) * 16;
    }
    const unsigned voffT = (8 * s + (lane >> 4)) * (D_ * 2) + ((lane & 15) ^ ((lane >> 4) << 2)) * 16;
#pragma unroll
    for (int e = 0; e < 3; e++) { const int Lb = e * 1024 + lane * 16; voffF[e] = (16 * s + Lb / 192) * (F_ * 2) + Lb % 192; }
    const unsigned l4 = lds0 + s * 4096, l3k = lds0 + s * 3072;
    auto kst = [&](int p) { const int v = p + rot; return v >= 10 ? v - 10 : v; };
    auto dmaP = [&](auto P, auto E, const char* xs, const char* qs) {
      constexpr int p = P, e = E;
      if constexpr (e < 4) glds16<L_A + (p % 5) * 16384 + e * 1024>(voffA[e], xs, l4);
      else glds16<L_B + (p & 3) * 16384 + (e - 4) * 1024>(voffB[e - 4], qs, l4);
    };
    auto dmaF = [&](auto Fs) {
      constexpr int f = Fs;
      const char* src = fbase + (size_t)(kst((f + 5) % 10) * (64 * F_ * 2));
      glds16<L_B + FP[f % 6]>(voffF[0], src, l3k); glds16<L_B + FP[f % 6] + 1024>(voffF[1], src, l3k); glds16<L_B + FP[f % 6] + 2048>(voffF[2], src, l3k);
    };
    auto dmaT = [&](int hc) {
      const unsigned kb = (unsigned)hc / 5u, cb = hc - kb * 5;
      const size_t base = kb < 48 ? (size_t)w2 : (size_t)wo - (size_t)48 * 32 * D_ * 2;
      const char* src = (const char*)(base + (size_t)(kb * (32 * D_ * 2) + cb * 256));
      glds16<L_T>(voffT, src, lds0 + s * 2048);
      glds16<L_T + 1024>(voffT, src + 4 * D_ * 2, lds0 + s * 2048);
    };
    u32x2 sx[5];
    {
      const unsigned so = ((8 * j + 2 * s + h) * D_ + 4 * r32) * 2;
#pragma unroll
      for (int i = 0; i < 5; i++) asm volatile("global_load_dwordx2 %0, %1, %2 offset:%3" : "=v"(sx[i]) : "v"(so), "s"(xbase), "n"(256 * i) : "memory");
    }
    if (w == 4) { if (lane < 12) glds16<AUX_B1>(lane * 16, (const char*)(b1 + 96 * j), lds0); }
    sfor<0, 3>([&](auto P) {
      const char* xs = xbase + kst(P) * 128;
      const char* qs = qbase + (size_t)(kst(P) * (64 * NQKV * 2));
      sfor<0, 8>([&](auto E) { dmaP(P, E, xs, qs); });
    });
    if (w == 5 && lane < 16) ((LAS unsigned*)(L + AUX_C))[lane] = lane < 4 ? 0x3f803f80u : 0u;
    const unsigned tag = read_epoch(ws) + 1u;
    {
      asm volatile("s_waitcnt vmcnt(24)" : "+v"(sx[0]), "+v"(sx[1]), "+v"(sx[2]), "+v"(sx[3]), "+v"(sx[4])::"memory");
      float v[20];
#pragma unroll
      for (int i = 0; i < 5; i++) {
        v[4 * i] = bf2f(sx[i].x & 0xffffu); v[4 * i + 1] = bf2f(sx[i].x >> 16); v[4 * i + 2] = bf2f(sx[i].y & 0xffffu); v[4 * i + 3] = bf2f(sx[i].y >> 16);
      }
      float sm = ((v[0] + v[1]) + (v[2] + v[3])) + ((v[4] + v[5]) + (v[6] + v[7])) + ((v[8] + v[9]) + (v[10] + v[11]));
      sm += ((v[12] + v[13]) + (v[14] + v[15])) + ((v[16] + v[17]) + (v[18] + v[19]));
      sm = half_sum(sm);
      const float mu = sm * (1.0f / D_);
      float qq[4] = {0.f, 0.f, 0.f, 0.f};
#pragma unroll
      for (int i = 0; i < 20; i++) qq[i & 3] = __builtin_fmaf(v[i] - mu, v[i] - mu, qq[i & 3]);
      float sq = (qq[0] + qq[1]) + (qq[2] + qq[3]);
      sq = half_sum(sq);
      const float r = __builtin_amdgcn_rsqf(sq * (1.0f / D_) + 1e-5f);
      if (r32 < 2) granule_store(ws, WS_STAT + (row0 + 8 * j + 2 * s + h) * 16 + r32 * 8, r32 ? r : mu * r, tag);
    }
    auto conv = [&](int hc) {
      const int kb = hc / 5, cb = hc % 5, lc = fresh_lane();
      const unsigned dst = WS_WT + ((kb * 4 + 2 * (s >> 1) + (lc >> 5)) * D_ + cb * 128 + 64 * (s & 1) + (lc & 31)) * 16;
      const unsigned src = (bb ^ (128 * (s & 1))) + (s >> 1) * 4096;
      st128c(rws, dst, tr_frag(src, L_T, 1024));
      st128c(rws, dst + 512, tr_frag(src ^ 64, L_T, 1024));
    };
    const unsigned cspub = WS_CSP + ((j * 16 + rb) * 224 + 32 * s + r32) * 8;
    const char* csgat = ws + WS_CSP + (size_t)(j * 16) * 224 * 8;
    const unsigned voffG = (8 * h * 224 + 32 * s + r32) * 8;
    u32x2 gr[8];
    auto gather_issue = [&](int off) {
#pragma unroll
      for (int i = 0; i < 8; i++)
        asm volatile("global_load_dwordx2 %0, %1, %2 sc1" : "=v"(gr[i]) : "v"(voffG), "s"(csgat + off + i * 224 * 8) : "memory");
    };
    auto gather_sum = [&](int off) -> float {
      for (int spin = 0; spin < (1 << 22); spin++) {
#pragma unroll
        for (int i = 0; i < 8; i++) asm volatile("" : "+v"(gr[i]));
        bool ok = true;
#pragma unroll
        for (int i = 0; i < 8; i++) ok &= gr[i].y == tag;
        if (__all(ok)) break;
        gather_issue(off);
        drain();
      }
      float a = 0.f;
#pragma unroll
      for (int i = 0; i < 8; i++) a += __uint_as_float(gr[i].x);
      return xhalf_add(a);
    };
    float cpart = 0.f;
    sfor<0, 10>([&](auto G) {
      constexpr int g = G;
      constexpr int NW[10] = {17, 19, 18, 17, 19, 20, 18, 24, 21, 11};
      wait_bar<NW[g]>();
      if constexpr (g == 1) { if (w == 7 && lane < 48) {
        const unsigned v = *(const LAS unsigned*)(L + AUX_B1 + lane * 4);
        *(LAS u32x2*)(L + AUX_BF + lane * 8) = u32x2{v << 16, v & 0xffff0000u};
      } }
      if constexpr (g == 0) dmaT(b);
      if constexpr (g == 4) dmaT(b < 84 ? 256 + b : b);
      if constexpr (g == 2) { cpart = xhalf_add(cpart); if (h == 0) granule_store(ws, cspub, cpart, tag); else asm volatile("" ::: "memory"); }
      if constexpr (g == 3) conv(b);
      if constexpr (g == 7) conv(b < 84 ? 256 + b : b);
      if constexpr (g == 6) gather_issue(0);
      const char* xs = xbase + kst(g < 7 ? g + 3 : 0) * 128;
      const char* qs = qbase + (size_t)(kst(g < 7 ? g + 3 : 0) * (64 * NQKV * 2));
      if constexpr (g < 7) sfor<0, 8>([&](auto E) { dmaP(IC<g + 3>{}, E, xs, qs); });
      else if constexpr (g == 7) dmaF(IC<0>{});
      else if constexpr (g == 8) { dmaF(IC<1>{}); dmaF(IC<2>{}); }
      else dmaF(IC<3>{});
      if constexpr (g < 2) {
#pragma unroll
        for (int t = 0; t < 4; t++)
          if (4 * g + t >= kc0 && 4 * g + t < kc1) cpart += vsum8(tr_frag(bb ^ (64 * s), (g & 3) * 16384 + t * 4096, 1024));
      }
      if constexpr (g == 9) {
        const float cs = gather_sum(0);
        if (h == 0) ((LAS float*)(L + AUX_CS))[32 * s + r32] = cs;
      }
    });
    cpart = 0.f;
    const int uf = s < 3 ? s : 2;
    unsigned voffW[5];
#pragma unroll
    for (int i = 0; i < 5; i++) voffW[i] = ((lane >> 4) * D_ + (lane & 15) + 16 * i) * 16;
    const char* wbase = ws + WS_WT + (size_t)n0 * 16 + (size_t)s * 4 * D_ * 16;
    const int rot3 = ((bi >> 2) + 3 * (bi & 3)) % 12, rot4 = ((bi >> 2) + (bi & 3)) % 5;
    auto kq = [&](int st) { int v; if (st < 12) { v = st + rot3; if (v >= 12) v -= 12; } else { v = st + rot4; if (v >= 17) v -= 5; } return v; };
    const unsigned l5 = lds0 + s * 5120;
    auto issueW = [&](auto ST) {
      constexpr int st = ST;
      const char* ww = wbase + (size_t)(kq(st) * (16 * D_ * 16));
      sfor<0, 5>([&](auto I) { glds16c<W3[st % 7] + I * 1024>(voffW[I], ww, l5); });
    };
    int idxq;
    {
      const int ty = lane >> 3, rbi = (lane >> 1) & 3, hi = lane & 1;
      const int c0 = ty == 2 ? 20 * hd : 8 * (odd ? cgX : cgA), c1 = ty == 2 ? 20 * hd + 19 : 8 * (odd ? cgA + 1 : cgX) + 7;
      const int tile = 5 * ty + (hi ? c1 : c0) / 128, rr = 2 * qi - 2 + rbi;
      idxq = (lane < 24 && rr >= 0 && (ty > 0 || rbi >= 2)) ? FL_1 + rr * 16 + tile : FL_1 + rb * 16 + j;
    }
    unsigned pv[5];
    auto poll_issue = [&](unsigned& dst, int idx) {
      asm volatile("global_load_dword %0, %1, %2 sc1" : "=v"(dst) : "v"(idx * 64), "s"(ws + WS_FLAG) : "memory");
    };
    wait_bar<6>();
    if (w == 4 && lane == 0) flag_store(ws, FL_T + b, tag);
    dmaF(IC<4>{}); dmaF(IC<5>{});
#pragma unroll
    for (int t = 0; t < 8; t++)
      if (t >= kc0 && t < kc1) cpart += vsum8(tr_frag(fa + uf * 64, FP[t >> 2] + (t & 3) * 3072, 768));
    cpart = xhalf_add(cpart);
    if (h == 0 && s < 3) granule_store(ws, cspub + 128 * 8, cpart, tag);
    wait_bar<6>();
    dmaF(IC<6>{}); dmaF(IC<7>{});
#pragma unroll
    for (int i = 0; i < 4; i++) poll_issue(pv[i], FL_T + 64 * i + lane);
    wait_bar<10>();
    dmaF(IC<8>{}); dmaF(IC<9>{});
    wait_bar<0>();
    gather_issue(128 * 8); poll_issue(pv[0 + 4], idxq);
#pragma unroll
    for (int i = 0; i < 4; i++) asm volatile("" : "+v"(pv[i]));
    if (!__all(pv[0] == tag && pv[1] == tag && pv[2] == tag && pv[3] == tag)) {
#pragma unroll
      for (int i = 0; i < 4; i++) flags_wait(ws, FL_T + 64 * i + lane, tag);
    }
    issueW(IC<0>{}); issueW(IC<1>{});
    asm volatile("s_waitcnt vmcnt(10)" ::: "memory");
    if (s < 3) {
      const float cf = gather_sum(128 * 8);
      if (h == 0) ((LAS float*)(L + AUX_CF))[32 * s + r32] = cf;
      asm volatile("s_waitcnt lgkmcnt(0)" ::: "memory");
      if (lane == 0) *(volatile LAS unsigned*)(L + AUX_W + 4 * s) = 1u;
    }
    STAMP(1);
    asm volatile("" : "+v"(pv[4]));
    if (!__all(pv[4] == tag)) flags_wait(ws, idxq, tag);
    if (lane == 0) *(volatile LAS unsigned*)(L + AUX_W + 16 + 4 * s) = 1u;
    STAMP(2);
    {
      const int key0 = 256 * qi - 256;
      const char* kb = ws + WS_QK + (size_t)80 * S_ * 16;
      const u32x4 z = {0u, 0u, 0u, 0u};
#pragma unroll
      for (int i = 0; i < 2; i++) {
        const int pc = 2 * s + i, kk = key0 + 64 * pc;
        if (kk >= 0) {
          glds16c<L_K>(lane * 16, kb + ((size_t)cgA * S_ + kk) * 16, lds0 + pc * 1024);
          glds16c<L_K + 8192>(lane * 16, kb + ((size_t)(cgA + 1) * S_ + kk) * 16, lds0 + pc * 1024);
          glds16c<L_K + 16384>(lane * 16, kb + ((size_t)cgX * S_ + kk) * 16, lds0 + pc * 1024);
        } else {
#pragma unroll
          for (int c = 0; c < 3; c++) *(LAS u32x4*)(L + L_K + c * 8192 + pc * 1024 + lane * 16) = z;
        }
      }
      const char* vb = ws + WS_VT + ((long)hd * 128 + key0 / 16) * 640;
#pragma unroll
      for (int i = 0; i < 5; i++) {
        const int pc = 5 * s + i;
        if (qi > 0 || pc >= 10) glds16c<L_V>(lane * 16, vb + pc * 1024, lds0 + pc * 1024);
        else *(LAS u32x4*)(L + L_V + pc * 1024 + lane * 16) = z;
      }
    }
    u32x4 qf0, qf1;
    load_q(rws, w, lane, qi, cgA, cgX, odd, qf0, qf1);
    wait_bar<0>();
    STAMP(3);
    if (w == 4 && lane == 0) flag_store(ws, FL_2 + rb * 16 + j, tag);
    issueW(IC<2>{}); issueW(IC<3>{}); issueW(IC<4>{});
    attention_task(rws, lds0, w, lane, qi, cgA, cgX, odd, qf0, qf1, []() {}, [&](auto T) {}, []() {});
    STAMP(4);
    sfor<1, 9>([&](auto SP) {
      constexpr int st = 2 * SP;
      wait_bar<(st == 2) ? 7 : (st == 4 ? 10 : ((st <= 12) ? 15 : (st == 14 ? 5 : 0)))>();
      if constexpr (st == 2) { STAMP(6); issueW(IC<5>{}); issueW(IC<6>{}); issueW(IC<7>{}); }
      if constexpr (st == 4) { if (w == 4 && lane == 0) flag_store(ws, FL_3 + hd * 8 + qi, tag); issueW(IC<8>{}); }
      if constexpr (st >= 4 && st + 6 < 17) { issueW(IC<(st + 6 < 17 ? st + 5 : 0)>{}); issueW(IC<(st + 6 < 17 ? st + 6 : 0)>{}); }
    });
    lds_bar();
    if (b == 0 && w == 7) {
      for (int spin = 0; spin < (1 << 22); spin++) {
        const unsigned f0 = flag_load(ws, FL_2 + lane), f1 = flag_load(ws, FL_2 + 64 + lane), f2 = flag_load(ws, FL_2 + 128 + lane), f3 = flag_load(ws, FL_2 + 192 + lane);
        if (__all(f0 == tag && f1 == tag && f2 == tag && f3 == tag)) break;
      }
      if (lane == 0) flag_store(ws, FL_EPOCH, tag);
    }
    STAMP(7);
    if (w == 4) { STAMP_DUMP(1); }
  }
}

extern "C" void kernel_launch(void* const* d_in, const int* in_sizes, int n_in, void* d_out, int out_size, void* d_ws, size_t ws_size,
                              hipStream_t stream) {
  (void)hipFuncSetAttribute((const void*)k_fused, hipFuncAttributeMaxDynamicSharedMemorySize, LDS_BYTES);
  k_fused<<<256, 512, LDS_BYTES, stream>>>((const u16*)d_in[0], (const u16*)d_in[1], (const u16*)d_in[2], (const u16*)d_in[4],
                                           (const u16*)d_in[5], (const u16*)d_in[6], (const u16*)d_in[7], (char*)d_ws, (u16*)d_out);
}
```

```cpp
#include <hip/hip_runtime.h>
#include <type_traits>

typedef unsigned short u16;
typedef unsigned u32x4 __attribute__((ext_vector_type(4)));
typedef unsigned u32x2 __attribute__((ext_vector_type(2)));
typedef float f32x4 __attribute__((ext_vector_type(4)));
typedef float f32x16 __attribute__((ext_vector_type(16)));
typedef short s16x8 __attribute__((ext_vector_type(8)));
typedef short s16x4 __attribute__((ext_vector_type(4)));
typedef __bf16 bf16x2_t __attribute__((ext_vector_type(2)));
#define LAS __attribute__((address_space(3)))
#define GAS __attribute__((address_space(1)))
#define DI __device__ __forceinline__
#define SB() __builtin_amdgcn_sched_barrier(0)
template <int N> using IC = std::integral_constant<int, N>;

constexpr int S_ = 2048, D_ = 640, F_ = 1536, NQKV = 1920;

constexpr unsigned WS_QK = 0;
constexpr unsigned WS_VT = 0x500000;
constexpr unsigned WS_ACT = 0x800000;
constexpr unsigned WS_WT = 0x1100000;
constexpr unsigned WS_FLAG = 0x1400000;
constexpr unsigned WS_STAT = 0x1418000;
constexpr unsigned WS_CSP = 0x1420000;
constexpr unsigned WS_BYTES = 0x14a0000;
constexpr int FL_EPOCH = 0, FL_1 = 16, FL_2 = FL_1 + 256, FL_3 = FL_2 + 256, FL_T = FL_3 + 256;

constexpr int L_B = 0;
constexpr int L_T = 65536;
constexpr int L_A = 78592;
constexpr int L_K = 115456;
constexpr int L_V = L_K + 24576;
constexpr int L_AUX = 160512;
constexpr int AUX_ST = L_AUX;
constexpr int AUX_CS = AUX_ST + 1024;
constexpr int AUX_CF = AUX_CS + 512;
constexpr int AUX_B1 = AUX_CF + 512;
constexpr int AUX_BF = AUX_B1 + 256;
constexpr int AUX_C = AUX_BF + 512;
constexpr int AUX_W = AUX_C + 32;
constexpr int LDS_BYTES = 163840;
constexpr int W3[7] = {73728, 94208, 0, 20480, 40960, 115456, 135936};
constexpr float QSCALE = 0.22360679774997896f * 1.4426950408889634f;

template <int I, int N, class Fn> DI void sfor(Fn&& f) {
  if constexpr (I < N) { f(IC<I>{}); sfor<I + 1, N>(f); }
}
template <int N> DI void wait_bar() { SB(); asm volatile("s_waitcnt vmcnt(%0) lgkmcnt(0)\n\ts_barrier" ::"n"(N) : "memory"); SB(); }
DI void lds_bar() { SB(); asm volatile("s_waitcnt lgkmcnt(0)\n\ts_barrier" ::: "memory"); SB(); }
DI void drain() { asm volatile("s_waitcnt vmcnt(0)" ::: "memory"); }
template <int IMM> DI void glds16(unsigned voff, const void* sbase, unsigned ldsbase) {
  asm volatile("s_add_i32 m0, %2, %3\n\ts_nop 0\n\tglobal_load_lds_dwordx4 %0, %1" ::"v"(voff), "s"(sbase), "s"(ldsbase), "n"(IMM) : "memory", "scc");
}
template <int IMM> DI void glds16c(unsigned voff, const void* sbase, unsigned ldsbase) {
  asm volatile("s_add_i32 m0, %2, %3\n\ts_nop 0\n\tglobal_load_lds_dwordx4 %0, %1 sc1" ::"v"(voff), "s"(sbase), "s"(ldsbase), "n"(IMM) : "memory", "scc");
}
typedef float f32x2 __attribute__((ext_vector_type(2)));
DI f32x2 fma2(f32x2 a, f32x2 b, f32x2 c) { return __builtin_elementwise_fma(a, b, c); }
DI unsigned pk2v(f32x2 v) { bf16x2_t p = {(__bf16)v.x, (__bf16)v.y}; return __builtin_bit_cast(unsigned, p); }
DI float max3(float a, float b, float c) { float r; asm("v_max3_f32 %0, %1, %2, %3" : "=v"(r) : "v"(a), "v"(b), "v"(c)); return r; }
DI float relu(float a) { float r; asm("v_max_f32 %0, 0, %1" : "=v"(r) : "v"(a)); return r; }
DI float bf2f(unsigned b) { return __uint_as_float(b << 16); }
DI unsigned pk2(float lo, float hi) { bf16x2_t p = {(__bf16)lo, (__bf16)hi}; return __builtin_bit_cast(unsigned, p); }
DI f32x16 mfma32(u32x4 a, u32x4 b, f32x16 c) {
  return __builtin_amdgcn_mfma_f32_32x32x16_bf16(__builtin_bit_cast(s16x8, a), __builtin_bit_cast(s16x8, b), c, 0, 0, 0);
}
DI f32x4 mfma16(u32x4 a, u32x4 b, f32x4 c) {
  return __builtin_amdgcn_mfma_f32_16x16x32_bf16(__builtin_bit_cast(s16x8, a), __builtin_bit_cast(s16x8, b), c, 0, 0, 0);
}
DI u32x4 lds128(unsigned a, int off = 0) { return *(const LAS u32x4*)((const LAS char*)(size_t)a + off); }
DI u32x4 tr_frag(unsigned a, int off, int second) {
  s16x4 lo = __builtin_amdgcn_ds_read_tr16_b64_v4i16((LAS s16x4*)((LAS char*)(size_t)a + off));
  s16x4 hi = __builtin_amdgcn_ds_read_tr16_b64_v4i16((LAS s16x4*)((LAS char*)(size_t)a + off + second));
  u32x2 x = __builtin_bit_cast(u32x2, lo), y = __builtin_bit_cast(u32x2, hi);
  return u32x4{x.x, x.y, y.x, y.y};
}
DI float dot2(unsigned a, unsigned b, float c) {
  return __builtin_amdgcn_fdot2_f32_bf16(__builtin_bit_cast(bf16x2_t, a), __builtin_bit_cast(bf16x2_t, b), c, false);
}
DI float sum8(u32x4 v, float c) {
  const unsigned one = 0x3f803f80u;
  c = dot2(v.x, one, c); c = dot2(v.y, one, c); c = dot2(v.z, one, c); return dot2(v.w, one, c);
}
DI float sq8(u32x4 v, float c) { c = dot2(v.x, v.x, c); c = dot2(v.y, v.y, c); c = dot2(v.z, v.z, c); return dot2(v.w, v.w, c); }
DI float vsum8(u32x4 v) {
  const float a = __uint_as_float(v.x << 16) + __uint_as_float(v.y << 16), b = __uint_as_float(v.x & 0xffff0000u) + __uint_as_float(v.y & 0xffff0000u);
  const float c = __uint_as_float(v.z << 16) + __uint_as_float(v.w << 16), d = __uint_as_float(v.z & 0xffff0000u) + __uint_as_float(v.w & 0xffff0000u);
  return (a + b) + (c + d);
}
DI void granule_store(char* ws, unsigned off, float v, unsigned tag) {
  __hip_atomic_store((GAS unsigned long long*)(ws + off), ((unsigned long long)tag << 32) | __float_as_uint(v), __ATOMIC_RELAXED, __HIP_MEMORY_SCOPE_AGENT);
}
DI unsigned long long granule_load(char* ws, unsigned off) {
  return __hip_atomic_load((GAS unsigned long long*)(ws + off), __ATOMIC_RELAXED, __HIP_MEMORY_SCOPE_AGENT);
}
template <int CTRL> DI float dpp_add(float v) {
  return v + __uint_as_float(__builtin_amdgcn_update_dpp(0u, __float_as_uint(v), CTRL, 0xf, 0xf, false));
}
DI float half_sum(float v) {
  v = dpp_add<0xB1>(v);
  v = dpp_add<0x4E>(v);
  v = dpp_add<0x141>(v);
  v = dpp_add<0x140>(v);
  auto r = __builtin_amdgcn_permlane16_swap(__float_as_uint(v), __float_as_uint(v), false, false);
  return __uint_as_float(r[0]) + __uint_as_float(r[1]);
}
DI float xhalf_add(float v) {
  auto r = __builtin_amdgcn_permlane32_swap(__float_as_uint(v), __float_as_uint(v), false, false);
  return __uint_as_float(r[0]) + __uint_as_float(r[1]);
}
DI float xhalf_max(float v) {
  auto r = __builtin_amdgcn_permlane32_swap(__float_as_uint(v), __float_as_uint(v), false, false);
  return fmaxf(__uint_as_float(r[0]), __uint_as_float(r[1]));
}
DI u32x4 pack_feat8(unsigned ax, unsigned ay, unsigned bx, unsigned by) {
  auto rx = __builtin_amdgcn_permlane32_swap(ax, bx, false, false);
  auto ry = __builtin_amdgcn_permlane32_swap(ay, by, false, false);
  return u32x4{rx[0], ry[0], rx[1], ry[1]};
}
DI f32x2 lo2(const f32x4& v) { return f32x2{v.x, v.y}; }
DI f32x2 hi2(const f32x4& v) { return f32x2{v.z, v.w}; }
template <int I> DI f32x2 pair(const f32x16& v) { return f32x2{v[2 * I], v[2 * I + 1]}; }
DI int fresh_lane() { int l; asm volatile("v_mbcnt_lo_u32_b32 %0, -1, 0\n\tv_mbcnt_hi_u32_b32 %0, -1, %0" : "=v"(l)); return l; }
typedef __amdgpu_buffer_rsrc_t rsrc_t;
DI void st128c(rsrc_t r, unsigned off, u32x4 v) { __builtin_amdgcn_raw_buffer_store_b128(v, r, off, 0, 16); }
DI void st64c(rsrc_t r, unsigned off, u32x2 v) { __builtin_amdgcn_raw_buffer_store_b64(v, r, off, 0, 16); }
DI u32x4 ld128c(rsrc_t r, unsigned off) { return __builtin_amdgcn_raw_buffer_load_b128(r, off, 0, 16); }
DI u32x2 ld64c(rsrc_t r, unsigned off) { return __builtin_amdgcn_raw_buffer_load_b64(r, off, 0, 16); }
DI unsigned flag_load(char* ws, int idx) {
  return __hip_atomic_load((GAS unsigned*)(ws + WS_FLAG) + idx * 16, __ATOMIC_RELAXED, __HIP_MEMORY_SCOPE_AGENT);
}
DI void flag_store(char* ws, int idx, unsigned v) {
  __hip_atomic_store((GAS unsigned*)(ws + WS_FLAG) + idx * 16, v, __ATOMIC_RELAXED, __HIP_MEMORY_SCOPE_AGENT);
}
DI unsigned read_epoch(const char* ws) {
  unsigned e;
  asm volatile("s_load_dword %0, %1, 0x0\n\ts_waitcnt lgkmcnt(0)" : "=s"(e) : "s"(ws + WS_FLAG + FL_EPOCH * 64) : "memory");
  return e;
}
DI void flags_wait(char* ws, int idx, unsigned tag) {
  for (int spin = 0; spin < (1 << 22); spin++) {
    const bool ok = idx < 0 || flag_load(ws, idx) == tag;
    if (__all(ok)) break;
    __builtin_amdgcn_s_sleep(1);
  }
  __builtin_amdgcn_fence(__ATOMIC_ACQUIRE, "wavefront");
}

#define STAMP_DECL
#define STAMP(i)
#define STAMP_DUMP(k)
#define STAMPC(i)

template <class Hook0, class Hook1, class Hook2>
DI void attention_task(rsrc_t rws, unsigned lds0, int w, int lane, int qi, int cgA, int cgX, int odd, u32x4 qf0, u32x4 qf1, Hook0&& mid,
                       Hook1&& per_tile, Hook2&& pre_store) {
  const int r32 = lane & 31, h = lane >> 5;
  const int q0 = 256 * qi + 32 * w;
  const unsigned ka = lds0 + L_K + h * 8192 + (32 * w + r32) * 16;
  const unsigned kx = lds0 + L_K + 16384 + (32 * w + r32) * 16;
  f32x16 sc[9];
  const f32x16 zero = {};
  const unsigned va = r32 < 20 ? lds0 + L_V + (2 * w) * 640 + h * 320 + r32 * 16 : lds0 + AUX_C + (r32 == 20 ? 0 : 16);
  const int vstep = r32 < 20 ? 640 : 0;
  const int tmin = qi == 0 ? 8 - w : 0;
  u32x4 vf[18];
  unsigned dm[8];
  float mxa[4] = {-INFINITY, -INFINITY, -INFINITY, -INFINITY};
  {
    u32x4 k0[9], k1[9];
#pragma unroll
    for (int n = 0; n < 9; n++) { k0[n] = lds128(ka, ((n + 8) % 9) * 512); k1[n] = lds128(kx, ((n + 8) % 9) * 512); }
    sfor<0, 11>([&](auto N) {
      constexpr int n = N, t = (n + 8) % 9, u = n - 3;
      SB();
      if constexpr (n < 9) { sc[t] = mfma32(k0[n], qf0, zero); sc[t] = mfma32(k1[n], qf1, sc[t]); }
      if constexpr (n >= 2 && n < 11) { vf[2 * (n - 2)] = lds128(va + (2 * (n - 2)) * vstep); vf[2 * (n - 2) + 1] = lds128(va + (2 * (n - 2) + 1) * vstep); }
      if constexpr (u >= 0) {
        if (u < tmin) {
#pragma unroll
          for (int i = 0; i < 16; i++) sc[u][i] = -INFINITY;
        }
        if constexpr (u == 0) {
#pragma unroll
          for (int i = 0; i < 16; i += 2) {
            const int kr = (i & 3) + 8 * (i >> 2) + 4 * h;
            const bool d0 = kr <= r32, d1 = kr + 1 <= r32;
            sc[0][i] = d0 ? sc[8][i] : sc[0][i];
            sc[0][i + 1] = d1 ? sc[8][i + 1] : sc[0][i + 1];
            dm[i >> 1] = d1 ? 0xffffffffu : (d0 ? 0x0000ffffu : 0u);
          }
        }
#pragma unroll
        for (int i = 0; i < 8; i++) mxa[i & 3] = max3(mxa[i & 3], sc[u][2 * i], sc[u][2 * i + 1]);
      }
    });
  }
  SB();
  float mx = max3(mxa[0], mxa[1], max3(mxa[2], mxa[3], mxa[3]));
  {
    auto r = __builtin_amdgcn_permlane32_swap(__float_as_uint(mx), __float_as_uint(mx), false, false);
    mx = max3(__uint_as_float(r[0]), __uint_as_float(r[1]), __uint_as_float(r[1]));
  }
  u32x4 nm;
  {
    const unsigned m0 = __float_as_uint(mx) & 0xffff0000u;
    const float r1 = mx - __uint_as_float(m0);
    const unsigned m1 = __float_as_uint(r1) & 0xffff0000u;
    const unsigned m2 = __float_as_uint(r1 - __uint_as_float(m1)) & 0xffff0000u;
    const unsigned a = ((m0 >> 16) | m1) ^ 0x80008000u, c = (m2 >> 16) ^ 0x8000u;
    nm = u32x4{h ? 0u : a, h ? 0u : c, 0u, 0u};
  }
  const u32x4 ones3 = {0x3f803f80u, 0x00003f80u, 0u, 0u};
  mid();
  SB();
  sc[1] = mfma32(ones3, nm, sc[1]);
  sc[2] = mfma32(ones3, nm, sc[2]);
  f32x16 o = {};
  sfor<1, 9>([&](auto T) {
    constexpr int tt = T, t = tt & 7;
    SB();
    if constexpr (tt + 2 < 9) sc[(tt + 2) & 7] = mfma32(ones3, nm, sc[(tt + 2) & 7]);
    per_tile(T);
    unsigned pk[8];
#pragma unroll
    for (int i = 0; i < 8; i++) pk[i] = pk2(__builtin_amdgcn_exp2f(sc[t][2 * i]), __builtin_amdgcn_exp2f(sc[t][2 * i + 1]));
    if constexpr (t != 0) {
      o = mfma32(vf[2 * t], u32x4{pk[0], pk[1], pk[2], pk[3]}, o);
      o = mfma32(vf[2 * t + 1], u32x4{pk[4], pk[5], pk[6], pk[7]}, o);
    } else {
      o = mfma32(vf[16], u32x4{pk[0] & dm[0], pk[1] & dm[1], pk[2] & dm[2], pk[3] & dm[3]}, o);
      o = mfma32(vf[17], u32x4{pk[4] & dm[4], pk[5] & dm[5], pk[6] & dm[6], pk[7] & dm[7]}, o);
      o = mfma32(vf[0], u32x4{pk[0] & ~dm[0], pk[1] & ~dm[1], pk[2] & ~dm[2], pk[3] & ~dm[3]}, o);
      o = mfma32(vf[1], u32x4{pk[4] & ~dm[4], pk[5] & ~dm[5], pk[6] & ~dm[6], pk[7] & ~dm[7]}, o);
    }
  });
  SB();
  pre_store();
  SB();
  auto lr = __builtin_amdgcn_permlane32_swap(__float_as_uint(o[8]), __float_as_uint(o[8]), false, false);
  const float inv = __builtin_amdgcn_rcpf(__uint_as_float(lr[1]));
  const unsigned ob = WS_ACT + 192u * S_ * 16;
  st128c(rws, ob + ((cgA + h) * S_ + q0 + r32) * 16,
         u32x4{pk2(o[0] * inv, o[1] * inv), pk2(o[2] * inv, o[3] * inv), pk2(o[4] * inv, o[5] * inv), pk2(o[6] * inv, o[7] * inv)});
  if (h == 0) st64c(rws, ob + (cgX * S_ + q0 + r32) * 16 + odd * 8, u32x2{pk2(o[8] * inv, o[9] * inv), pk2(o[10] * inv, o[11] * inv)});
}
DI void load_q(rsrc_t rws, int w, int lane, int qi, int cgA, int cgX, int odd, u32x4& qf0, u32x4& qf1) {
  const int r32 = lane & 31, h = lane >> 5, q0 = 256 * qi + 32 * w;
  qf0 = ld128c(rws, WS_QK + ((cgA + h) * S_ + q0 + r32) * 16);
  const u32x2 xq = ld64c(rws, WS_QK + (cgX * S_ + q0 + r32) * 16 + odd * 8);
  const unsigned x0 = h ? 0u : xq.x, x1 = h ? 0u : xq.y;
  qf1 = odd ? u32x4{0u, 0u, x0, x1} : u32x4{x0, x1, 0u, 0u};
}

__global__ __launch_bounds__(512, 2) void k_fused(const u16* __restrict__ x, const u16* __restrict__ wqkv, const u16* __restrict__ wo,
                                                  const u16* __restrict__ w1, const u16* __restrict__ b1, const u16* __restrict__ w2,
                                                  const u16* __restrict__ b2, char* __restrict__ ws, u16* __restrict__ out) {
  extern __shared__ __attribute__((aligned(16))) char smem[];
  LAS char* L = (LAS char*)smem;
  const unsigned lds0 = (unsigned)(size_t)L;
  const int tid = threadIdx.x, lane = tid & 63, w = __builtin_amdgcn_readfirstlane(tid >> 6);
  const int b = blockIdx.x, xcd = b & 7, bi = b >> 3;
  const int s = w & 3;
  const rsrc_t rws = __builtin_amdgcn_make_buffer_rsrc(ws, 0, WS_BYTES, 0x00020000);
  const int rb = (xcd & 1) * 8 + (bi >> 2), j = (xcd >> 1) * 4 + (bi & 3);
  const int row0 = rb * 128;
  const int kc = (5 * rb) >> 1, rot = ((kc >> 2) + 3 * j) % 10, kc0 = kc & 3, kc1 = kc0 + 2 + (rb & 1);
  const int hd = xcd * 4 + (bi & 3), qi = bi >> 2;
  const int odd = hd & 1, cgA = (hd >> 1) * 5 + (odd ? 3 : 0), cgX = (hd >> 1) * 5 + 2;
  const int rb3 = (xcd >> 1) * 8 + (bi >> 2), cgp = (xcd & 1) * 4 + (bi & 3);
  const int row3 = rb3 * 64, n0 = cgp * 80;
  STAMP_DECL;
  STAMP(0); STAMPC(38);
  const int r32 = lane & 31, h = lane >> 5;
  const int blk = (lane >> 4) & 1, q4 = (lane & 15) >> 2, p4 = lane & 3;
  const unsigned a0 = lds0 + L_A + (32 * s + r32) * 128 + ((h ^ ((r32 >> 1) & 7)) * 16);
  auto rdA = [&](int p, int t) -> u32x4 {
    if (p % 5 < 4) return lds128(a0 ^ (t * 32), (p % 5) * 16384);
    unsigned ad;
    asm volatile("v_add_u32 %0, 0x10000, %1" : "=v"(ad) : "v"(a0 ^ (t * 32)));
    return lds128(ad);
  };
  const unsigned bb = lds0 + L_B + (8 * h + q4) * 256 + (4 * q4 + 2 * blk + (p4 >> 1)) * 16 + 8 * (p4 & 1);
  const unsigned fa = lds0 + L_B + (8 * h + q4) * 192 + (2 * blk + (p4 >> 1)) * 16 + 8 * (p4 & 1);
  constexpr int FP[5] = {32768, 49152, 61440, 0, 16384};
  auto rdB = [&](int p, int t, int u) { return tr_frag(bb ^ (64 * u), (p & 3) * 16384 + t * 4096, 1024); };
  auto rdF = [&](int f, int t, int u) { return tr_frag(fa, FP[f % 5] + t * 3072 + u * 64, 768); };

  if (w < 4) {
    const int srow = row0 + 8 * j + 2 * s + h;
    u32x2 sx[5];
#pragma unroll
    for (int i = 0; i < 5; i++) sx[i] = *(const u32x2*)(x + (size_t)srow * D_ + 128 * i + 4 * r32);
    const unsigned tag = read_epoch(ws) + 1u;
    u32x4 af[20], at[4];
    auto getA = [&](auto P, auto T) -> u32x4& { if constexpr (P < 5) return af[4 * P + T]; else return at[T]; };
    auto row_stats = [&]() {
      float v[20];
#pragma unroll
      for (int i = 0; i < 5; i++) {
        v[4 * i] = bf2f(sx[i].x & 0xffffu); v[4 * i + 1] = bf2f(sx[i].x >> 16); v[4 * i + 2] = bf2f(sx[i].y & 0xffffu); v[4 * i + 3] = bf2f(sx[i].y >> 16);
      }
      float sm = ((v[0] + v[1]) + (v[2] + v[3])) + ((v[4] + v[5]) + (v[6] + v[7])) + ((v[8] + v[9]) + (v[10] + v[11]));
      sm += ((v[12] + v[13]) + (v[14] + v[15])) + ((v[16] + v[17]) + (v[18] + v[19]));
      sm = half_sum(sm);
      const float mu = sm * (1.0f / D_);
      float q4[4] = {0.f, 0.f, 0.f, 0.f};
#pragma unroll
      for (int i = 0; i < 20; i++) q4[i & 3] = __builtin_fmaf(v[i] - mu, v[i] - mu, q4[i & 3]);
      float sq = (q4[0] + q4[1]) + (q4[2] + q4[3]);
      sq = half_sum(sq);
      const float r = __builtin_amdgcn_rsqf(sq * (1.0f / D_) + 1e-5f);
      const int lc = fresh_lane();
      if ((lc & 31) < 2) granule_store(ws, WS_STAT + (row0 + 8 * j + 2 * s + (lc >> 5)) * 16 + (lc & 1) * 8, (lc & 1) ? r : mu * r, tag);
    };
    unsigned long long stg = 0;
    auto stoff = [&]() { const int lc = fresh_lane(); return WS_STAT + (row0 + 32 * s + (lc & 31)) * 16 + (lc >> 5) * 8; };
    auto conv = [&](int hc) {
      const int kb = hc / 5, cb = hc % 5, lc = fresh_lane();
      const unsigned dst = WS_WT + ((kb * 4 + 2 * (s >> 1) + (lc >> 5)) * D_ + cb * 128 + 64 * (s & 1) + (lc & 31)) * 16;
      const unsigned src = (bb ^ (128 * (s & 1))) + (s >> 1) * 4096;
      st128c(rws, dst, tr_frag(src, L_T, 1024));
      st128c(rws, dst + 512, tr_frag(src ^ 64, L_T, 1024));
    };
    f32x16 acc[4] = {};
    auto body = [&](auto MODE) {
      u32x4 bs[3][4];
      auto mm = [&](auto N, int u) {
        constexpr int n = N;
        if constexpr (MODE != 2) {
          const u32x4 a = getA(IC<n / 4>{}, IC<n % 4>{});
          if constexpr (MODE == 0) acc[u] = mfma32(bs[n % 3][u], a, acc[u]); else acc[u] = mfma32(a, bs[n % 3][u], acc[u]);
        }
      };
      auto rB = [&](int g, int t, int u) { if constexpr (MODE != 2) bs[(4 * g + t) % 3][u] = rdB(g, t, u); };
      sfor<0, 10>([&](auto G) {
        constexpr int g = G;
        lds_bar();
        if constexpr (g == 0 || g == 5) { STAMP(1 + g); }
#pragma unroll
        for (int u = 0; u < 4; u++) rB(g, 0, u);
        sfor<1, 4>([&](auto T) {
          constexpr int t = T;
          SB();
#pragma unroll
          for (int u = 0; u < 4; u++) {
            if constexpr (4 * g + t - 3 >= 0) mm(IC<(4 * g + t - 3 >= 0 ? 4 * g + t - 3 : 0)>{}, u);
            if (t == 1 && u == 3) getA(G, IC<0>{}) = rdA(g, 0);
            if (t == 1 && u == 3) getA(G, IC<1>{}) = rdA(g, 1);
            if (t == 2 && u == 3) getA(G, IC<2>{}) = rdA(g, 2);
            if (t == 3 && u == 0) getA(G, IC<3>{}) = rdA(g, 3);
            rB(g, t, u);
            SB();
          }
        });
#pragma unroll
        for (int u = 0; u < 4; u++) mm(IC<4 * g + 1>{}, u);
        if constexpr (g == 8) stg = granule_load(ws, stoff());
        if constexpr (g == 0) { SB(); row_stats(); }
        if constexpr (g == 3) conv(b);
        if constexpr (g == 7) { if (b < 84) conv(256 + b); }
      });
#pragma unroll
      for (int u = 0; u < 4; u++) mm(IC<38>{}, u);
#pragma unroll
      for (int u = 0; u < 4; u++) mm(IC<39>{}, u);
    };
    if (j < 10) body(IC<0>{}); else if (j < 15) body(IC<1>{}); else body(IC<2>{});
    STAMP(11);
    float mur, rs;
    {
      for (int spin = 0; spin < (1 << 22) && !__all((unsigned)(stg >> 32) == tag); spin++) stg = granule_load(ws, stoff());
      auto r = __builtin_amdgcn_permlane32_swap((unsigned)stg, (unsigned)stg, false, false);
      mur = __uint_as_float(r[0]); rs = __uint_as_float(r[1]);
    }
    const int l2 = fresh_lane(), r2 = l2 & 31, h2 = l2 >> 5;
    if (j >= 10 && h2 == 0) {
      ((LAS float*)(L + AUX_ST))[32 * s + r2] = mur;
      ((LAS float*)(L + AUX_ST))[128 + 32 * s + r2] = rs;
    }
    wait_bar<0>();
    if (j < 10) {
      const float sc = (j < 5) ? QSCALE : 1.0f;
      const f32x2 a2 = {rs * sc, rs * sc}, b2q = {-mur * sc, -mur * sc};
      const unsigned dst = WS_QK + ((16 * j + h2) * S_ + row0 + 32 * s + r2) * 16;
      const unsigned csa = lds0 + AUX_CS + 16 * h2;
      sfor<0, 2>([&](auto UU) {
        f32x4 cs[8];
#pragma unroll
        for (int i = 0; i < 8; i++) cs[i] = __builtin_bit_cast(f32x4, lds128(csa, (64 * UU + 8 * i) * 4));
        SB();
        sfor<0, 4>([&](auto C) {
          constexpr int u = 2 * UU + C / 2, k = C % 2, ci = 2 * C;
          const unsigned p0 = pk2v(fma2(pair<4 * k>(acc[u]), a2, lo2(cs[ci]) * b2q)), p1 = pk2v(fma2(pair<4 * k + 1>(acc[u]), a2, hi2(cs[ci]) * b2q));
          const unsigned p2 = pk2v(fma2(pair<4 * k + 2>(acc[u]), a2, lo2(cs[ci + 1]) * b2q)), p3 = pk2v(fma2(pair<4 * k + 3>(acc[u]), a2, hi2(cs[ci + 1]) * b2q));
          st128c(rws, dst + (4 * u + 2 * k) * (S_ * 16), pack_feat8(p0, p1, p2, p3));
        });
      });
    } else if (j < 15) {
      f32x4 murv[4], rsv[4];
      float csv[4];
#pragma unroll
      for (int g = 0; g < 4; g++) {
        murv[g] = *(const LAS f32x4*)(L + AUX_ST + (32 * s + 8 * g + 4 * h2) * 4);
        rsv[g] = *(const LAS f32x4*)(L + AUX_ST + 512 + (32 * s + 8 * g + 4 * h2) * 4);
        csv[g] = -*(const LAS float*)(L + AUX_CS + (32 * g + r2) * 4);
      }
      sfor<0, 4>([&](auto U) {
        constexpr int u = U;
        const f32x2 cs2 = {csv[u], csv[u]};
        const int colv = 128 * (j - 10) + 32 * u + r2;
        const int head = colv / 20, d = colv - head * 20;
        const int dd = (head & 1) ? (d < 4 ? d + 16 : d - 4) : d;
        const int slot = dd < 16 ? ((dd & 3) + 8 * ((dd >> 2) & 1) + 4 * (dd >> 3)) : dd;
        const unsigned dst = WS_VT + ((head * 128 + (row0 + 32 * s) / 16) * 2 + h2) * 320 + slot * 16;
        sfor<0, 2>([&](auto SP) {
          constexpr int sp = SP;
          const unsigned p0 = pk2v(fma2(pair<4 * sp>(acc[u]), lo2(rsv[2 * sp]), cs2 * lo2(murv[2 * sp])));
          const unsigned p1 = pk2v(fma2(pair<4 * sp + 1>(acc[u]), hi2(rsv[2 * sp]), cs2 * hi2(murv[2 * sp])));
          const unsigned p2 = pk2v(fma2(pair<4 * sp + 2>(acc[u]), lo2(rsv[2 * sp + 1]), cs2 * lo2(murv[2 * sp + 1])));
          const unsigned p3 = pk2v(fma2(pair<4 * sp + 3>(acc[u]), hi2(rsv[2 * sp + 1]), cs2 * hi2(murv[2 * sp + 1])));
          st128c(rws, dst + sp * 640, u32x4{p0, p1, p2, p3});
        });
      });
    }
    STAMP(12);
    f32x16 facc[3] = {};
    {
      u32x4 bs[3][3];
      auto pos = [](int f) { return (f + 5) % 10; };
      auto mm = [&](auto N, int u) {
        constexpr int n = N;
        facc[u] = mfma32(bs[n % 3][u], getA(IC<(n / 4 + 5) % 10>{}, IC<n % 4>{}), facc[u]);
      };
      sfor<0, 10>([&](auto Fs) {
        constexpr int f = Fs;
        if constexpr (f == 3) wait_bar<0>();
        else if constexpr (f > 0 && f < 9) lds_bar();
        if constexpr (f == 5) { STAMP(13 + f); }
#pragma unroll
        for (int u = 0; u < 3; u++) bs[(4 * f) % 3][u] = rdF(f, 0, u);
        sfor<1, 4>([&](auto T) {
          constexpr int t = T;
          SB();
#pragma unroll
          for (int u = 0; u < 3; u++) {
            if constexpr (4 * f + t - 3 >= 0) mm(IC<(4 * f + t - 3 >= 0 ? 4 * f + t - 3 : 0)>{}, u);
            if constexpr (f < 5) {
              if (t == 1 && u == 2) at[0] = rdA(pos(f), 0);
              if (t == 1 && u == 2) at[1] = rdA(pos(f), 1);
              if (t == 2 && u == 2) at[2] = rdA(pos(f), 2);
              if (t == 3 && u == 0) at[3] = rdA(pos(f), 3);
            }
            bs[(4 * f + t) % 3][u] = rdF(f, t, u);
            SB();
          }
        });
#pragma unroll
        for (int u = 0; u < 3; u++) mm(IC<4 * f + 1>{}, u);
      });
#pragma unroll
      for (int u = 0; u < 3; u++) mm(IC<38>{}, u);
#pragma unroll
      for (int u = 0; u < 3; u++) mm(IC<39>{}, u);
    }
    STAMP(23);
    {
      const int l3 = fresh_lane(), r3 = l3 & 31, h3 = l3 >> 5;
      while (true) {
        const u32x4 rdy = *(const volatile LAS u32x4*)(L + AUX_W);
        if (__builtin_amdgcn_readfirstlane(rdy.x & rdy.y & rdy.z) == 1u) break;
      }
      asm volatile("" ::: "memory");
      const unsigned dst = WS_ACT + ((12 * j + h3) * S_ + row0 + 32 * s + r3) * 16;
      const unsigned ca = lds0 + AUX_CF + 16 * h3;
      const f32x2 rs2 = {rs, rs}, nm2 = {-mur, -mur};
      auto act = [&](f32x2 a, f32x2 cs, f32x2 bv) {
        f32x2 t = fma2(a, rs2, fma2(cs, nm2, bv));
        t = f32x2{relu(t.x), relu(t.y)};
        return pk2v(t * t);
      };
      sfor<0, 3>([&](auto U) {
        constexpr int u = U;
        f32x4 cs[4], bv[4];
#pragma unroll
        for (int i = 0; i < 4; i++) {
          cs[i] = __builtin_bit_cast(f32x4, lds128(ca, (32 * u + 8 * i) * 4));
          bv[i] = __builtin_bit_cast(f32x4, lds128(ca, AUX_BF - AUX_CF + (32 * u + 8 * i) * 4));
        }
        SB();
        sfor<0, 2>([&](auto K) {
          constexpr int k = K;
          const unsigned p0 = act(pair<4 * k>(facc[u]), lo2(cs[2 * k]), lo2(bv[2 * k])), p1 = act(pair<4 * k + 1>(facc[u]), hi2(cs[2 * k]), hi2(bv[2 * k]));
          const unsigned p2 = act(pair<4 * k + 2>(facc[u]), lo2(cs[2 * k + 1]), lo2(bv[2 * k + 1]));
          const unsigned p3 = act(pair<4 * k + 3>(facc[u]), hi2(cs[2 * k + 1]), hi2(bv[2 * k + 1]));
          st128c(rws, dst + (4 * u + 2 * k) * (S_ * 16), pack_feat8(p0, p1, p2, p3));
        });
      });
    }
    STAMP(24);
    while (__builtin_amdgcn_readfirstlane(*(const volatile LAS unsigned*)(L + AUX_W + 16 + 4 * s)) != 1u) __builtin_amdgcn_s_sleep(1);
    asm volatile("" ::: "memory");
    const int lanem = fresh_lane();
    u32x4 qf0, qf1;
    load_q(rws, w, lanem, qi, cgA, cgX, odd, qf0, qf1);
    wait_bar<0>();
    STAMP(25);
    const int mh = s & 1, kh = s >> 1;
    const int rot3 = ((bi >> 2) + 3 * (bi & 3)) % 12, rot4 = ((bi >> 2) + (bi & 3)) % 5;
    auto kq = [&](int st) { int v; if (st < 12) { v = st + rot3; if (v >= 12) v -= 12; } else { v = st + rot4; if (v >= 17) v -= 5; } return v; };
    u32x4 ar[4][4];
    unsigned avo;
    auto ldA = [&](auto ST) {
      constexpr int st = ST;
      const unsigned so = kq(st) * (16 * S_ * 16);
#pragma unroll
      for (int i = 0; i < 4; i++) ar[st & 3][i] = __builtin_amdgcn_raw_buffer_load_b128(rws, (avo ^ ((i & 1) * 256)) + (i >> 1) * (8 * S_ * 16), so, 16);
    };
    unsigned f2 = tag;
    attention_task(rws, lds0, w, lanem, qi, cgA, cgX, odd, qf0, qf1,
                   [&]() {}, [&](auto T) { if constexpr (T == 5) { if (lanem < 16) f2 = flag_load(ws, FL_2 + (rb3 >> 1) * 16 + lanem); } },
                   [&]() {
                     if (!__all(f2 == tag)) flags_wait(ws, lanem < 16 ? FL_2 + (rb3 >> 1) * 16 + lanem : -1, tag);
                     avo = WS_ACT + ((4 * kh + (lanem >> 4)) * S_ + row3 + 32 * mh + 16 * kh + (lanem & 15)) * 16;
                     ldA(IC<0>{}); ldA(IC<1>{}); ldA(IC<2>{});
                   });
    STAMP(26);
    const int tok = row3 + 32 * mh + 16 * kh + (lanem & 15);
    u32x2 xv[5], bv[5];
    f32x4 accf[2][5] = {}, accs[2][5] = {};
    const unsigned wr = lds0 + kh * 5120 + lanem * 16;
    u32x4 fw[2][5];
    unsigned f3 = tag;
    auto mm1 = [&](auto ST, auto KK, auto X) {
      constexpr int st = ST, kk = KK, i = X / 2, r = X % 2;
      if constexpr (st < 12) accf[r][i] = mfma16(fw[kk][i], ar[st & 3][2 * kk + r], accf[r][i]);
      else accs[r][i] = mfma16(fw[kk][i], ar[st & 3][2 * kk + r], accs[r][i]);
    };
    auto ldA1 = [&](auto ST, auto I) {
      constexpr int st = ST, i = I;
      ar[st & 3][i] = __builtin_amdgcn_raw_buffer_load_b128(rws, (avo ^ ((i & 1) * 256)) + (i >> 1) * (8 * S_ * 16), kq(st) * (16 * S_ * 16), 16);
    };
    sfor<0, 17>([&](auto ST) {
      constexpr int st = ST;
      if constexpr (st == 3) wait_bar<12>(); else if constexpr (st >= 2) lds_bar();
      if constexpr (st == 2) { STAMP(27); }
      if constexpr (st == 12) { STAMP(28); }
      if constexpr (st == 9) { if (!__all(f3 == tag)) flags_wait(ws, lanem < 32 ? FL_3 + lanem * 8 + (rb3 >> 2) : -1, tag); }
      sfor<0, 10>([&](auto X) {
        constexpr int xx = X;
        if constexpr (xx < 5) fw[0][xx] = lds128(wr, W3[st % 7] + xx * 1024);
        if constexpr (st > 0) mm1(IC<(st > 0 ? st - 1 : 0)>{}, IC<1>{}, X);
        SB();
      });
      sfor<0, 10>([&](auto X) {
        constexpr int xx = X;
        if constexpr (xx < 5) fw[1][xx] = lds128(wr, W3[st % 7] + 10240 + xx * 1024);
        mm1(ST, IC<0>{}, X);
        if constexpr (xx >= 6 && st + 3 < 17) ldA1(IC<(st + 3 < 17 ? st + 3 : 0)>{}, IC<(xx >= 6 ? xx - 6 : 0)>{});
        SB();
      });
      if constexpr (st == 7) { if (lanem < 32) f3 = flag_load(ws, FL_3 + lanem * 8 + (rb3 >> 2)); }
      if constexpr (st == 14) {
#pragma unroll
        for (int i = 0; i < 5; i++) {
          xv[i] = *(const u32x2*)(x + (size_t)tok * D_ + n0 + 16 * i + 4 * (lanem >> 4));
          bv[i] = *(const u32x2*)(b2 + n0 + 16 * i + 4 * (lanem >> 4));
        }
      }
    });
    sfor<0, 10>([&](auto X) { mm1(IC<16>{}, IC<1>{}, X); });
    STAMP(29);
    f32x4 gf[5], gs[5];
    {
      LAS f32x4* ex = (LAS f32x4*)(L + W3[5]) + s * 640 + lanem;
#pragma unroll
      for (int i = 0; i < 5; i++) { ex[i * 64] = accf[1][i]; ex[320 + i * 64] = accs[1][i]; }
      lds_bar();
      const LAS f32x4* ey = (const LAS f32x4*)(L + W3[5]) + (s ^ 2) * 640 + lanem;
#pragma unroll
      for (int i = 0; i < 5; i++) { gf[i] = accf[0][i] + ey[i * 64]; gs[i] = accs[0][i] + ey[320 + i * 64]; }
    }
#pragma unroll
    for (int i = 0; i < 5; i++) {
      const float xf[4] = {bf2f(xv[i].x & 0xffffu), bf2f(xv[i].x >> 16), bf2f(xv[i].y & 0xffffu), bf2f(xv[i].y >> 16)};
      const float bf[4] = {bf2f(bv[i].x & 0xffffu), bf2f(bv[i].x >> 16), bf2f(bv[i].y & 0xffffu), bf2f(bv[i].y >> 16)};
      float r[4];
#pragma unroll
      for (int e = 0; e < 4; e++) {
        const float g = gf[i][e] + bf[e];
        const float sg = g * __builtin_amdgcn_rcpf(1.0f + __builtin_amdgcn_exp2f(-1.4426950408889634f * g));
        r[e] = __builtin_fmaf(gs[i][e], sg, xf[e]);
      }
      *(u32x2*)(out + (size_t)tok * D_ + n0 + 16 * i + 4 * (lanem >> 4)) = u32x2{pk2(r[0], r[1]), pk2(r[2], r[3])};
    }
    STAMP(30); STAMPC(39);
    if (w == 0) { STAMP_DUMP(0); }
  } else {
    const char* xbase = (const char*)(x + (size_t)row0 * D_);
    const char* fbase = (const char*)(w1 + 96 * j);
    const char* qbase = (const char*)(wqkv + 128 * (j < 15 ? j : 14));
    unsigned voffA[4], voffB[4], voffF[3];
#pragma unroll
    for (int e = 0; e < 4; e++) {
      voffA[e] = ((32 * s + 8 * e + (lane >> 3)) * D_ + (((lane & 7) ^ (4 * (e & 1) + (lane >> 4))) * 8)) * 2;
      voffB[e] = (16 * s + 4 * e + (lane >> 4)) * (NQKV * 2) + ((lane & 15) ^ ((lane >> 4) << 2)) * 16;
    }
    const unsigned voffT = (8 * s + (lane >> 4)) * (D_ * 2) + ((lane & 15) ^ ((lane >> 4) << 2)) * 16;
#pragma unroll
    for (int e = 0; e < 3; e++) { const int Lb = e * 1024 + lane * 16; voffF[e] = (16 * s + Lb / 192) * (F_ * 2) + Lb % 192; }
    const unsigned l4 = lds0 + s * 4096, l3k = lds0 + s * 3072;
    auto kst = [&](int p) { const int v = p + rot; return v >= 10 ? v - 10 : v; };
    auto dmaP = [&](auto P, auto E, const char* xs, const char* qs) {
      constexpr int p = P, e = E;
      if constexpr (e < 4) glds16<L_A + (p % 5) * 16384 + e * 1024>(voffA[e], xs, l4);
      else glds16<L_B + (p & 3) * 16384 + (e - 4) * 1024>(voffB[e - 4], qs, l4);
    };
    auto dmaF = [&](auto Fs) {
      constexpr int f = Fs;
      const char* src = fbase + (size_t)(kst((f + 5) % 10) * (64 * F_ * 2));
      glds16<L_B + FP[f % 5]>(voffF[0], src, l3k); glds16<L_B + FP[f % 5] + 1024>(voffF[1], src, l3k); glds16<L_B + FP[f % 5] + 2048>(voffF[2], src, l3k);
    };
    auto dmaT = [&](int hc) {
      const unsigned kb = (unsigned)hc / 5u, cb = hc - kb * 5;
      const size_t base = kb < 48 ? (size_t)w2 : (size_t)wo - (size_t)48 * 32 * D_ * 2;
      const char* src = (const char*)(base + (size_t)(kb * (32 * D_ * 2) + cb * 256));
      glds16<L_T>(voffT, src, lds0 + s * 2048);
      glds16<L_T + 1024>(voffT, src + 4 * D_ * 2, lds0 + s * 2048);
    };
    if (w == 4) { if (lane < 12) glds16<AUX_B1>(lane * 16, (const char*)(b1 + 96 * j), lds0); }
    sfor<0, 3>([&](auto P) {
      const char* xs = xbase + kst(P) * 128;
      const char* qs = qbase + (size_t)(kst(P) * (64 * NQKV * 2));
      sfor<0, 8>([&](auto E) { dmaP(P, E, xs, qs); });
    });
    if (w == 5 && lane < 16) ((LAS unsigned*)(L + AUX_C))[lane] = lane < 4 ? 0x3f803f80u : 0u;
    const unsigned tag = read_epoch(ws) + 1u;
    const unsigned cspub = WS_CSP + ((j * 16 + rb) * 224 + 32 * s + r32) * 8;
    const char* csgat = ws + WS_CSP + (size_t)(j * 16) * 224 * 8;
    const unsigned voffG = (8 * h * 224 + 32 * s + r32) * 8;
    u32x2 gr[8];
    auto gather_issue = [&](int off) {
#pragma unroll
      for (int i = 0; i < 8; i++)
        asm volatile("global_load_dwordx2 %0, %1, %2 sc1" : "=v"(gr[i]) : "v"(voffG), "s"(csgat + off + i * 224 * 8) : "memory");
    };
    auto gather_sum = [&](int off) -> float {
      for (int spin = 0; spin < (1 << 22); spin++) {
#pragma unroll
        for (int i = 0; i < 8; i++) asm volatile("" : "+v"(gr[i]));
        bool ok = true;
#pragma unroll
        for (int i = 0; i < 8; i++) ok &= gr[i].y == tag;
        if (__all(ok)) break;
        gather_issue(off);
        drain();
      }
      float a = 0.f;
#pragma unroll
      for (int i = 0; i < 8; i++) a += __uint_as_float(gr[i].x);
      return xhalf_add(a);
    };
    float cpart = 0.f;
    sfor<0, 10>([&](auto G) {
      constexpr int g = G;
      constexpr int NW[10] = {16, 18, 18, 17, 17, 18, 18, 24, 11, 9};
      wait_bar<NW[g]>();
      if constexpr (g == 1) { if (w == 7 && lane < 48) {
        const unsigned v = *(const LAS unsigned*)(L + AUX_B1 + lane * 4);
        *(LAS u32x2*)(L + AUX_BF + lane * 8) = u32x2{v << 16, v & 0xffff0000u};
      } }
      if constexpr (g == 0) dmaT(b);
      if constexpr (g == 4) dmaT(b < 84 ? 256 + b : b);
      if constexpr (g == 2) { cpart = xhalf_add(cpart); if (h == 0) granule_store(ws, cspub, cpart, tag); else asm volatile("" ::: "memory"); }
      if constexpr (g == 6) gather_issue(0);
      const char* xs = xbase + kst(g < 7 ? g + 3 : 0) * 128;
      const char* qs = qbase + (size_t)(kst(g < 7 ? g + 3 : 0) * (64 * NQKV * 2));
      if constexpr (g < 7) sfor<0, 8>([&](auto E) { dmaP(IC<g + 3>{}, E, xs, qs); });
      else if constexpr (g == 7) dmaF(IC<0>{});
      else if constexpr (g == 8) { dmaF(IC<1>{}); dmaF(IC<2>{}); }
      else dmaF(IC<3>{});
      if constexpr (g < 2) {
#pragma unroll
        for (int t = 0; t < 4; t++)
          if (4 * g + t >= kc0 && 4 * g + t < kc1) cpart += vsum8(tr_frag(bb ^ (64 * s), (g & 3) * 16384 + t * 4096, 1024));
      }
      if constexpr (g == 9) {
        const float cs = gather_sum(0);
        if (h == 0) ((LAS float*)(L + AUX_CS))[32 * s + r32] = cs;
      }
    });
    cpart = 0.f;
    const int uf = s < 3 ? s : 2;
    unsigned voffW[5];
#pragma unroll
    for (int i = 0; i < 5; i++) voffW[i] = ((lane >> 4) * D_ + (lane & 15) + 16 * i) * 16;
    const char* wbase = ws + WS_WT + (size_t)n0 * 16 + (size_t)s * 4 * D_ * 16;
    const int rot3 = ((bi >> 2) + 3 * (bi & 3)) % 12, rot4 = ((bi >> 2) + (bi & 3)) % 5;
    auto kq = [&](int st) { int v; if (st < 12) { v = st + rot3; if (v >= 12) v -= 12; } else { v = st + rot4; if (v >= 17) v -= 5; } return v; };
    const unsigned l5 = lds0 + s * 5120;
    auto issueW = [&](auto ST) {
      constexpr int st = ST;
      const char* ww = wbase + (size_t)(kq(st) * (16 * D_ * 16));
      sfor<0, 5>([&](auto I) { glds16c<W3[st % 7] + I * 1024>(voffW[I], ww, l5); });
    };
    int idxq;
    {
      const int ty = lane >> 3, rbi = (lane >> 1) & 3, hi = lane & 1;
      const int c0 = ty == 2 ? 20 * hd : 8 * (odd ? cgX : cgA), c1 = ty == 2 ? 20 * hd + 19 : 8 * (odd ? cgA + 1 : cgX) + 7;
      const int tile = 5 * ty + (hi ? c1 : c0) / 128, rr = 2 * qi - 2 + rbi;
      idxq = (lane < 24 && rr >= 0 && (ty > 0 || rbi >= 2)) ? FL_1 + rr * 16 + tile : FL_1 + rb * 16 + j;
    }
    unsigned pv[4];
    auto poll_issue = [&](unsigned& dst, int idx) {
      asm volatile("global_load_dword %0, %1, %2 sc1" : "=v"(dst) : "v"(idx * 64), "s"(ws + WS_FLAG) : "memory");
    };
    sfor<0, 9>([&](auto Fs) {
      constexpr int f = Fs;
      if constexpr (f <= 5) wait_bar<9>(); else if constexpr (f == 6) wait_bar<6>(); else if constexpr (f == 7) lds_bar(); else wait_bar<19>();
      if constexpr (f == 0) { if (w == 4 && lane == 0) flag_store(ws, FL_T + b, tag); }
      if constexpr (f == 3) { if (w == 4 && lane == 0) flag_store(ws, FL_1 + rb * 16 + j, tag); }
      if constexpr (f == 2) { cpart = xhalf_add(cpart); if (h == 0 && s < 3) granule_store(ws, cspub + 128 * 8, cpart, tag); }
      if constexpr (f == 4) {
#pragma unroll
        for (int i = 0; i < 4; i++) poll_issue(pv[i], FL_T + 64 * i + lane);
      }
      if constexpr (f < 6) dmaF(IC<(f < 6 ? f + 4 : 0)>{});
      if constexpr (f == 6) {
#pragma unroll
        for (int i = 0; i < 4; i++) asm volatile("" : "+v"(pv[i]));
        if (!__all(pv[0] == tag && pv[1] == tag && pv[2] == tag && pv[3] == tag)) {
#pragma unroll
          for (int i = 0; i < 4; i++) flags_wait(ws, FL_T + 64 * i + lane, tag);
        }
        issueW(IC<0>{});
      }
      if constexpr (f == 7) { gather_issue(128 * 8); poll_issue(pv[0], idxq); issueW(IC<1>{}); }
      if constexpr (f < 2) {
#pragma unroll
        for (int t = 0; t < 4; t++)
          if (4 * f + t >= kc0 && 4 * f + t < kc1) cpart += vsum8(tr_frag(fa + uf * 64, FP[f % 5] + t * 3072, 768));
      }
    });
    asm volatile("s_waitcnt vmcnt(5)" ::: "memory");
    if (s < 3) {
      const float cf = gather_sum(128 * 8);
      if (h == 0) ((LAS float*)(L + AUX_CF))[32 * s + r32] = cf;
      asm volatile("s_waitcnt lgkmcnt(0)" ::: "memory");
      if (lane == 0) *(volatile LAS unsigned*)(L + AUX_W + 4 * s) = 1u;
    }
    STAMP(1);
    asm volatile("" : "+v"(pv[0]));
    if (!__all(pv[0] == tag)) flags_wait(ws, idxq, tag);
    if (lane == 0) *(volatile LAS unsigned*)(L + AUX_W + 16 + 4 * s) = 1u;
    STAMP(2);
    {
      const int key0 = 256 * qi - 256;
      const char* kb = ws + WS_QK + (size_t)80 * S_ * 16;
      const u32x4 z = {0u, 0u, 0u, 0u};
#pragma unroll
      for (int i = 0; i < 2; i++) {
        const int pc = 2 * s + i, kk = key0 + 64 * pc;
        if (kk >= 0) {
          glds16c<L_K>(lane * 16, kb + ((size_t)cgA * S_ + kk) * 16, lds0 + pc * 1024);
          glds16c<L_K + 8192>(lane * 16, kb + ((size_t)(cgA + 1) * S_ + kk) * 16, lds0 + pc * 1024);
          glds16c<L_K + 16384>(lane * 16, kb + ((size_t)cgX * S_ + kk) * 16, lds0 + pc * 1024);
        } else {
#pragma unroll
          for (int c = 0; c < 3; c++) *(LAS u32x4*)(L + L_K + c * 8192 + pc * 1024 + lane * 16) = z;
        }
      }
      const char* vb = ws + WS_VT + ((long)hd * 128 + key0 / 16) * 640;
#pragma unroll
      for (int i = 0; i < 5; i++) {
        const int pc = 5 * s + i;
        if (qi > 0 || pc >= 10) glds16c<L_V>(lane * 16, vb + pc * 1024, lds0 + pc * 1024);
        else *(LAS u32x4*)(L + L_V + pc * 1024 + lane * 16) = z;
      }
    }
    u32x4 qf0, qf1;
    load_q(rws, w, lane, qi, cgA, cgX, odd, qf0, qf1);
    wait_bar<0>();
    STAMP(3);
    if (w == 4 && lane == 0) flag_store(ws, FL_2 + rb * 16 + j, tag);
    issueW(IC<2>{}); issueW(IC<3>{}); issueW(IC<4>{});
    attention_task(rws, lds0, w, lane, qi, cgA, cgX, odd, qf0, qf1, []() {}, [&](auto T) {}, []() {});
    STAMP(4);
    sfor<2, 17>([&](auto ST) {
      constexpr int st = ST;
      wait_bar<(st == 2) ? 12 : ((st == 3) ? 20 : ((st <= 11) ? 25 : 5 * (16 - st)))>();
      if constexpr (st == 2) { STAMP(6); issueW(IC<5>{}); issueW(IC<6>{}); issueW(IC<7>{}); }
      if constexpr (st == 3) { if (w == 4 && lane == 0) flag_store(ws, FL_3 + hd * 8 + qi, tag); }
      if constexpr (st + 6 < 17) issueW(IC<(st + 6 < 17 ? st + 6 : 0)>{});
    });
    lds_bar();
    if (b == 0 && w == 7) {
      for (int spin = 0; spin < (1 << 22); spin++) {
        const unsigned f0 = flag_load(ws, FL_2 + lane), f1 = flag_load(ws, FL_2 + 64 + lane), f2 = flag_load(ws, FL_2 + 128 + lane), f3 = flag_load(ws, FL_2 + 192 + lane);
        if (__all(f0 == tag && f1 == tag && f2 == tag && f3 == tag)) break;
      }
      if (lane == 0) flag_store(ws, FL_EPOCH, tag);
    }
    STAMP(7);
    if (w == 4) { STAMP_DUMP(1); }
  }
}

extern "C" void kernel_launch(void* const* d_in, const int* in_sizes, int n_in, void* d_out, int out_size, void* d_ws, size_t ws_size,
                              hipStream_t stream) {
  (void)hipFuncSetAttribute((const void*)k_fused, hipFuncAttributeMaxDynamicSharedMemorySize, LDS_BYTES);
  k_fused<<<256, 512, LDS_BYTES, stream>>>((const u16*)d_in[0], (const u16*)d_in[1], (const u16*)d_in[2], (const u16*)d_in[4],
                                           (const u16*)d_in[5], (const u16*)d_in[6], (const u16*)d_in[7], (char*)d_ws, (u16*)d_out);
}
```

```cpp
#define STAMPS
#define DIAGB 9
#define DIAG 11
#define DIAGT 11
#include <hip/hip_runtime.h>
#include <type_traits>

typedef unsigned short u16;
typedef unsigned u32x4 __attribute__((ext_vector_type(4)));
typedef unsigned u32x2 __attribute__((ext_vector_type(2)));
typedef float f32x4 __attribute__((ext_vector_type(4)));
typedef float f32x16 __attribute__((ext_vector_type(16)));
typedef short s16x8 __attribute__((ext_vector_type(8)));
typedef short s16x4 __attribute__((ext_vector_type(4)));
typedef __bf16 bf16x2_t __attribute__((ext_vector_type(2)));
#define LAS __attribute__((address_space(3)))
#define GAS __attribute__((address_space(1)))
#define DI __device__ __forceinline__
#define SB() __builtin_amdgcn_sched_barrier(0)
template <int N> using IC = std::integral_constant<int, N>;

constexpr int S_ = 2048, D_ = 640, F_ = 1536, NQKV = 1920;

constexpr unsigned WS_QK = 0;
constexpr unsigned WS_VT = 0x500000;
constexpr unsigned WS_ACT = 0x800000;
constexpr unsigned WS_WT = 0x1100000;
constexpr unsigned WS_FLAG = 0x1400000;
constexpr unsigned WS_STAT = 0x1418000;
constexpr unsigned WS_CSP = 0x1420000;
constexpr unsigned WS_BYTES = 0x14a0000;
constexpr int FL_EPOCH = 0, FL_1 = 16, FL_2 = FL_1 + 256, FL_3 = FL_2 + 256, FL_T = FL_3 + 256;

constexpr int L_B = 0;
constexpr int L_T = 65536;
constexpr int L_A = 78592;
constexpr int L_K = 115456;
constexpr int L_V = L_K + 24576;
constexpr int L_AUX = 160512;
constexpr int AUX_ST = L_AUX;
constexpr int AUX_CS = AUX_ST + 1024;
constexpr int AUX_CF = AUX_CS + 512;
constexpr int AUX_B1 = AUX_CF + 512;
constexpr int AUX_BF = AUX_B1 + 256;
constexpr int AUX_C = AUX_BF + 512;
constexpr int AUX_W = AUX_C + 32;
constexpr int LDS_BYTES = 163840;
constexpr int W3[7] = {73728, 94208, 0, 20480, 40960, 115456, 135936};
constexpr float QSCALE = 0.22360679774997896f * 1.4426950408889634f;

template <int I, int N, class Fn> DI void sfor(Fn&& f) {
  if constexpr (I < N) { f(IC<I>{}); sfor<I + 1, N>(f); }
}
template <int N> DI void wait_bar() { SB(); asm volatile("s_waitcnt vmcnt(%0) lgkmcnt(0)\n\ts_barrier" ::"n"(N) : "memory"); SB(); }
DI void lds_bar() { SB(); asm volatile("s_waitcnt lgkmcnt(0)\n\ts_barrier" ::: "memory"); SB(); }
DI void drain() { asm volatile("s_waitcnt vmcnt(0)" ::: "memory"); }
template <int IMM> DI void glds16(unsigned voff, const void* sbase, unsigned ldsbase) {
  asm volatile("s_add_i32 m0, %2, %3\n\ts_nop 0\n\tglobal_load_lds_dwordx4 %0, %1" ::"v"(voff), "s"(sbase), "s"(ldsbase), "n"(IMM) : "memory", "scc");
}
template <int IMM> DI void glds16c(unsigned voff, const void* sbase, unsigned ldsbase) {
  asm volatile("s_add_i32 m0, %2, %3\n\ts_nop 0\n\tglobal_load_lds_dwordx4 %0, %1 sc1" ::"v"(voff), "s"(sbase), "s"(ldsbase), "n"(IMM) : "memory", "scc");
}
typedef float f32x2 __attribute__((ext_vector_type(2)));
DI f32x2 fma2(f32x2 a, f32x2 b, f32x2 c) { return __builtin_elementwise_fma(a, b, c); }
DI unsigned pk2v(f32x2 v) { bf16x2_t p = {(__bf16)v.x, (__bf16)v.y}; return __builtin_bit_cast(unsigned, p); }
DI float max3(float a, float b, float c) { float r; asm("v_max3_f32 %0, %1, %2, %3" : "=v"(r) : "v"(a), "v"(b), "v"(c)); return r; }
DI float relu(float a) { float r; asm("v_max_f32 %0, 0, %1" : "=v"(r) : "v"(a)); return r; }
DI float bf2f(unsigned b) { return __uint_as_float(b << 16); }
DI unsigned pk2(float lo, float hi) { bf16x2_t p = {(__bf16)lo, (__bf16)hi}; return __builtin_bit_cast(unsigned, p); }
DI f32x16 mfma32(u32x4 a, u32x4 b, f32x16 c) {
  return __builtin_amdgcn_mfma_f32_32x32x16_bf16(__builtin_bit_cast(s16x8, a), __builtin_bit_cast(s16x8, b), c, 0, 0, 0);
}
DI f32x4 mfma16(u32x4 a, u32x4 b, f32x4 c) {
  return __builtin_amdgcn_mfma_f32_16x16x32_bf16(__builtin_bit_cast(s16x8, a), __builtin_bit_cast(s16x8, b), c, 0, 0, 0);
}
DI u32x4 lds128(unsigned a, int off = 0) { return *(const LAS u32x4*)((const LAS char*)(size_t)a + off); }
DI u32x4 tr_frag(unsigned a, int off, int second) {
  s16x4 lo = __builtin_amdgcn_ds_read_tr16_b64_v4i16((LAS s16x4*)((LAS char*)(size_t)a + off));
  s16x4 hi = __builtin_amdgcn_ds_read_tr16_b64_v4i16((LAS s16x4*)((LAS char*)(size_t)a + off + second));
  u32x2 x = __builtin_bit_cast(u32x2, lo), y = __builtin_bit_cast(u32x2, hi);
  return u32x4{x.x, x.y, y.x, y.y};
}
DI float dot2(unsigned a, unsigned b, float c) {
  return __builtin_amdgcn_fdot2_f32_bf16(__builtin_bit_cast(bf16x2_t, a), __builtin_bit_cast(bf16x2_t, b), c, false);
}
DI float sum8(u32x4 v, float c) {
  const unsigned one = 0x3f803f80u;
  c = dot2(v.x, one, c); c = dot2(v.y, one, c); c = dot2(v.z, one, c); return dot2(v.w, one, c);
}
DI float sq8(u32x4 v, float c) { c = dot2(v.x, v.x, c); c = dot2(v.y, v.y, c); c = dot2(v.z, v.z, c); return dot2(v.w, v.w, c); }
DI float vsum8(u32x4 v) {
  const float a = __uint_as_float(v.x << 16) + __uint_as_float(v.y << 16), b = __uint_as_float(v.x & 0xffff0000u) + __uint_as_float(v.y & 0xffff0000u);
  const float c = __uint_as_float(v.z << 16) + __uint_as_float(v.w << 16), d = __uint_as_float(v.z & 0xffff0000u) + __uint_as_float(v.w & 0xffff0000u);
  return (a + b) + (c + d);
}
DI void granule_store(char* ws, unsigned off, float v, unsigned tag) {
  __hip_atomic_store((GAS unsigned long long*)(ws + off), ((unsigned long long)tag << 32) | __float_as_uint(v), __ATOMIC_RELAXED, __HIP_MEMORY_SCOPE_AGENT);
}
DI unsigned long long granule_load(char* ws, unsigned off) {
  return __hip_atomic_load((GAS unsigned long long*)(ws + off), __ATOMIC_RELAXED, __HIP_MEMORY_SCOPE_AGENT);
}
template <int CTRL> DI float dpp_add(float v) {
  return v + __uint_as_float(__builtin_amdgcn_update_dpp(0u, __float_as_uint(v), CTRL, 0xf, 0xf, false));
}
DI float half_sum(float v) {
  v = dpp_add<0xB1>(v);
  v = dpp_add<0x4E>(v);
  v = dpp_add<0x141>(v);
  v = dpp_add<0x140>(v);
  auto r = __builtin_amdgcn_permlane16_swap(__float_as_uint(v), __float_as_uint(v), false, false);
  return __uint_as_float(r[0]) + __uint_as_float(r[1]);
}
DI float xhalf_add(float v) {
  auto r = __builtin_amdgcn_permlane32_swap(__float_as_uint(v), __float_as_uint(v), false, false);
  return __uint_as_float(r[0]) + __uint_as_float(r[1]);
}
DI float xhalf_max(float v) {
  auto r = __builtin_amdgcn_permlane32_swap(__float_as_uint(v), __float_as_uint(v), false, false);
  return fmaxf(__uint_as_float(r[0]), __uint_as_float(r[1]));
}
DI u32x4 pack_feat8(unsigned ax, unsigned ay, unsigned bx, unsigned by) {
  auto rx = __builtin_amdgcn_permlane32_swap(ax, bx, false, false);
  auto ry = __builtin_amdgcn_permlane32_swap(ay, by, false, false);
  return u32x4{rx[0], ry[0], rx[1], ry[1]};
}
DI f32x2 lo2(const f32x4& v) { return f32x2{v.x, v.y}; }
DI f32x2 hi2(const f32x4& v) { return f32x2{v.z, v.w}; }
template <int I> DI f32x2 pair(const f32x16& v) { return f32x2{v[2 * I], v[2 * I + 1]}; }
DI int fresh_lane() { int l; asm volatile("v_mbcnt_lo_u32_b32 %0, -1, 0\n\tv_mbcnt_hi_u32_b32 %0, -1, %0" : "=v"(l)); return l; }
typedef __amdgpu_buffer_rsrc_t rsrc_t;
DI void st128c(rsrc_t r, unsigned off, u32x4 v) { __builtin_amdgcn_raw_buffer_store_b128(v, r, off, 0, 16); }
DI void st64c(rsrc_t r, unsigned off, u32x2 v) { __builtin_amdgcn_raw_buffer_store_b64(v, r, off, 0, 16); }
DI u32x4 ld128c(rsrc_t r, unsigned off) { return __builtin_amdgcn_raw_buffer_load_b128(r, off, 0, 16); }
DI u32x2 ld64c(rsrc_t r, unsigned off) { return __builtin_amdgcn_raw_buffer_load_b64(r, off, 0, 16); }
DI unsigned flag_load(char* ws, int idx) {
  return __hip_atomic_load((GAS unsigned*)(ws + WS_FLAG) + idx * 16, __ATOMIC_RELAXED, __HIP_MEMORY_SCOPE_AGENT);
}
DI void flag_store(char* ws, int idx, unsigned v) {
  __hip_atomic_store((GAS unsigned*)(ws + WS_FLAG) + idx * 16, v, __ATOMIC_RELAXED, __HIP_MEMORY_SCOPE_AGENT);
}
DI unsigned read_epoch(const char* ws) {
  unsigned e;
  asm volatile("s_load_dword %0, %1, 0x0\n\ts_waitcnt lgkmcnt(0)" : "=s"(e) : "s"(ws + WS_FLAG + FL_EPOCH * 64) : "memory");
  return e;
}
DI void flags_wait(char* ws, int idx, unsigned tag) {
  for (int spin = 0; spin < (1 << 22); spin++) {
    const bool ok = idx < 0 || flag_load(ws, idx) == tag;
    if (__all(ok)) break;
    __builtin_amdgcn_s_sleep(1);
  }
  __builtin_amdgcn_fence(__ATOMIC_ACQUIRE, "wavefront");
}

#define STAMP_DECL unsigned stv_[40] = {}
#define STAMP(i) do{ unsigned long long t_; asm volatile("s_memrealtime %0\n\ts_waitcnt lgkmcnt(0)" : "=s"(t_) :: "memory"); stv_[i]=(unsigned)t_; }while(0)
#define STAMPC(i) do{ unsigned long long t_; asm volatile("s_memtime %0\n\ts_waitcnt lgkmcnt(0)" : "=s"(t_) :: "memory"); stv_[i]=(unsigned)t_; }while(0)
#define STAMP_DUMP(k)

template <class Hook0, class Hook1, class Hook2>
DI void attention_task(rsrc_t rws, unsigned lds0, int w, int lane, int qi, int cgA, int cgX, int odd, u32x4 qf0, u32x4 qf1, Hook0&& mid,
                       Hook1&& per_tile, Hook2&& pre_store) {
  const int r32 = lane & 31, h = lane >> 5;
  const int q0 = 256 * qi + 32 * w;
  const unsigned ka = lds0 + L_K + h * 8192 + (32 * w + r32) * 16;
  const unsigned kx = lds0 + L_K + 16384 + (32 * w + r32) * 16;
  f32x16 sc[9];
  const f32x16 zero = {};
  const unsigned va = r32 < 20 ? lds0 + L_V + (2 * w) * 640 + h * 320 + r32 * 16 : lds0 + AUX_C + (r32 == 20 ? 0 : 16);
  const int vstep = r32 < 20 ? 640 : 0;
  const int tmin = qi == 0 ? 8 - w : 0;
  u32x4 vf[18];
  unsigned dm[8];
  float mxa[4] = {-INFINITY, -INFINITY, -INFINITY, -INFINITY};
  {
    u32x4 k0[9], k1[9];
#pragma unroll
    for (int n = 0; n < 9; n++) { k0[n] = lds128(ka, ((n + 8) % 9) * 512); k1[n] = lds128(kx, ((n + 8) % 9) * 512); }
    sfor<0, 11>([&](auto N) {
      constexpr int n = N, t = (n + 8) % 9, u = n - 3;
      SB();
      if constexpr (n < 9) { sc[t] = mfma32(k0[n], qf0, zero); sc[t] = mfma32(k1[n], qf1, sc[t]); }
      if constexpr (n >= 2 && n < 11) { vf[2 * (n - 2)] = lds128(va + (2 * (n - 2)) * vstep); vf[2 * (n - 2) + 1] = lds128(va + (2 * (n - 2) + 1) * vstep); }
      if constexpr (u >= 0) {
        if (u < tmin) {
#pragma unroll
          for (int i = 0; i < 16; i++) sc[u][i] = -INFINITY;
        }
        if constexpr (u == 0) {
#pragma unroll
          for (int i = 0; i < 16; i += 2) {
            const int kr = (i & 3) + 8 * (i >> 2) + 4 * h;
            const bool d0 = kr <= r32, d1 = kr + 1 <= r32;
            sc[0][i] = d0 ? sc[8][i] : sc[0][i];
            sc[0][i + 1] = d1 ? sc[8][i + 1] : sc[0][i + 1];
            dm[i >> 1] = d1 ? 0xffffffffu : (d0 ? 0x0000ffffu : 0u);
          }
        }
#pragma unroll
        for (int i = 0; i < 8; i++) mxa[i & 3] = max3(mxa[i & 3], sc[u][2 * i], sc[u][2 * i + 1]);
      }
    });
  }
  SB();
  float mx = max3(mxa[0], mxa[1], max3(mxa[2], mxa[3], mxa[3]));
  {
    auto r = __builtin_amdgcn_permlane32_swap(__float_as_uint(mx), __float_as_uint(mx), false, false);
    mx = max3(__uint_as_float(r[0]), __uint_as_float(r[1]), __uint_as_float(r[1]));
  }
  u32x4 nm;
  {
    const unsigned m0 = __float_as_uint(mx) & 0xffff0000u;
    const float r1 = mx - __uint_as_float(m0);
    const unsigned m1 = __float_as_uint(r1) & 0xffff0000u;
    const unsigned m2 = __float_as_uint(r1 - __uint_as_float(m1)) & 0xffff0000u;
    const unsigned a = ((m0 >> 16) | m1) ^ 0x80008000u, c = (m2 >> 16) ^ 0x8000u;
    nm = u32x4{h ? 0u : a, h ? 0u : c, 0u, 0u};
  }
  const u32x4 ones3 = {0x3f803f80u, 0x00003f80u, 0u, 0u};
  mid();
  SB();
  sc[1] = mfma32(ones3, nm, sc[1]);
  sc[2] = mfma32(ones3, nm, sc[2]);
  f32x16 o = {};
  sfor<1, 9>([&](auto T) {
    constexpr int tt = T, t = tt & 7;
    SB();
    if constexpr (tt + 2 < 9) sc[(tt + 2) & 7] = mfma32(ones3, nm, sc[(tt + 2) & 7]);
    per_tile(T);
    unsigned pk[8];
#pragma unroll
    for (int i = 0; i < 8; i++) pk[i] = pk2(__builtin_amdgcn_exp2f(sc[t][2 * i]), __builtin_amdgcn_exp2f(sc[t][2 * i + 1]));
    if constexpr (t != 0) {
      o = mfma32(vf[2 * t], u32x4{pk[0], pk[1], pk[2], pk[3]}, o);
      o = mfma32(vf[2 * t + 1], u32x4{pk[4], pk[5], pk[6], pk[7]}, o);
    } else {
      o = mfma32(vf[16], u32x4{pk[0] & dm[0], pk[1] & dm[1], pk[2] & dm[2], pk[3] & dm[3]}, o);
      o = mfma32(vf[17], u32x4{pk[4] & dm[4], pk[5] & dm[5], pk[6] & dm[6], pk[7] & dm[7]}, o);
      o = mfma32(vf[0], u32x4{pk[0] & ~dm[0], pk[1] & ~dm[1], pk[2] & ~dm[2], pk[3] & ~dm[3]}, o);
      o = mfma32(vf[1], u32x4{pk[4] & ~dm[4], pk[5] & ~dm[5], pk[6] & ~dm[6], pk[7] & ~dm[7]}, o);
    }
  });
  SB();
  pre_store();
  SB();
  auto lr = __builtin_amdgcn_permlane32_swap(__float_as_uint(o[8]), __float_as_uint(o[8]), false, false);
  const float inv = __builtin_amdgcn_rcpf(__uint_as_float(lr[1]));
  const unsigned ob = WS_ACT + 192u * S_ * 16;
  st128c(rws, ob + ((cgA + h) * S_ + q0 + r32) * 16,
         u32x4{pk2(o[0] * inv, o[1] * inv), pk2(o[2] * inv, o[3] * inv), pk2(o[4] * inv, o[5] * inv), pk2(o[6] * inv, o[7] * inv)});
  if (h == 0) st64c(rws, ob + (cgX * S_ + q0 + r32) * 16 + odd * 8, u32x2{pk2(o[8] * inv, o[9] * inv), pk2(o[10] * inv, o[11] * inv)});
}
DI void load_q(rsrc_t rws, int w, int lane, int qi, int cgA, int cgX, int odd, u32x4& qf0, u32x4& qf1) {
  const int r32 = lane & 31, h = lane >> 5, q0 = 256 * qi + 32 * w;
  qf0 = ld128c(rws, WS_QK + ((cgA + h) * S_ + q0 + r32) * 16);
  const u32x2 xq = ld64c(rws, WS_QK + (cgX * S_ + q0 + r32) * 16 + odd * 8);
  const unsigned x0 = h ? 0u : xq.x, x1 = h ? 0u : xq.y;
  qf1 = odd ? u32x4{0u, 0u, x0, x1} : u32x4{x0, x1, 0u, 0u};
}

__global__ __launch_bounds__(512, 2) void k_fused(const u16* __restrict__ x, const u16* __restrict__ wqkv, const u16* __restrict__ wo,
                                                  const u16* __restrict__ w1, const u16* __restrict__ b1, const u16* __restrict__ w2,
                                                  const u16* __restrict__ b2, char* __restrict__ ws, u16* __restrict__ out) {
  extern __shared__ __attribute__((aligned(16))) char smem[];
  LAS char* L = (LAS char*)smem;
  const unsigned lds0 = (unsigned)(size_t)L;
  const int tid = threadIdx.x, lane = tid & 63, w = __builtin_amdgcn_readfirstlane(tid >> 6);
  const int b = blockIdx.x, xcd = b & 7, bi = b >> 3;
  const int s = w & 3;
  const rsrc_t rws = __builtin_amdgcn_make_buffer_rsrc(ws, 0, WS_BYTES, 0x00020000);
  const int rb = (xcd & 1) * 8 + (bi >> 2), j = (xcd >> 1) * 4 + (bi & 3);
  const int row0 = rb * 128;
  const int kc = (5 * rb) >> 1, rot = ((kc >> 2) + 3 * j) % 10, kc0 = kc & 3, kc1 = kc0 + 2 + (rb & 1);
  const int hd = xcd * 4 + (bi & 3), qi = bi >> 2;
  const int odd = hd & 1, cgA = (hd >> 1) * 5 + (odd ? 3 : 0), cgX = (hd >> 1) * 5 + 2;
  const int rb3 = (xcd >> 1) * 8 + (bi >> 2), cgp = (xcd & 1) * 4 + (bi & 3);
  const int row3 = rb3 * 64, n0 = cgp * 80;
  STAMP_DECL;
  STAMP(0); STAMPC(38);
  const int r32 = lane & 31, h = lane >> 5;
  const int blk = (lane >> 4) & 1, q4 = (lane & 15) >> 2, p4 = lane & 3;
  const unsigned a0 = lds0 + L_A + (32 * s + r32) * 128 + ((h ^ ((r32 >> 1) & 7)) * 16);
  auto rdA = [&](int p, int t) -> u32x4 {
    if (p % 5 < 4) return lds128(a0 ^ (t * 32), (p % 5) * 16384);
    unsigned ad;
    asm volatile("v_add_u32 %0, 0x10000, %1" : "=v"(ad) : "v"(a0 ^ (t * 32)));
    return lds128(ad);
  };
  const unsigned bb = lds0 + L_B + (8 * h + q4) * 256 + (4 * q4 + 2 * blk + (p4 >> 1)) * 16 + 8 * (p4 & 1);
  const unsigned fa = lds0 + L_B + (8 * h + q4) * 192 + (2 * blk + (p4 >> 1)) * 16 + 8 * (p4 & 1);
  constexpr int FP[5] = {32768, 49152, 61440, 0, 16384};
  auto rdB = [&](int p, int t, int u) { return tr_frag(bb ^ (64 * u), (p & 3) * 16384 + t * 4096, 1024); };
  auto rdF = [&](int f, int t, int u) { return tr_frag(fa, FP[f % 5] + t * 3072 + u * 64, 768); };

  if (w < 4) {
    const int srow = row0 + 8 * j + 2 * s + h;
    u32x2 sx[5];
#pragma unroll
    for (int i = 0; i < 5; i++) sx[i] = *(const u32x2*)(x + (size_t)srow * D_ + 128 * i + 4 * r32);
    const unsigned tag = read_epoch(ws) + 1u;
    u32x4 af[20], at[4];
    auto getA = [&](auto P, auto T) -> u32x4& { if constexpr (P < 5) return af[4 * P + T]; else return at[T]; };
    auto row_stats = [&]() {
      float v[20];
#pragma unroll
      for (int i = 0; i < 5; i++) {
        v[4 * i] = bf2f(sx[i].x & 0xffffu); v[4 * i + 1] = bf2f(sx[i].x >> 16); v[4 * i + 2] = bf2f(sx[i].y & 0xffffu); v[4 * i + 3] = bf2f(sx[i].y >> 16);
      }
      float sm = ((v[0] + v[1]) + (v[2] + v[3])) + ((v[4] + v[5]) + (v[6] + v[7])) + ((v[8] + v[9]) + (v[10] + v[11]));
      sm += ((v[12] + v[13]) + (v[14] + v[15])) + ((v[16] + v[17]) + (v[18] + v[19]));
      sm = half_sum(sm);
      const float mu = sm * (1.0f / D_);
      float q4[4] = {0.f, 0.f, 0.f, 0.f};
#pragma unroll
      for (int i = 0; i < 20; i++) q4[i & 3] = __builtin_fmaf(v[i] - mu, v[i] - mu, q4[i & 3]);
      float sq = (q4[0] + q4[1]) + (q4[2] + q4[3]);
      sq = half_sum(sq);
      const float r = __builtin_amdgcn_rsqf(sq * (1.0f / D_) + 1e-5f);
      const int lc = fresh_lane();
      if ((lc & 31) < 2) granule_store(ws, WS_STAT + (row0 + 8 * j + 2 * s + (lc >> 5)) * 16 + (lc & 1) * 8, (lc & 1) ? r : mu * r, tag);
    };
    unsigned long long stg = 0;
    auto stoff = [&]() { const int lc = fresh_lane(); return WS_STAT + (row0 + 32 * s + (lc & 31)) * 16 + (lc >> 5) * 8; };
    auto conv = [&](int hc) {
      const int kb = hc / 5, cb = hc % 5, lc = fresh_lane();
      const unsigned dst = WS_WT + ((kb * 4 + 2 * (s >> 1) + (lc >> 5)) * D_ + cb * 128 + 64 * (s & 1) + (lc & 31)) * 16;
      const unsigned src = (bb ^ (128 * (s & 1))) + (s >> 1) * 4096;
      st128c(rws, dst, tr_frag(src, L_T, 1024));
      st128c(rws, dst + 512, tr_frag(src ^ 64, L_T, 1024));
    };
    f32x16 acc[4] = {};
    auto body = [&](auto MODE) {
      u32x4 bs[3][4];
      auto mm = [&](auto N, int u) {
        constexpr int n = N;
        if constexpr (MODE != 2) {
          const u32x4 a = getA(IC<n / 4>{}, IC<n % 4>{});
          if constexpr (MODE == 0) acc[u] = mfma32(bs[n % 3][u], a, acc[u]); else acc[u] = mfma32(a, bs[n % 3][u], acc[u]);
        }
      };
      auto rB = [&](int g, int t, int u) { if constexpr (MODE != 2) bs[(4 * g + t) % 3][u] = rdB(g, t, u); };
      sfor<0, 10>([&](auto G) {
        constexpr int g = G;
        lds_bar();
        if constexpr (g == 0 || g == 5) { STAMP(1 + g); }
#pragma unroll
        for (int u = 0; u < 4; u++) rB(g, 0, u);
        sfor<1, 4>([&](auto T) {
          constexpr int t = T;
          SB();
#pragma unroll
          for (int u = 0; u < 4; u++) {
            if constexpr (4 * g + t - 3 >= 0) mm(IC<(4 * g + t - 3 >= 0 ? 4 * g + t - 3 : 0)>{}, u);
            if (t == 1 && u == 3) getA(G, IC<0>{}) = rdA(g, 0);
            if (t == 1 && u == 3) getA(G, IC<1>{}) = rdA(g, 1);
            if (t == 2 && u == 3) getA(G, IC<2>{}) = rdA(g, 2);
            if (t == 3 && u == 0) getA(G, IC<3>{}) = rdA(g, 3);
            rB(g, t, u);
            SB();
          }
        });
#pragma unroll
        for (int u = 0; u < 4; u++) mm(IC<4 * g + 1>{}, u);
        if constexpr (g == 8) stg = granule_load(ws, stoff());
        if constexpr (g == 0) { SB(); row_stats(); }
        if constexpr (g == 3) conv(b);
        if constexpr (g == 7) { if (b < 84) conv(256 + b); }
      });
#pragma unroll
      for (int u = 0; u < 4; u++) mm(IC<38>{}, u);
#pragma unroll
      for (int u = 0; u < 4; u++) mm(IC<39>{}, u);
    };
    if (j < 10) body(IC<0>{}); else if (j < 15) body(IC<1>{}); else body(IC<2>{});
    STAMP(11);
    float mur, rs;
    {
      for (int spin = 0; spin < (1 << 22) && !__all((unsigned)(stg >> 32) == tag); spin++) stg = granule_load(ws, stoff());
      auto r = __builtin_amdgcn_permlane32_swap((unsigned)stg, (unsigned)stg, false, false);
      mur = __uint_as_float(r[0]); rs = __uint_as_float(r[1]);
    }
    const int l2 = fresh_lane(), r2 = l2 & 31, h2 = l2 >> 5;
    if (j >= 10 && h2 == 0) {
      ((LAS float*)(L + AUX_ST))[32 * s + r2] = mur;
      ((LAS float*)(L + AUX_ST))[128 + 32 * s + r2] = rs;
    }
    wait_bar<0>();
    if (j < 10) {
      const float sc = (j < 5) ? QSCALE : 1.0f;
      const f32x2 a2 = {rs * sc, rs * sc}, b2q = {-mur * sc, -mur * sc};
      const unsigned dst = WS_QK + ((16 * j + h2) * S_ + row0 + 32 * s + r2) * 16;
      const unsigned csa = lds0 + AUX_CS + 16 * h2;
      sfor<0, 2>([&](auto UU) {
        f32x4 cs[8];
#pragma unroll
        for (int i = 0; i < 8; i++) cs[i] = __builtin_bit_cast(f32x4, lds128(csa, (64 * UU + 8 * i) * 4));
        SB();
        sfor<0, 4>([&](auto C) {
          constexpr int u = 2 * UU + C / 2, k = C % 2, ci = 2 * C;
          const unsigned p0 = pk2v(fma2(pair<4 * k>(acc[u]), a2, lo2(cs[ci]) * b2q)), p1 = pk2v(fma2(pair<4 * k + 1>(acc[u]), a2, hi2(cs[ci]) * b2q));
          const unsigned p2 = pk2v(fma2(pair<4 * k + 2>(acc[u]), a2, lo2(cs[ci + 1]) * b2q)), p3 = pk2v(fma2(pair<4 * k + 3>(acc[u]), a2, hi2(cs[ci + 1]) * b2q));
          st128c(rws, dst + (4 * u + 2 * k) * (S_ * 16), pack_feat8(p0, p1, p2, p3));
        });
      });
    } else if (j < 15) {
      f32x4 murv[4], rsv[4];
      float csv[4];
#pragma unroll
      for (int g = 0; g < 4; g++) {
        murv[g] = *(const LAS f32x4*)(L + AUX_ST + (32 * s + 8 * g + 4 * h2) * 4);
        rsv[g] = *(const LAS f32x4*)(L + AUX_ST + 512 + (32 * s + 8 * g + 4 * h2) * 4);
        csv[g] = -*(const LAS float*)(L + AUX_CS + (32 * g + r2) * 4);
      }
      sfor<0, 4>([&](auto U) {
        constexpr int u = U;
        const f32x2 cs2 = {csv[u], csv[u]};
        const int colv = 128 * (j - 10) + 32 * u + r2;
        const int head = colv / 20, d = colv - head * 20;
        const int dd = (head & 1) ? (d < 4 ? d + 16 : d - 4) : d;
        const int slot = dd < 16 ? ((dd & 3) + 8 * ((dd >> 2) & 1) + 4 * (dd >> 3)) : dd;
        const unsigned dst = WS_VT + ((head * 128 + (row0 + 32 * s) / 16) * 2 + h2) * 320 + slot * 16;
        sfor<0, 2>([&](auto SP) {
          constexpr int sp = SP;
          const unsigned p0 = pk2v(fma2(pair<4 * sp>(acc[u]), lo2(rsv[2 * sp]), cs2 * lo2(murv[2 * sp])));
          const unsigned p1 = pk2v(fma2(pair<4 * sp + 1>(acc[u]), hi2(rsv[2 * sp]), cs2 * hi2(murv[2 * sp])));
          const unsigned p2 = pk2v(fma2(pair<4 * sp + 2>(acc[u]), lo2(rsv[2 * sp + 1]), cs2 * lo2(murv[2 * sp + 1])));
          const unsigned p3 = pk2v(fma2(pair<4 * sp + 3>(acc[u]), hi2(rsv[2 * sp + 1]), cs2 * hi2(murv[2 * sp + 1])));
          st128c(rws, dst + sp * 640, u32x4{p0, p1, p2, p3});
        });
      });
    }
    STAMP(12);
    f32x16 facc[3] = {};
    {
      u32x4 bs[3][3];
      auto pos = [](int f) { return (f + 5) % 10; };
      auto mm = [&](auto N, int u) {
        constexpr int n = N;
        facc[u] = mfma32(bs[n % 3][u], getA(IC<(n / 4 + 5) % 10>{}, IC<n % 4>{}), facc[u]);
      };
      sfor<0, 10>([&](auto Fs) {
        constexpr int f = Fs;
        if constexpr (f == 3) wait_bar<0>();
        else if constexpr (f > 0 && f < 9) lds_bar();
        if constexpr (f == 5) { STAMP(13 + f); }
#pragma unroll
        for (int u = 0; u < 3; u++) bs[(4 * f) % 3][u] = rdF(f, 0, u);
        sfor<1, 4>([&](auto T) {
          constexpr int t = T;
          SB();
#pragma unroll
          for (int u = 0; u < 3; u++) {
            if constexpr (4 * f + t - 3 >= 0) mm(IC<(4 * f + t - 3 >= 0 ? 4 * f + t - 3 : 0)>{}, u);
            if constexpr (f < 5) {
              if (t == 1 && u == 2) at[0] = rdA(pos(f), 0);
              if (t == 1 && u == 2) at[1] = rdA(pos(f), 1);
              if (t == 2 && u == 2) at[2] = rdA(pos(f), 2);
              if (t == 3 && u == 0) at[3] = rdA(pos(f), 3);
            }
            bs[(4 * f + t) % 3][u] = rdF(f, t, u);
            SB();
          }
        });
#pragma unroll
        for (int u = 0; u < 3; u++) mm(IC<4 * f + 1>{}, u);
      });
#pragma unroll
      for (int u = 0; u < 3; u++) mm(IC<38>{}, u);
#pragma unroll
      for (int u = 0; u < 3; u++) mm(IC<39>{}, u);
    }
    STAMP(23);
    {
      const int l3 = fresh_lane(), r3 = l3 & 31, h3 = l3 >> 5;
      while (true) {
        const u32x4 rdy = *(const volatile LAS u32x4*)(L + AUX_W);
        if (__builtin_amdgcn_readfirstlane(rdy.x & rdy.y & rdy.z) == 1u) break;
      }
      asm volatile("" ::: "memory");
      const unsigned dst = WS_ACT + ((12 * j + h3) * S_ + row0 + 32 * s + r3) * 16;
      const unsigned ca = lds0 + AUX_CF + 16 * h3;
      const f32x2 rs2 = {rs, rs}, nm2 = {-mur, -mur};
      auto act = [&](f32x2 a, f32x2 cs, f32x2 bv) {
        f32x2 t = fma2(a, rs2, fma2(cs, nm2, bv));
        t = f32x2{relu(t.x), relu(t.y)};
        return pk2v(t * t);
      };
      sfor<0, 3>([&](auto U) {
        constexpr int u = U;
        f32x4 cs[4], bv[4];
#pragma unroll
        for (int i = 0; i < 4; i++) {
          cs[i] = __builtin_bit_cast(f32x4, lds128(ca, (32 * u + 8 * i) * 4));
          bv[i] = __builtin_bit_cast(f32x4, lds128(ca, AUX_BF - AUX_CF + (32 * u + 8 * i) * 4));
        }
        SB();
        sfor<0, 2>([&](auto K) {
          constexpr int k = K;
          const unsigned p0 = act(pair<4 * k>(facc[u]), lo2(cs[2 * k]), lo2(bv[2 * k])), p1 = act(pair<4 * k + 1>(facc[u]), hi2(cs[2 * k]), hi2(bv[2 * k]));
          const unsigned p2 = act(pair<4 * k + 2>(facc[u]), lo2(cs[2 * k + 1]), lo2(bv[2 * k + 1]));
          const unsigned p3 = act(pair<4 * k + 3>(facc[u]), hi2(cs[2 * k + 1]), hi2(bv[2 * k + 1]));
          st128c(rws, dst + (4 * u + 2 * k) * (S_ * 16), pack_feat8(p0, p1, p2, p3));
        });
      });
    }
    STAMP(24);
    while (__builtin_amdgcn_readfirstlane(*(const volatile LAS unsigned*)(L + AUX_W + 16 + 4 * s)) != 1u) __builtin_amdgcn_s_sleep(1);
    asm volatile("" ::: "memory");
    const int lanem = fresh_lane();
    u32x4 qf0, qf1;
    load_q(rws, w, lanem, qi, cgA, cgX, odd, qf0, qf1);
    wait_bar<0>();
    STAMP(25);
    const int mh = s & 1, kh = s >> 1;
    const int rot3 = ((bi >> 2) + 3 * (bi & 3)) % 12, rot4 = ((bi >> 2) + (bi & 3)) % 5;
    auto kq = [&](int st) { int v; if (st < 12) { v = st + rot3; if (v >= 12) v -= 12; } else { v = st + rot4; if (v >= 17) v -= 5; } return v; };
    u32x4 ar[4][4];
    unsigned avo;
    auto ldA = [&](auto ST) {
      constexpr int st = ST;
      const unsigned so = kq(st) * (16 * S_ * 16);
#pragma unroll
      for (int i = 0; i < 4; i++) ar[st & 3][i] = __builtin_amdgcn_raw_buffer_load_b128(rws, (avo ^ ((i & 1) * 256)) + (i >> 1) * (8 * S_ * 16), so, 16);
    };
    unsigned f2 = tag;
    attention_task(rws, lds0, w, lanem, qi, cgA, cgX, odd, qf0, qf1,
                   [&]() {}, [&](auto T) { if constexpr (T == 5) { if (lanem < 16) f2 = flag_load(ws, FL_2 + (rb3 >> 1) * 16 + lanem); } },
                   [&]() {
                     if (!__all(f2 == tag)) flags_wait(ws, lanem < 16 ? FL_2 + (rb3 >> 1) * 16 + lanem : -1, tag);
                     avo = WS_ACT + ((4 * kh + (lanem >> 4)) * S_ + row3 + 32 * mh + 16 * kh + (lanem & 15)) * 16;
                     ldA(IC<0>{}); ldA(IC<1>{}); ldA(IC<2>{});
                   });
    STAMP(26);
    const int tok = row3 + 32 * mh + 16 * kh + (lanem & 15);
    u32x2 xv[5], bv[5];
    f32x4 accf[2][5] = {}, accs[2][5] = {};
    const unsigned wr = lds0 + kh * 5120 + lanem * 16;
    u32x4 fw[2][5];
    unsigned f3 = tag;
    auto mm1 = [&](auto ST, auto KK, auto X) {
      constexpr int st = ST, kk = KK, i = X / 2, r = X % 2;
      if constexpr (st < 12) accf[r][i] = mfma16(fw[kk][i], ar[st & 3][2 * kk + r], accf[r][i]);
      else accs[r][i] = mfma16(fw[kk][i], ar[st & 3][2 * kk + r], accs[r][i]);
    };
    auto ldA1 = [&](auto ST, auto I) {
      constexpr int st = ST, i = I;
      ar[st & 3][i] = __builtin_amdgcn_raw_buffer_load_b128(rws, (avo ^ ((i & 1) * 256)) + (i >> 1) * (8 * S_ * 16), kq(st) * (16 * S_ * 16), 16);
    };
    sfor<0, 17>([&](auto ST) {
      constexpr int st = ST;
      if constexpr (st == 3) wait_bar<12>(); else if constexpr (st >= 2) lds_bar();
      if constexpr (st == 2) { STAMP(27); }
      if constexpr (st == 12) { STAMP(28); }
      if constexpr (st == 9) { if (!__all(f3 == tag)) flags_wait(ws, lanem < 32 ? FL_3 + lanem * 8 + (rb3 >> 2) : -1, tag); }
      sfor<0, 10>([&](auto X) {
        constexpr int xx = X;
        if constexpr (xx < 5) fw[0][xx] = lds128(wr, W3[st % 7] + xx * 1024);
        if constexpr (st > 0) mm1(IC<(st > 0 ? st - 1 : 0)>{}, IC<1>{}, X);
        SB();
      });
      sfor<0, 10>([&](auto X) {
        constexpr int xx = X;
        if constexpr (xx < 5) fw[1][xx] = lds128(wr, W3[st % 7] + 10240 + xx * 1024);
        mm1(ST, IC<0>{}, X);
        if constexpr (xx >= 6 && st + 3 < 17) ldA1(IC<(st + 3 < 17 ? st + 3 : 0)>{}, IC<(xx >= 6 ? xx - 6 : 0)>{});
        SB();
      });
      if constexpr (st == 7) { if (lanem < 32) f3 = flag_load(ws, FL_3 + lanem * 8 + (rb3 >> 2)); }
      if constexpr (st == 14) {
#pragma unroll
        for (int i = 0; i < 5; i++) {
          xv[i] = *(const u32x2*)(x + (size_t)tok * D_ + n0 + 16 * i + 4 * (lanem >> 4));
          bv[i] = *(const u32x2*)(b2 + n0 + 16 * i + 4 * (lanem >> 4));
        }
      }
    });
    sfor<0, 10>([&](auto X) { mm1(IC<16>{}, IC<1>{}, X); });
    STAMP(29);
    f32x4 gf[5], gs[5];
    {
      LAS f32x4* ex = (LAS f32x4*)(L + W3[5]) + s * 640 + lanem;
#pragma unroll
      for (int i = 0; i < 5; i++) { ex[i * 64] = accf[1][i]; ex[320 + i * 64] = accs[1][i]; }
      lds_bar();
      const LAS f32x4* ey = (const LAS f32x4*)(L + W3[5]) + (s ^ 2) * 640 + lanem;
#pragma unroll
      for (int i = 0; i < 5; i++) { gf[i] = accf[0][i] + ey[i * 64]; gs[i] = accs[0][i] + ey[320 + i * 64]; }
    }
#pragma unroll
    for (int i = 0; i < 5; i++) {
      const float xf[4] = {bf2f(xv[i].x & 0xffffu), bf2f(xv[i].x >> 16), bf2f(xv[i].y & 0xffffu), bf2f(xv[i].y >> 16)};
      const float bf[4] = {bf2f(bv[i].x & 0xffffu), bf2f(bv[i].x >> 16), bf2f(bv[i].y & 0xffffu), bf2f(bv[i].y >> 16)};
      float r[4];
#pragma unroll
      for (int e = 0; e < 4; e++) {
        const float g = gf[i][e] + bf[e];
        const float sg = g * __builtin_amdgcn_rcpf(1.0f + __builtin_amdgcn_exp2f(-1.4426950408889634f * g));
        r[e] = __builtin_fmaf(gs[i][e], sg, xf[e]);
        if (b == DIAGB && fabsf(r[e]) < 0.1f && (stv_[DIAG] - stv_[0]) > DIAGT * 100u) r[e] += 2.0f + (float)(stv_[DIAG] - stv_[0]) * (0.01f / 20.0f);
      }
      *(u32x2*)(out + (size_t)tok * D_ + n0 + 16 * i + 4 * (lanem >> 4)) = u32x2{pk2(r[0], r[1]), pk2(r[2], r[3])};
    }
    STAMP(30); STAMPC(39);
    if (w == 0) { STAMP_DUMP(0); }
  } else {
    const char* xbase = (const char*)(x + (size_t)row0 * D_);
    const char* fbase = (const char*)(w1 + 96 * j);
    const char* qbase = (const char*)(wqkv + 128 * (j < 15 ? j : 14));
    unsigned voffA[4], voffB[4], voffF[3];
#pragma unroll
    for (int e = 0; e < 4; e++) {
      voffA[e] = ((32 * s + 8 * e + (lane >> 3)) * D_ + (((lane & 7) ^ (4 * (e & 1) + (lane >> 4))) * 8)) * 2;
      voffB[e] = (16 * s + 4 * e + (lane >> 4)) * (NQKV * 2) + ((lane & 15) ^ ((lane >> 4) << 2)) * 16;
    }
    const unsigned voffT = (8 * s + (lane >> 4)) * (D_ * 2) + ((lane & 15) ^ ((lane >> 4) << 2)) * 16;
#pragma unroll
    for (int e = 0; e < 3; e++) { const int Lb = e * 1024 + lane * 16; voffF[e] = (16 * s + Lb / 192) * (F_ * 2) + Lb % 192; }
    const unsigned l4 = lds0 + s * 4096, l3k = lds0 + s * 3072;
    auto kst = [&](int p) { const int v = p + rot; return v >= 10 ? v - 10 : v; };
    auto dmaP = [&](auto P, auto E, const char* xs, const char* qs) {
      constexpr int p = P, e = E;
      if constexpr (e < 4) glds16<L_A + (p % 5) * 16384 + e * 1024>(voffA[e], xs, l4);
      else glds16<L_B + (p & 3) * 16384 + (e - 4) * 1024>(voffB[e - 4], qs, l4);
    };
    auto dmaF = [&](auto Fs) {
      constexpr int f = Fs;
      const char* src = fbase + (size_t)(kst((f + 5) % 10) * (64 * F_ * 2));
      glds16<L_B + FP[f % 5]>(voffF[0], src, l3k); glds16<L_B + FP[f % 5] + 1024>(voffF[1], src, l3k); glds16<L_B + FP[f % 5] + 2048>(voffF[2], src, l3k);
    };
    auto dmaT = [&](int hc) {
      const unsigned kb = (unsigned)hc / 5u, cb = hc - kb * 5;
      const size_t base = kb < 48 ? (size_t)w2 : (size_t)wo - (size_t)48 * 32 * D_ * 2;
      const char* src = (const char*)(base + (size_t)(kb * (32 * D_ * 2) + cb * 256));
      glds16<L_T>(voffT, src, lds0 + s * 2048);
      glds16<L_T + 1024>(voffT, src + 4 * D_ * 2, lds0 + s * 2048);
    };
    if (w == 4) { if (lane < 12) glds16<AUX_B1>(lane * 16, (const char*)(b1 + 96 * j), lds0); }
    sfor<0, 3>([&](auto P) {
      const char* xs = xbase + kst(P) * 128;
      const char* qs = qbase + (size_t)(kst(P) * (64 * NQKV * 2));
      sfor<0, 8>([&](auto E) { dmaP(P, E, xs, qs); });
    });
    if (w == 5 && lane < 16) ((LAS unsigned*)(L + AUX_C))[lane] = lane < 4 ? 0x3f803f80u : 0u;
    const unsigned tag = read_epoch(ws) + 1u;
    const unsigned cspub = WS_CSP + ((j * 16 + rb) * 224 + 32 * s + r32) * 8;
    const char* csgat = ws + WS_CSP + (size_t)(j * 16) * 224 * 8;
    const unsigned voffG = (8 * h * 224 + 32 * s + r32) * 8;
    u32x2 gr[8];
    auto gather_issue = [&](int off) {
#pragma unroll
      for (int i = 0; i < 8; i++)
        asm volatile("global_load_dwordx2 %0, %1, %2 sc1" : "=v"(gr[i]) : "v"(voffG), "s"(csgat + off + i * 224 * 8) : "memory");
    };
    auto gather_sum = [&](int off) -> float {
      for (int spin = 0; spin < (1 << 22); spin++) {
#pragma unroll
        for (int i = 0; i < 8; i++) asm volatile("" : "+v"(gr[i]));
        bool ok = true;
#pragma unroll
        for (int i = 0; i < 8; i++) ok &= gr[i].y == tag;
        if (__all(ok)) break;
        gather_issue(off);
        drain();
      }
      float a = 0.f;
#pragma unroll
      for (int i = 0; i < 8; i++) a += __uint_as_float(gr[i].x);
      return xhalf_add(a);
    };
    float cpart = 0.f;
    sfor<0, 10>([&](auto G) {
      constexpr int g = G;
      constexpr int NW[10] = {16, 18, 18, 17, 17, 18, 18, 24, 11, 9};
      wait_bar<NW[g]>();
      if constexpr (g == 1) { if (w == 7 && lane < 48) {
        const unsigned v = *(const LAS unsigned*)(L + AUX_B1 + lane * 4);
        *(LAS u32x2*)(L + AUX_BF + lane * 8) = u32x2{v << 16, v & 0xffff0000u};
      } }
      if constexpr (g == 0) dmaT(b);
      if constexpr (g == 4) dmaT(b < 84 ? 256 + b : b);
      if constexpr (g == 2) { cpart = xhalf_add(cpart); if (h == 0) granule_store(ws, cspub, cpart, tag); else asm volatile("" ::: "memory"); }
      if constexpr (g == 6) gather_issue(0);
      const char* xs = xbase + kst(g < 7 ? g + 3 : 0) * 128;
      const char* qs = qbase + (size_t)(kst(g < 7 ? g + 3 : 0) * (64 * NQKV * 2));
      if constexpr (g < 7) sfor<0, 8>([&](auto E) { dmaP(IC<g + 3>{}, E, xs, qs); });
      else if constexpr (g == 7) dmaF(IC<0>{});
      else if constexpr (g == 8) { dmaF(IC<1>{}); dmaF(IC<2>{}); }
      else dmaF(IC<3>{});
      if constexpr (g < 2) {
#pragma unroll
        for (int t = 0; t < 4; t++)
          if (4 * g + t >= kc0 && 4 * g + t < kc1) cpart += vsum8(tr_frag(bb ^ (64 * s), (g & 3) * 16384 + t * 4096, 1024));
      }
      if constexpr (g == 9) {
        const float cs = gather_sum(0);
        if (h == 0) ((LAS float*)(L + AUX_CS))[32 * s + r32] = cs;
      }
    });
    cpart = 0.f;
    const int uf = s < 3 ? s : 2;
    unsigned voffW[5];
#pragma unroll
    for (int i = 0; i < 5; i++) voffW[i] = ((lane >> 4) * D_ + (lane & 15) + 16 * i) * 16;
    const char* wbase = ws + WS_WT + (size_t)n0 * 16 + (size_t)s * 4 * D_ * 16;
    const int rot3 = ((bi >> 2) + 3 * (bi & 3)) % 12, rot4 = ((bi >> 2) + (bi & 3)) % 5;
    auto kq = [&](int st) { int v; if (st < 12) { v = st + rot3; if (v >= 12) v -= 12; } else { v = st + rot4; if (v >= 17) v -= 5; } return v; };
    const unsigned l5 = lds0 + s * 5120;
    auto issueW = [&](auto ST) {
      constexpr int st = ST;
      const char* ww = wbase + (size_t)(kq(st) * (16 * D_ * 16));
      sfor<0, 5>([&](auto I) { glds16c<W3[st % 7] + I * 1024>(voffW[I], ww, l5); });
    };
    int idxq;
    {
      const int ty = lane >> 3, rbi = (lane >> 1) & 3, hi = lane & 1;
      const int c0 = ty == 2 ? 20 * hd : 8 * (odd ? cgX : cgA), c1 = ty == 2 ? 20 * hd + 19 : 8 * (odd ? cgA + 1 : cgX) + 7;
      const int tile = 5 * ty + (hi ? c1 : c0) / 128, rr = 2 * qi - 2 + rbi;
      idxq = (lane < 24 && rr >= 0 && (ty > 0 || rbi >= 2)) ? FL_1 + rr * 16 + tile : FL_1 + rb * 16 + j;
    }
    unsigned pv[4];
    auto poll_issue = [&](unsigned& dst, int idx) {
      asm volatile("global_load_dword %0, %1, %2 sc1" : "=v"(dst) : "v"(idx * 64), "s"(ws + WS_FLAG) : "memory");
    };
    sfor<0, 9>([&](auto Fs) {
      constexpr int f = Fs;
      if constexpr (f <= 5) wait_bar<9>(); else if constexpr (f == 6) wait_bar<6>(); else if constexpr (f == 7) lds_bar(); else wait_bar<19>();
      if constexpr (f == 0) { if (w == 4 && lane == 0) flag_store(ws, FL_T + b, tag); }
      if constexpr (f == 3) { if (w == 4 && lane == 0) flag_store(ws, FL_1 + rb * 16 + j, tag); }
      if constexpr (f == 2) { cpart = xhalf_add(cpart); if (h == 0 && s < 3) granule_store(ws, cspub + 128 * 8, cpart, tag); }
      if constexpr (f == 4) {
#pragma unroll
        for (int i = 0; i < 4; i++) poll_issue(pv[i], FL_T + 64 * i + lane);
      }
      if constexpr (f < 6) dmaF(IC<(f < 6 ? f + 4 : 0)>{});
      if constexpr (f == 6) {
#pragma unroll
        for (int i = 0; i < 4; i++) asm volatile("" : "+v"(pv[i]));
        if (!__all(pv[0] == tag && pv[1] == tag && pv[2] == tag && pv[3] == tag)) {
#pragma unroll
          for (int i = 0; i < 4; i++) flags_wait(ws, FL_T + 64 * i + lane, tag);
        }
        issueW(IC<0>{});
      }
      if constexpr (f == 7) { gather_issue(128 * 8); poll_issue(pv[0], idxq); issueW(IC<1>{}); }
      if constexpr (f < 2) {
#pragma unroll
        for (int t = 0; t < 4; t++)
          if (4 * f + t >= kc0 && 4 * f + t < kc1) cpart += vsum8(tr_frag(fa + uf * 64, FP[f % 5] + t * 3072, 768));
      }
    });
    asm volatile("s_waitcnt vmcnt(5)" ::: "memory");
    if (s < 3) {
      const float cf = gather_sum(128 * 8);
      if (h == 0) ((LAS float*)(L + AUX_CF))[32 * s + r32] = cf;
      asm volatile("s_waitcnt lgkmcnt(0)" ::: "memory");
      if (lane == 0) *(volatile LAS unsigned*)(L + AUX_W + 4 * s) = 1u;
    }
    STAMP(1);
    asm volatile("" : "+v"(pv[0]));
    if (!__all(pv[0] == tag)) flags_wait(ws, idxq, tag);
    if (lane == 0) *(volatile LAS unsigned*)(L + AUX_W + 16 + 4 * s) = 1u;
    STAMP(2);
    {
      const int key0 = 256 * qi - 256;
      const char* kb = ws + WS_QK + (size_t)80 * S_ * 16;
      const u32x4 z = {0u, 0u, 0u, 0u};
#pragma unroll
      for (int i = 0; i < 2; i++) {
        const int pc = 2 * s + i, kk = key0 + 64 * pc;
        if (kk >= 0) {
          glds16c<L_K>(lane * 16, kb + ((size_t)cgA * S_ + kk) * 16, lds0 + pc * 1024);
          glds16c<L_K + 8192>(lane * 16, kb + ((size_t)(cgA + 1) * S_ + kk) * 16, lds0 + pc * 1024);
          glds16c<L_K + 16384>(lane * 16, kb + ((size_t)cgX * S_ + kk) * 16, lds0 + pc * 1024);
        } else {
#pragma unroll
          for (int c = 0; c < 3; c++) *(LAS u32x4*)(L + L_K + c * 8192 + pc * 1024 + lane * 16) = z;
        }
      }
      const char* vb = ws + WS_VT + ((long)hd * 128 + key0 / 16) * 640;
#pragma unroll
      for (int i = 0; i < 5; i++) {
        const int pc = 5 * s + i;
        if (qi > 0 || pc >= 10) glds16c<L_V>(lane * 16, vb + pc * 1024, lds0 + pc * 1024);
        else *(LAS u32x4*)(L + L_V + pc * 1024 + lane * 16) = z;
      }
    }
    u32x4 qf0, qf1;
    load_q(rws, w, lane, qi, cgA, cgX, odd, qf0, qf1);
    wait_bar<0>();
    STAMP(3);
    if (w == 4 && lane == 0) flag_store(ws, FL_2 + rb * 16 + j, tag);
    issueW(IC<2>{}); issueW(IC<3>{}); issueW(IC<4>{});
    attention_task(rws, lds0, w, lane, qi, cgA, cgX, odd, qf0, qf1, []() {}, [&](auto T) {}, []() {});
    STAMP(4);
    sfor<2, 17>([&](auto ST) {
      constexpr int st = ST;
      wait_bar<(st == 2) ? 12 : ((st == 3) ? 20 : ((st <= 11) ? 25 : 5 * (16 - st)))>();
      if constexpr (st == 2) { STAMP(6); issueW(IC<5>{}); issueW(IC<6>{}); issueW(IC<7>{}); }
      if constexpr (st == 3) { if (w == 4 && lane == 0) flag_store(ws, FL_3 + hd * 8 + qi, tag); }
      if constexpr (st + 6 < 17) issueW(IC<(st + 6 < 17 ? st + 6 : 0)>{});
    });
    lds_bar();
    if (b == 0 && w == 7) {
      for (int spin = 0; spin < (1 << 22); spin++) {
        const unsigned f0 = flag_load(ws, FL_2 + lane), f1 = flag_load(ws, FL_2 + 64 + lane), f2 = flag_load(ws, FL_2 + 128 + lane), f3 = flag_load(ws, FL_2 + 192 + lane);
        if (__all(f0 == tag && f1 == tag && f2 == tag && f3 == tag)) break;
      }
      if (lane == 0) flag_store(ws, FL_EPOCH, tag);
    }
    STAMP(7);
    if (w == 4) { STAMP_DUMP(1); }
  }
}

extern "C" void kernel_launch(void* const* d_in, const int* in_sizes, int n_in, void* d_out, int out_size, void* d_ws, size_t ws_size,
                              hipStream_t stream) {
  (void)hipFuncSetAttribute((const void*)k_fused, hipFuncAttributeMaxDynamicSharedMemorySize, LDS_BYTES);
  k_fused<<<256, 512, LDS_BYTES, stream>>>((const u16*)d_in[0], (const u16*)d_in[1], (const u16*)d_in[2], (const u16*)d_in[4],
                                           (const u16*)d_in[5], (const u16*)d_in[6], (const u16*)d_in[7], (char*)d_ws, (u16*)d_out);
}
```

```cpp
#include <hip/hip_runtime.h>
#include <type_traits>

typedef unsigned short u16;
typedef unsigned u32x4 __attribute__((ext_vector_type(4)));
typedef unsigned u32x2 __attribute__((ext_vector_type(2)));
typedef float f32x4 __attribute__((ext_vector_type(4)));
typedef float f32x16 __attribute__((ext_vector_type(16)));
typedef short s16x8 __attribute__((ext_vector_type(8)));
typedef short s16x4 __attribute__((ext_vector_type(4)));
typedef __bf16 bf16x2_t __attribute__((ext_vector_type(2)));
#define LAS __attribute__((address_space(3)))
#define GAS __attribute__((address_space(1)))
#define DI __device__ __forceinline__
#define SB() __builtin_amdgcn_sched_barrier(0)
template <int N> using IC = std::integral_constant<int, N>;

constexpr int S_ = 2048, D_ = 640, F_ = 1536, NQKV = 1920;

constexpr unsigned WS_QK = 0;
constexpr unsigned WS_VT = 0x500000;
constexpr unsigned WS_ACT = 0x800000;
constexpr unsigned WS_WT = 0x1100000;
constexpr unsigned WS_FLAG = 0x1400000;
constexpr unsigned WS_STAT = 0x1418000;
constexpr unsigned WS_CSP = 0x1420000;
constexpr unsigned WS_BYTES = 0x14a0000;
constexpr int FL_EPOCH = 0, FL_1 = 16, FL_2 = FL_1 + 256, FL_3 = FL_2 + 256, FL_T = FL_3 + 256;

constexpr int L_B = 0;
constexpr int L_T = 65536;
constexpr int L_A = 78592;
constexpr int L_K = 115456;
constexpr int L_V = L_K + 24576;
constexpr int L_AUX = 160512;
constexpr int AUX_ST = L_AUX;
constexpr int AUX_CS = AUX_ST + 1024;
constexpr int AUX_CF = AUX_CS + 512;
constexpr int AUX_B1 = AUX_CF + 512;
constexpr int AUX_BF = AUX_B1 + 256;
constexpr int AUX_C = AUX_BF + 512;
constexpr int AUX_W = AUX_C + 32;
constexpr int LDS_BYTES = 163840;
constexpr int W3[7] = {73728, 94208, 0, 20480, 40960, 115456, 135936};
constexpr float QSCALE = 0.22360679774997896f * 1.4426950408889634f;

template <int I, int N, class Fn> DI void sfor(Fn&& f) {
  if constexpr (I < N) { f(IC<I>{}); sfor<I + 1, N>(f); }
}
template <int N> DI void wait_bar() { SB(); asm volatile("s_waitcnt vmcnt(%0) lgkmcnt(0)\n\ts_barrier" ::"n"(N) : "memory"); SB(); }
DI void lds_bar() { SB(); asm volatile("s_waitcnt lgkmcnt(0)\n\ts_barrier" ::: "memory"); SB(); }
DI void drain() { asm volatile("s_waitcnt vmcnt(0)" ::: "memory"); }
template <int IMM> DI void glds16(unsigned voff, const void* sbase, unsigned ldsbase) {
  asm volatile("s_add_i32 m0, %2, %3\n\ts_nop 0\n\tglobal_load_lds_dwordx4 %0, %1" ::"v"(voff), "s"(sbase), "s"(ldsbase), "n"(IMM) : "memory", "scc");
}
template <int IMM> DI void glds16c(unsigned voff, const void* sbase, unsigned ldsbase) {
  asm volatile("s_add_i32 m0, %2, %3\n\ts_nop 0\n\tglobal_load_lds_dwordx4 %0, %1 sc1" ::"v"(voff), "s"(sbase), "s"(ldsbase), "n"(IMM) : "memory", "scc");
}
template <int IMM> DI void glds16x4(const unsigned* v, const void* sbase, unsigned ldsbase) {
  asm volatile("s_add_i32 m0, %5, %6\n\ts_nop 0\n\tglobal_load_lds_dwordx4 %0, %4\n\tglobal_load_lds_dwordx4 %1, %4 offset:1024\n\t"
               "global_load_lds_dwordx4 %2, %4 offset:2048\n\tglobal_load_lds_dwordx4 %3, %4 offset:3072"
               ::"v"(v[0]), "v"(v[1]), "v"(v[2]), "v"(v[3]), "s"(sbase), "s"(ldsbase), "n"(IMM) : "memory", "scc");
}
template <int IMM> DI void glds16x3(const unsigned* v, const void* sbase, unsigned ldsbase) {
  asm volatile("s_add_i32 m0, %4, %5\n\ts_nop 0\n\tglobal_load_lds_dwordx4 %0, %3\n\tglobal_load_lds_dwordx4 %1, %3 offset:1024\n\t"
               "global_load_lds_dwordx4 %2, %3 offset:2048"
               ::"v"(v[0]), "v"(v[1]), "v"(v[2]), "s"(sbase), "s"(ldsbase), "n"(IMM) : "memory", "scc");
}
template <int IMM> DI void glds16x5c(const unsigned* v, const void* sbase, unsigned ldsbase) {
  asm volatile("s_add_i32 m0, %6, %7\n\ts_nop 0\n\tglobal_load_lds_dwordx4 %0, %5 offset:-2048 sc1\n\tglobal_load_lds_dwordx4 %1, %5 offset:-1024 sc1\n\t"
               "global_load_lds_dwordx4 %2, %5 sc1\n\tglobal_load_lds_dwordx4 %3, %5 offset:1024 sc1\n\tglobal_load_lds_dwordx4 %4, %5 offset:2048 sc1"
               ::"v"(v[0]), "v"(v[1]), "v"(v[2]), "v"(v[3]), "v"(v[4]), "s"(sbase), "s"(ldsbase), "n"(IMM + 2048) : "memory", "scc");
}
typedef float f32x2 __attribute__((ext_vector_type(2)));
DI f32x2 fma2(f32x2 a, f32x2 b, f32x2 c) { return __builtin_elementwise_fma(a, b, c); }
DI unsigned pk2v(f32x2 v) { bf16x2_t p = {(__bf16)v.x, (__bf16)v.y}; return __builtin_bit_cast(unsigned, p); }
DI float max3(float a, float b, float c) { float r; asm("v_max3_f32 %0, %1, %2, %3" : "=v"(r) : "v"(a), "v"(b), "v"(c)); return r; }
DI float relu(float a) { float r; asm("v_max_f32 %0, 0, %1" : "=v"(r) : "v"(a)); return r; }
DI float bf2f(unsigned b) { return __uint_as_float(b << 16); }
DI unsigned pk2(float lo, float hi) { bf16x2_t p = {(__bf16)lo, (__bf16)hi}; return __builtin_bit_cast(unsigned, p); }
DI f32x16 mfma32(u32x4 a, u32x4 b, f32x16 c) {
  return __builtin_amdgcn_mfma_f32_32x32x16_bf16(__builtin_bit_cast(s16x8, a), __builtin_bit_cast(s16x8, b), c, 0, 0, 0);
}
DI f32x4 mfma16(u32x4 a, u32x4 b, f32x4 c) {
  return __builtin_amdgcn_mfma_f32_16x16x32_bf16(__builtin_bit_cast(s16x8, a), __builtin_bit_cast(s16x8, b), c, 0, 0, 0);
}
DI u32x4 lds128(unsigned a, int off = 0) { return *(const LAS u32x4*)((const LAS char*)(size_t)a + off); }
DI u32x4 tr_frag(unsigned a, int off, int second) {
  s16x4 lo = __builtin_amdgcn_ds_read_tr16_b64_v4i16((LAS s16x4*)((LAS char*)(size_t)a + off));
  s16x4 hi = __builtin_amdgcn_ds_read_tr16_b64_v4i16((LAS s16x4*)((LAS char*)(size_t)a + off + second));
  u32x2 x = __builtin_bit_cast(u32x2, lo), y = __builtin_bit_cast(u32x2, hi);
  return u32x4{x.x, x.y, y.x, y.y};
}
DI float dot2(unsigned a, unsigned b, float c) {
  return __builtin_amdgcn_fdot2_f32_bf16(__builtin_bit_cast(bf16x2_t, a), __builtin_bit_cast(bf16x2_t, b), c, false);
}
DI float sum8(u32x4 v, float c) {
  const unsigned one = 0x3f803f80u;
  c = dot2(v.x, one, c); c = dot2(v.y, one, c); c = dot2(v.z, one, c); return dot2(v.w, one, c);
}
DI float sq8(u32x4 v, float c) { c = dot2(v.x, v.x, c); c = dot2(v.y, v.y, c); c = dot2(v.z, v.z, c); return dot2(v.w, v.w, c); }
DI float vsum8(u32x4 v) {
  const float a = __uint_as_float(v.x << 16) + __uint_as_float(v.y << 16), b = __uint_as_float(v.x & 0xffff0000u) + __uint_as_float(v.y & 0xffff0000u);
  const float c = __uint_as_float(v.z << 16) + __uint_as_float(v.w << 16), d = __uint_as_float(v.z & 0xffff0000u) + __uint_as_float(v.w & 0xffff0000u);
  return (a + b) + (c + d);
}
DI void granule_store(char* ws, unsigned off, float v, unsigned tag) {
  __hip_atomic_store((GAS unsigned long long*)(ws + off), ((unsigned long long)tag << 32) | __float_as_uint(v), __ATOMIC_RELAXED, __HIP_MEMORY_SCOPE_AGENT);
}
DI unsigned long long granule_load(char* ws, unsigned off) {
  return __hip_atomic_load((GAS unsigned long long*)(ws + off), __ATOMIC_RELAXED, __HIP_MEMORY_SCOPE_AGENT);
}
template <int CTRL> DI float dpp_add(float v) {
  return v + __uint_as_float(__builtin_amdgcn_update_dpp(0u, __float_as_uint(v), CTRL, 0xf, 0xf, false));
}
DI float half_sum(float v) {
  v = dpp_add<0xB1>(v);
  v = dpp_add<0x4E>(v);
  v = dpp_add<0x141>(v);
  v = dpp_add<0x140>(v);
  auto r = __builtin_amdgcn_permlane16_swap(__float_as_uint(v), __float_as_uint(v), false, false);
  return __uint_as_float(r[0]) + __uint_as_float(r[1]);
}
DI float xhalf_add(float v) {
  auto r = __builtin_amdgcn_permlane32_swap(__float_as_uint(v), __float_as_uint(v), false, false);
  return __uint_as_float(r[0]) + __uint_as_float(r[1]);
}
DI float xhalf_max(float v) {
  auto r = __builtin_amdgcn_permlane32_swap(__float_as_uint(v), __float_as_uint(v), false, false);
  return fmaxf(__uint_as_float(r[0]), __uint_as_float(r[1]));
}
DI u32x4 pack_feat8(unsigned ax, unsigned ay, unsigned bx, unsigned by) {
  auto rx = __builtin_amdgcn_permlane32_swap(ax, bx, false, false);
  auto ry = __builtin_amdgcn_permlane32_swap(ay, by, false, false);
  return u32x4{rx[0], ry[0], rx[1], ry[1]};
}
DI f32x2 lo2(const f32x4& v) { return f32x2{v.x, v.y}; }
DI f32x2 hi2(const f32x4& v) { return f32x2{v.z, v.w}; }
template <int I> DI f32x2 pair(const f32x16& v) { return f32x2{v[2 * I], v[2 * I + 1]}; }
DI int fresh_lane() { int l; asm volatile("v_mbcnt_lo_u32_b32 %0, -1, 0\n\tv_mbcnt_hi_u32_b32 %0, -1, %0" : "=v"(l)); return l; }
typedef __amdgpu_buffer_rsrc_t rsrc_t;
DI void st128c(rsrc_t r, unsigned off, u32x4 v) { __builtin_amdgcn_raw_buffer_store_b128(v, r, off, 0, 16); }
DI void st64c(rsrc_t r, unsigned off, u32x2 v) { __builtin_amdgcn_raw_buffer_store_b64(v, r, off, 0, 16); }
DI u32x4 ld128c(rsrc_t r, unsigned off) { return __builtin_amdgcn_raw_buffer_load_b128(r, off, 0, 16); }
DI u32x2 ld64c(rsrc_t r, unsigned off) { return __builtin_amdgcn_raw_buffer_load_b64(r, off, 0, 16); }
DI unsigned flag_load(char* ws, int idx) {
  return __hip_atomic_load((GAS unsigned*)(ws + WS_FLAG) + idx * 16, __ATOMIC_RELAXED, __HIP_MEMORY_SCOPE_AGENT);
}
DI void flag_store(char* ws, int idx, unsigned v) {
  __hip_atomic_store((GAS unsigned*)(ws + WS_FLAG) + idx * 16, v, __ATOMIC_RELAXED, __HIP_MEMORY_SCOPE_AGENT);
}
DI unsigned read_epoch(const char* ws) {
  unsigned e;
  asm volatile("s_load_dword %0, %1, 0x0\n\ts_waitcnt lgkmcnt(0)" : "=s"(e) : "s"(ws + WS_FLAG + FL_EPOCH * 64) : "memory");
  return e;
}
DI void arrive_publish(LAS char* L, int word, unsigned total, char* ws, int flag, unsigned tag, int lane) {
  if (lane == 0) {
    if (__hip_atomic_fetch_add((LAS unsigned*)(L + word), 1u, __ATOMIC_RELAXED, __HIP_MEMORY_SCOPE_WORKGROUP) == total - 1u) flag_store(ws, flag, tag);
  }
}
DI void flags_wait(char* ws, int idx, unsigned tag) {
  for (int spin = 0; spin < (1 << 22); spin++) {
    const bool ok = idx < 0 || flag_load(ws, idx) == tag;
    if (__all(ok)) break;
    __builtin_amdgcn_s_sleep(1);
  }
  __builtin_amdgcn_fence(__ATOMIC_ACQUIRE, "wavefront");
}

#define STAMP_DECL
#define STAMP(i)
#define STAMP_DUMP(k)
#define STAMPC(i)

template <class Hook0, class Hook1, class Hook2>
DI void attention_task(rsrc_t rws, unsigned lds0, int w, int lane, int qi, int cgA, int cgX, int odd, u32x4 qf0, u32x4 qf1, Hook0&& mid,
                       Hook1&& per_tile, Hook2&& pre_store) {
  const int r32 = lane & 31, h = lane >> 5;
  const int q0 = 256 * qi + 32 * w;
  const unsigned ka = lds0 + L_K + h * 8192 + (32 * w + r32) * 16;
  const unsigned kx = lds0 + L_K + 16384 + (32 * w + r32) * 16;
  f32x16 sc[9];
  const f32x16 zero = {};
  const unsigned va = r32 < 20 ? lds0 + L_V + (2 * w) * 640 + h * 320 + r32 * 16 : lds0 + AUX_C + (r32 == 20 ? 0 : 16);
  const int vstep = r32 < 20 ? 640 : 0;
  const int tmin = qi == 0 ? 8 - w : 0;
  u32x4 vf[18];
  unsigned dm[8];
  float mxa[4] = {-INFINITY, -INFINITY, -INFINITY, -INFINITY};
  {
    u32x4 k0[9], k1[9];
#pragma unroll
    for (int n = 0; n < 9; n++) { k0[n] = lds128(ka, ((n + 8) % 9) * 512); k1[n] = lds128(kx, ((n + 8) % 9) * 512); }
    sfor<0, 11>([&](auto N) {
      constexpr int n = N, t = (n + 8) % 9, u = n - 3;
      SB();
      if constexpr (n < 9) { sc[t] = mfma32(k0[n], qf0, zero); sc[t] = mfma32(k1[n], qf1, sc[t]); }
      if constexpr (n >= 2 && n < 11) { vf[2 * (n - 2)] = lds128(va + (2 * (n - 2)) * vstep); vf[2 * (n - 2) + 1] = lds128(va + (2 * (n - 2) + 1) * vstep); }
      if constexpr (u >= 0) {
        if (u < tmin) {
#pragma unroll
          for (int i = 0; i < 16; i++) sc[u][i] = -INFINITY;
        }
        if constexpr (u == 0) {
#pragma unroll
          for (int i = 0; i < 16; i += 2) {
            const int kr = (i & 3) + 8 * (i >> 2) + 4 * h;
            const bool d0 = kr <= r32, d1 = kr + 1 <= r32;
            sc[0][i] = d0 ? sc[8][i] : sc[0][i];
            sc[0][i + 1] = d1 ? sc[8][i + 1] : sc[0][i + 1];
            dm[i >> 1] = d1 ? 0xffffffffu : (d0 ? 0x0000ffffu : 0u);
          }
        }
#pragma unroll
        for (int i = 0; i < 8; i++) mxa[i & 3] = max3(mxa[i & 3], sc[u][2 * i], sc[u][2 * i + 1]);
      }
    });
  }
  SB();
  float mx = max3(mxa[0], mxa[1], max3(mxa[2], mxa[3], mxa[3]));
  {
    auto r = __builtin_amdgcn_permlane32_swap(__float_as_uint(mx), __float_as_uint(mx), false, false);
    mx = max3(__uint_as_float(r[0]), __uint_as_float(r[1]), __uint_as_float(r[1]));
  }
  u32x4 nm;
  {
    const unsigned m0 = __float_as_uint(mx) & 0xffff0000u;
    const float r1 = mx - __uint_as_float(m0);
    const unsigned m1 = __float_as_uint(r1) & 0xffff0000u;
    const unsigned m2 = __float_as_uint(r1 - __uint_as_float(m1)) & 0xffff0000u;
    const unsigned a = ((m0 >> 16) | m1) ^ 0x80008000u, c = (m2 >> 16) ^ 0x8000u;
    nm = u32x4{h ? 0u : a, h ? 0u : c, 0u, 0u};
  }
  const u32x4 ones3 = {0x3f803f80u, 0x00003f80u, 0u, 0u};
  mid();
  SB();
  sc[1] = mfma32(ones3, nm, sc[1]);
  sc[2] = mfma32(ones3, nm, sc[2]);
  f32x16 o = {};
  sfor<1, 9>([&](auto T) {
    constexpr int tt = T, t = tt & 7;
    SB();
    if constexpr (tt + 2 < 9) sc[(tt + 2) & 7] = mfma32(ones3, nm, sc[(tt + 2) & 7]);
    per_tile(T);
    unsigned pk[8];
#pragma unroll
    for (int i = 0; i < 8; i++) pk[i] = pk2(__builtin_amdgcn_exp2f(sc[t][2 * i]), __builtin_amdgcn_exp2f(sc[t][2 * i + 1]));
    if constexpr (t != 0) {
      o = mfma32(vf[2 * t], u32x4{pk[0], pk[1], pk[2], pk[3]}, o);
      o = mfma32(vf[2 * t + 1], u32x4{pk[4], pk[5], pk[6], pk[7]}, o);
    } else {
      o = mfma32(vf[16], u32x4{pk[0] & dm[0], pk[1] & dm[1], pk[2] & dm[2], pk[3] & dm[3]}, o);
      o = mfma32(vf[17], u32x4{pk[4] & dm[4], pk[5] & dm[5], pk[6] & dm[6], pk[7] & dm[7]}, o);
      o = mfma32(vf[0], u32x4{pk[0] & ~dm[0], pk[1] & ~dm[1], pk[2] & ~dm[2], pk[3] & ~dm[3]}, o);
      o = mfma32(vf[1], u32x4{pk[4] & ~dm[4], pk[5] & ~dm[5], pk[6] & ~dm[6], pk[7] & ~dm[7]}, o);
    }
  });
  SB();
  pre_store();
  SB();
  auto lr = __builtin_amdgcn_permlane32_swap(__float_as_uint(o[8]), __float_as_uint(o[8]), false, false);
  const float inv = __builtin_amdgcn_rcpf(__uint_as_float(lr[1]));
  const unsigned ob = WS_ACT + 192u * S_ * 16;
  st128c(rws, ob + ((cgA + h) * S_ + q0 + r32) * 16,
         u32x4{pk2(o[0] * inv, o[1] * inv), pk2(o[2] * inv, o[3] * inv), pk2(o[4] * inv, o[5] * inv), pk2(o[6] * inv, o[7] * inv)});
  if (h == 0) st64c(rws, ob + (cgX * S_ + q0 + r32) * 16 + odd * 8, u32x2{pk2(o[8] * inv, o[9] * inv), pk2(o[10] * inv, o[11] * inv)});
}
DI void load_q(rsrc_t rws, int w, int lane, int qi, int cgA, int cgX, int odd, u32x4& qf0, u32x4& qf1) {
  const int r32 = lane & 31, h = lane >> 5, q0 = 256 * qi + 32 * w;
  qf0 = ld128c(rws, WS_QK + ((cgA + h) * S_ + q0 + r32) * 16);
  const u32x2 xq = ld64c(rws, WS_QK + (cgX * S_ + q0 + r32) * 16 + odd * 8);
  const unsigned x0 = h ? 0u : xq.x, x1 = h ? 0u : xq.y;
  qf1 = odd ? u32x4{0u, 0u, x0, x1} : u32x4{x0, x1, 0u, 0u};
}

__global__ __launch_bounds__(512, 2) void k_fused(const u16* __restrict__ x, const u16* __restrict__ wqkv, const u16* __restrict__ wo,
                                                  const u16* __restrict__ w1, const u16* __restrict__ b1, const u16* __restrict__ w2,
                                                  const u16* __restrict__ b2, char* __restrict__ ws, u16* __restrict__ out) {
  extern __shared__ __attribute__((aligned(16))) char smem[];
  LAS char* L = (LAS char*)smem;
  const unsigned lds0 = (unsigned)(size_t)L;
  const int tid = threadIdx.x, lane = tid & 63, w = __builtin_amdgcn_readfirstlane(tid >> 6);
  const int b = blockIdx.x, xcd = b & 7, bi = b >> 3;
  const int s = w & 3;
  const rsrc_t rws = __builtin_amdgcn_make_buffer_rsrc(ws, 0, WS_BYTES, 0x00020000);
  const int rb = (xcd & 1) * 8 + (bi >> 2), j = (xcd >> 1) * 4 + (bi & 3);
  const int row0 = rb * 128;
  const int kc = (5 * rb) >> 1, rot = ((kc >> 2) + 3 * j) % 10, kc0 = kc & 3, kc1 = kc0 + 2 + (rb & 1);
  const int hd = xcd * 4 + (bi & 3), qi = bi >> 2;
  const int odd = hd & 1, cgA = (hd >> 1) * 5 + (odd ? 3 : 0), cgX = (hd >> 1) * 5 + 2;
  const int rb3 = (xcd >> 1) * 8 + (bi >> 2), cgp = (xcd & 1) * 4 + (bi & 3);
  const int row3 = rb3 * 64, n0 = cgp * 80;
  STAMP_DECL;
  STAMP(0); STAMPC(38);
  const int r32 = lane & 31, h = lane >> 5;
  const int blk = (lane >> 4) & 1, q4 = (lane & 15) >> 2, p4 = lane & 3;
  const unsigned a0 = lds0 + L_A + (32 * s + r32) * 128 + ((h ^ ((r32 >> 1) & 7)) * 16);
  auto rdA = [&](int p, int t) -> u32x4 {
    if (p % 5 < 4) return lds128(a0 ^ (t * 32), (p % 5) * 16384);
    unsigned ad;
    asm volatile("v_add_u32 %0, 0x10000, %1" : "=v"(ad) : "v"(a0 ^ (t * 32)));
    return lds128(ad);
  };
  const unsigned bb = lds0 + L_B + (8 * h + q4) * 256 + (4 * q4 + 2 * blk + (p4 >> 1)) * 16 + 8 * (p4 & 1);
  const unsigned fa = lds0 + L_B + (8 * h + q4) * 192 + (2 * blk + (p4 >> 1)) * 16 + 8 * (p4 & 1);
  constexpr int FP[6] = {36864, 49152, 61440, 0, 12288, 24576};
  auto rdB = [&](int p, int t, int u) { return tr_frag(bb ^ (64 * u), (p & 3) * 16384 + t * 4096, 1024); };
  auto rdF = [&](int f, int t, int u) { return tr_frag(fa, FP[f % 6] + t * 3072 + u * 64, 768); };

  if (w < 4) {
    const int srow = row0 + 8 * j + 2 * s + h;
    u32x2 sx[5];
#pragma unroll
    for (int i = 0; i < 5; i++) sx[i] = *(const u32x2*)(x + (size_t)srow * D_ + 128 * i + 4 * r32);
    const unsigned tag = read_epoch(ws) + 1u;
    u32x4 af[20], at[4];
    auto getA = [&](auto P, auto T) -> u32x4& { if constexpr (P < 5) return af[4 * P + T]; else return at[T]; };
    auto row_stats = [&]() {
      float v[20];
#pragma unroll
      for (int i = 0; i < 5; i++) {
        v[4 * i] = bf2f(sx[i].x & 0xffffu); v[4 * i + 1] = bf2f(sx[i].x >> 16); v[4 * i + 2] = bf2f(sx[i].y & 0xffffu); v[4 * i + 3] = bf2f(sx[i].y >> 16);
      }
      float sm = ((v[0] + v[1]) + (v[2] + v[3])) + ((v[4] + v[5]) + (v[6] + v[7])) + ((v[8] + v[9]) + (v[10] + v[11]));
      sm += ((v[12] + v[13]) + (v[14] + v[15])) + ((v[16] + v[17]) + (v[18] + v[19]));
      sm = half_sum(sm);
      const float mu = sm * (1.0f / D_);
      float q4[4] = {0.f, 0.f, 0.f, 0.f};
#pragma unroll
      for (int i = 0; i < 20; i++) q4[i & 3] = __builtin_fmaf(v[i] - mu, v[i] - mu, q4[i & 3]);
      float sq = (q4[0] + q4[1]) + (q4[2] + q4[3]);
      sq = half_sum(sq);
      const float r = __builtin_amdgcn_rsqf(sq * (1.0f / D_) + 1e-5f);
      const int lc = fresh_lane();
      if ((lc & 31) < 2) granule_store(ws, WS_STAT + (row0 + 8 * j + 2 * s + (lc >> 5)) * 16 + (lc & 1) * 8, (lc & 1) ? r : mu * r, tag);
    };
    row_stats();
    unsigned long long stg = 0;
    auto stoff = [&]() { const int lc = fresh_lane(); return WS_STAT + (row0 + 32 * s + (lc & 31)) * 16 + (lc >> 5) * 8; };
    auto conv = [&](int hc) {
      const int kb = hc / 5, cb = hc % 5, lc = fresh_lane();
      const unsigned dst = WS_WT + ((kb * 4 + 2 * (s >> 1) + (lc >> 5)) * D_ + cb * 128 + 64 * (s & 1) + (lc & 31)) * 16;
      const unsigned src = (bb ^ (128 * (s & 1))) + (s >> 1) * 4096;
      st128c(rws, dst, tr_frag(src, L_T, 1024));
      st128c(rws, dst + 512, tr_frag(src ^ 64, L_T, 1024));
    };
    f32x16 acc[4] = {};
    auto body = [&](auto MODE) {
      u32x4 bs[3][4];
      auto mm = [&](auto N, int u) {
        constexpr int n = N;
        if constexpr (MODE != 2) {
          const u32x4 a = getA(IC<n / 4>{}, IC<n % 4>{});
          if constexpr (MODE == 0) acc[u] = mfma32(bs[n % 3][u], a, acc[u]); else acc[u] = mfma32(a, bs[n % 3][u], acc[u]);
        }
      };
      auto rB = [&](int g, int t, int u) { if constexpr (MODE != 2) bs[(4 * g + t) % 3][u] = rdB(g, t, u); };
      sfor<0, 10>([&](auto G) {
        constexpr int g = G;
        lds_bar();
        if constexpr (g == 0 || g == 5) { STAMP(1 + g); }
#pragma unroll
        for (int u = 0; u < 4; u++) rB(g, 0, u);
        sfor<1, 4>([&](auto T) {
          constexpr int t = T;
          SB();
#pragma unroll
          for (int u = 0; u < 4; u++) {
            if constexpr (4 * g + t - 3 >= 0) mm(IC<(4 * g + t - 3 >= 0 ? 4 * g + t - 3 : 0)>{}, u);
            if (t == 1 && u == 3) getA(G, IC<0>{}) = rdA(g, 0);
            if (t == 1 && u == 3) getA(G, IC<1>{}) = rdA(g, 1);
            if (t == 2 && u == 3) getA(G, IC<2>{}) = rdA(g, 2);
            if (t == 3 && u == 0) getA(G, IC<3>{}) = rdA(g, 3);
            rB(g, t, u);
            SB();
          }
        });
#pragma unroll
        for (int u = 0; u < 4; u++) mm(IC<4 * g + 1>{}, u);
        if constexpr (g == 8) stg = granule_load(ws, stoff());
        if constexpr (g == 3) conv(b);
        if constexpr (g == 7) { if (b < 84) conv(256 + b); }
      });
#pragma unroll
      for (int u = 0; u < 4; u++) mm(IC<38>{}, u);
#pragma unroll
      for (int u = 0; u < 4; u++) mm(IC<39>{}, u);
    };
    if (j < 10) body(IC<0>{}); else if (j < 15) body(IC<1>{}); else body(IC<2>{});
    STAMP(11);
    float mur, rs;
    {
      for (int spin = 0; spin < (1 << 22) && !__all((unsigned)(stg >> 32) == tag); spin++) stg = granule_load(ws, stoff());
      auto r = __builtin_amdgcn_permlane32_swap((unsigned)stg, (unsigned)stg, false, false);
      mur = __uint_as_float(r[0]); rs = __uint_as_float(r[1]);
    }
    const int l2 = fresh_lane(), r2 = l2 & 31, h2 = l2 >> 5;
    if (j >= 10 && h2 == 0) {
      ((LAS float*)(L + AUX_ST))[32 * s + r2] = mur;
      ((LAS float*)(L + AUX_ST))[128 + 32 * s + r2] = rs;
    }
    wait_bar<0>();
    if (j < 10) {
      const float sc = (j < 5) ? QSCALE : 1.0f;
      const f32x2 a2 = {rs * sc, rs * sc}, b2q = {-mur * sc, -mur * sc};
      const unsigned dst = WS_QK + ((16 * j + h2) * S_ + row0 + 32 * s + r2) * 16;
      const unsigned csa = lds0 + AUX_CS + 16 * h2;
      sfor<0, 2>([&](auto UU) {
        f32x4 cs[8];
#pragma unroll
        for (int i = 0; i < 8; i++) cs[i] = __builtin_bit_cast(f32x4, lds128(csa, (64 * UU + 8 * i) * 4));
        SB();
        sfor<0, 4>([&](auto C) {
          constexpr int u = 2 * UU + C / 2, k = C % 2, ci = 2 * C;
          const unsigned p0 = pk2v(fma2(pair<4 * k>(acc[u]), a2, lo2(cs[ci]) * b2q)), p1 = pk2v(fma2(pair<4 * k + 1>(acc[u]), a2, hi2(cs[ci]) * b2q));
          const unsigned p2 = pk2v(fma2(pair<4 * k + 2>(acc[u]), a2, lo2(cs[ci + 1]) * b2q)), p3 = pk2v(fma2(pair<4 * k + 3>(acc[u]), a2, hi2(cs[ci + 1]) * b2q));
          st128c(rws, dst + (4 * u + 2 * k) * (S_ * 16), pack_feat8(p0, p1, p2, p3));
        });
      });
    } else if (j < 15) {
      f32x4 murv[4], rsv[4];
      float csv[4];
#pragma unroll
      for (int g = 0; g < 4; g++) {
        murv[g] = *(const LAS f32x4*)(L + AUX_ST + (32 * s + 8 * g + 4 * h2) * 4);
        rsv[g] = *(const LAS f32x4*)(L + AUX_ST + 512 + (32 * s + 8 * g + 4 * h2) * 4);
        csv[g] = -*(const LAS float*)(L + AUX_CS + (32 * g + r2) * 4);
      }
      sfor<0, 4>([&](auto U) {
        constexpr int u = U;
        const f32x2 cs2 = {csv[u], csv[u]};
        const int colv = 128 * (j - 10) + 32 * u + r2;
        const int head = colv / 20, d = colv - head * 20;
        const int dd = (head & 1) ? (d < 4 ? d + 16 : d - 4) : d;
        const int slot = dd < 16 ? ((dd & 3) + 8 * ((dd >> 2) & 1) + 4 * (dd >> 3)) : dd;
        const unsigned dst = WS_VT + ((head * 128 + (row0 + 32 * s) / 16) * 2 + h2) * 320 + slot * 16;
        sfor<0, 2>([&](auto SP) {
          constexpr int sp = SP;
          const unsigned p0 = pk2v(fma2(pair<4 * sp>(acc[u]), lo2(rsv[2 * sp]), cs2 * lo2(murv[2 * sp])));
          const unsigned p1 = pk2v(fma2(pair<4 * sp + 1>(acc[u]), hi2(rsv[2 * sp]), cs2 * hi2(murv[2 * sp])));
          const unsigned p2 = pk2v(fma2(pair<4 * sp + 2>(acc[u]), lo2(rsv[2 * sp + 1]), cs2 * lo2(murv[2 * sp + 1])));
          const unsigned p3 = pk2v(fma2(pair<4 * sp + 3>(acc[u]), hi2(rsv[2 * sp + 1]), cs2 * hi2(murv[2 * sp + 1])));
          st128c(rws, dst + sp * 640, u32x4{p0, p1, p2, p3});
        });
      });
    }
    STAMP(12);
    f32x16 facc[3] = {};
    {
      u32x4 bs[3][3];
      auto pos = [](int f) { return (f + 5) % 10; };
      auto mm = [&](auto N, int u) {
        constexpr int n = N;
        facc[u] = mfma32(bs[n % 3][u], getA(IC<(n / 4 + 5) % 10>{}, IC<n % 4>{}), facc[u]);
      };
      sfor<0, 10>([&](auto Fs) {
        constexpr int f = Fs;
        if constexpr (f == 2 || f == 4 || f == 6 || f == 8) lds_bar();
        if constexpr (f == 3) { asm volatile("" : "+v"(facc[0]), "+v"(facc[1]), "+v"(facc[2])); drain(); arrive_publish(L, AUX_W + 12, 4, ws, FL_1 + rb * 16 + j, tag, fresh_lane()); SB(); }
        if constexpr (f == 5) { STAMP(13 + f); }
#pragma unroll
        for (int u = 0; u < 3; u++) bs[(4 * f) % 3][u] = rdF(f, 0, u);
        sfor<1, 4>([&](auto T) {
          constexpr int t = T;
          SB();
#pragma unroll
          for (int u = 0; u < 3; u++) {
            if constexpr (4 * f + t - 3 >= 0) mm(IC<(4 * f + t - 3 >= 0 ? 4 * f + t - 3 : 0)>{}, u);
            if constexpr (f < 5) {
              if (t == 1 && u == 2) at[0] = rdA(pos(f), 0);
              if (t == 1 && u == 2) at[1] = rdA(pos(f), 1);
              if (t == 2 && u == 2) at[2] = rdA(pos(f), 2);
              if (t == 3 && u == 0) at[3] = rdA(pos(f), 3);
            }
            bs[(4 * f + t) % 3][u] = rdF(f, t, u);
            SB();
          }
        });
#pragma unroll
        for (int u = 0; u < 3; u++) mm(IC<4 * f + 1>{}, u);
      });
#pragma unroll
      for (int u = 0; u < 3; u++) mm(IC<38>{}, u);
#pragma unroll
      for (int u = 0; u < 3; u++) mm(IC<39>{}, u);
    }
    STAMP(23);
    {
      const int l3 = fresh_lane(), r3 = l3 & 31, h3 = l3 >> 5;
      while (true) {
        const u32x4 rdy = *(const volatile LAS u32x4*)(L + AUX_W);
        if (__builtin_amdgcn_readfirstlane(rdy.x & rdy.y & rdy.z) == 1u) break;
      }
      asm volatile("" ::: "memory");
      const unsigned dst = WS_ACT + ((12 * j + h3) * S_ + row0 + 32 * s + r3) * 16;
      const unsigned ca = lds0 + AUX_CF + 16 * h3;
      const f32x2 rs2 = {rs, rs}, nm2 = {-mur, -mur};
      auto act = [&](f32x2 a, f32x2 cs, f32x2 bv) {
        f32x2 t = fma2(a, rs2, fma2(cs, nm2, bv));
        t = f32x2{relu(t.x), relu(t.y)};
        return pk2v(t * t);
      };
      sfor<0, 3>([&](auto U) {
        constexpr int u = U;
        f32x4 cs[4], bv[4];
#pragma unroll
        for (int i = 0; i < 4; i++) {
          cs[i] = __builtin_bit_cast(f32x4, lds128(ca, (32 * u + 8 * i) * 4));
          bv[i] = __builtin_bit_cast(f32x4, lds128(ca, AUX_BF - AUX_CF + (32 * u + 8 * i) * 4));
        }
        SB();
        sfor<0, 2>([&](auto K) {
          constexpr int k = K;
          const unsigned p0 = act(pair<4 * k>(facc[u]), lo2(cs[2 * k]), lo2(bv[2 * k])), p1 = act(pair<4 * k + 1>(facc[u]), hi2(cs[2 * k]), hi2(bv[2 * k]));
          const unsigned p2 = act(pair<4 * k + 2>(facc[u]), lo2(cs[2 * k + 1]), lo2(bv[2 * k + 1]));
          const unsigned p3 = act(pair<4 * k + 3>(facc[u]), hi2(cs[2 * k + 1]), hi2(bv[2 * k + 1]));
          st128c(rws, dst + (4 * u + 2 * k) * (S_ * 16), pack_feat8(p0, p1, p2, p3));
        });
      });
    }
    STAMP(24);
    while (__builtin_amdgcn_readfirstlane(*(const volatile LAS unsigned*)(L + AUX_W + 16 + 4 * s)) != 1u) __builtin_amdgcn_s_sleep(1);
    asm volatile("" ::: "memory");
    const int lanem = fresh_lane();
    u32x4 qf0, qf1;
    load_q(rws, w, lanem, qi, cgA, cgX, odd, qf0, qf1);
    wait_bar<0>();
    STAMP(25);
    const int mh = s & 1, kh = s >> 1;
    const int rot3 = ((bi >> 2) + 3 * (bi & 3)) % 12, rot4 = ((bi >> 2) + (bi & 3)) % 5;
    auto kq = [&](int st) { int v; if (st < 12) { v = st + rot3; if (v >= 12) v -= 12; } else { v = st + rot4; if (v >= 17) v -= 5; } return v; };
    u32x4 ar[4][4];
    unsigned avo;
    auto ldA = [&](auto ST) {
      constexpr int st = ST;
      const unsigned so = kq(st) * (16 * S_ * 16);
#pragma unroll
      for (int i = 0; i < 4; i++) ar[st & 3][i] = __builtin_amdgcn_raw_buffer_load_b128(rws, (avo ^ ((i & 1) * 256)) + (i >> 1) * (8 * S_ * 16), so, 16);
    };
    unsigned f2 = tag;
    attention_task(rws, lds0, w, lanem, qi, cgA, cgX, odd, qf0, qf1,
                   [&]() {}, [&](auto T) { if constexpr (T == 5) f2 = flag_load(ws, FL_2 + (rb3 >> 1) * 16 + (lanem & 15)); },
                   [&]() {
                     if (!__all(f2 == tag)) flags_wait(ws, lanem < 16 ? FL_2 + (rb3 >> 1) * 16 + lanem : -1, tag);
                     avo = WS_ACT + ((4 * kh + (lanem >> 4)) * S_ + row3 + 32 * mh + 16 * kh + (lanem & 15)) * 16;
                     ldA(IC<0>{}); ldA(IC<1>{}); ldA(IC<2>{});
                   });
    STAMP(26);
    while (true) {
      const u32x4 rdy = *(const volatile LAS u32x4*)(L + AUX_W + 48);
      if (__builtin_amdgcn_readfirstlane(rdy.x & rdy.y & rdy.z & rdy.w) == 1u) break;
      __builtin_amdgcn_s_sleep(1);
    }
    asm volatile("" ::: "memory");
    const int tok = row3 + 32 * mh + 16 * kh + (lanem & 15);
    u32x2 xv[5], bv[5];
    f32x4 accf[2][5] = {}, accs[2][5] = {}, gate[5];
    const unsigned wr = lds0 + kh * 5120 + lanem * 16;
    u32x4 fw[2][5];
    unsigned f3 = tag;
    auto mm1 = [&](auto ST, auto KK, auto X) {
      constexpr int st = ST, kk = KK, i = X / 2, r = X % 2;
      if constexpr (st < 12) accf[r][i] = mfma16(fw[kk][i], ar[st & 3][2 * kk + r], accf[r][i]);
      else accs[r][i] = mfma16(fw[kk][i], ar[st & 3][2 * kk + r], accs[r][i]);
    };
    auto ldA1 = [&](auto ST, auto I) {
      constexpr int st = ST, i = I;
      ar[st & 3][i] = __builtin_amdgcn_raw_buffer_load_b128(rws, (avo ^ ((i & 1) * 256)) + (i >> 1) * (8 * S_ * 16), kq(st) * (16 * S_ * 16), 16);
    };
    sfor<0, 17>([&](auto ST) {
      constexpr int st = ST;
      if constexpr (st == 5) wait_bar<12>(); else if constexpr (st >= 3) lds_bar();
      if constexpr (st == 2) { STAMP(27); }
      if constexpr (st == 12) { STAMP(28); }
      if constexpr (st == 9) { if (!__all(f3 == tag)) flags_wait(ws, lanem < 32 ? FL_3 + lanem * 8 + (rb3 >> 2) : -1, tag); }
      sfor<0, 10>([&](auto X) {
        constexpr int xx = X;
        if constexpr (xx < 5) fw[0][xx] = lds128(wr, W3[st % 7] + xx * 1024);
        if constexpr (st > 0) mm1(IC<(st > 0 ? st - 1 : 0)>{}, IC<1>{}, X);
        if constexpr (st == 13 && xx >= 5) bv[xx - 5] = *(const u32x2*)(b2 + n0 + 16 * (xx - 5) + 4 * (lanem >> 4));
        if constexpr (st == 14 && xx >= 5) xv[xx - 5] = *(const u32x2*)(x + (size_t)tok * D_ + n0 + 16 * (xx - 5) + 4 * (lanem >> 4));
        SB();
      });
      sfor<0, 10>([&](auto X) {
        constexpr int xx = X;
        if constexpr (xx < 5) fw[1][xx] = lds128(wr, W3[st % 7] + 10240 + xx * 1024);
        mm1(ST, IC<0>{}, X);
        if constexpr (xx >= 6 && st + 3 < 17) ldA1(IC<(st + 3 < 17 ? st + 3 : 0)>{}, IC<(xx >= 6 ? xx - 6 : 0)>{});
        SB();
      });
      if constexpr (st == 1) {
#pragma unroll
        for (int i = 0; i < 5; i++) asm volatile("" : "+v"(accf[0][i]), "+v"(accf[1][i]));
        asm volatile("s_waitcnt vmcnt(8)" ::: "memory");
        arrive_publish(L, AUX_W + 32, 8, ws, FL_3 + hd * 8 + qi, tag, lanem);
      }
      if constexpr (st == 7) f3 = flag_load(ws, FL_3 + (lanem & 31) * 8 + (rb3 >> 2));
      if constexpr (st == 12) {
        LAS f32x4* ex = (LAS f32x4*)(L + W3[3]) + s * 320 + lanem;
#pragma unroll
        for (int i = 0; i < 5; i++) ex[i * 64] = accf[1][i];
      }
      if constexpr (st == 15) {
        const LAS f32x4* ey = (const LAS f32x4*)(L + W3[3]) + (s ^ 2) * 320 + lanem;
#pragma unroll
        for (int i = 0; i < 5; i++) {
          const f32x4 pf = ey[i * 64];
          const float bf[4] = {bf2f(bv[i].x & 0xffffu), __uint_as_float(bv[i].x & 0xffff0000u), bf2f(bv[i].y & 0xffffu), __uint_as_float(bv[i].y & 0xffff0000u)};
#pragma unroll
          for (int e = 0; e < 4; e++) {
            const float g = accf[0][i][e] + pf[e] + bf[e];
            gate[i][e] = g * __builtin_amdgcn_rcpf(1.0f + __builtin_amdgcn_exp2f(-1.4426950408889634f * g));
          }
        }
      }
    });
    sfor<0, 10>([&](auto X) { mm1(IC<16>{}, IC<1>{}, X); });
    STAMP(29);
    {
      LAS f32x4* ez = (LAS f32x4*)(L + W3[5]) + s * 320 + lanem;
#pragma unroll
      for (int i = 0; i < 5; i++) ez[i * 64] = accs[1][i];
    }
    lds_bar();
    {
      const LAS f32x4* ey = (const LAS f32x4*)(L + W3[5]) + (s ^ 2) * 320 + lanem;
      f32x4 ps[5];
#pragma unroll
      for (int i = 0; i < 5; i++) ps[i] = ey[i * 64];
#pragma unroll
      for (int i = 0; i < 5; i++) {
        const float xf[4] = {bf2f(xv[i].x & 0xffffu), __uint_as_float(xv[i].x & 0xffff0000u), bf2f(xv[i].y & 0xffffu), __uint_as_float(xv[i].y & 0xffff0000u)};
        float r[4];
#pragma unroll
        for (int e = 0; e < 4; e++) r[e] = __builtin_fmaf(accs[0][i][e] + ps[i][e], gate[i][e], xf[e]);
        *(u32x2*)(out + (size_t)tok * D_ + n0 + 16 * i + 4 * (lanem >> 4)) = u32x2{pk2(r[0], r[1]), pk2(r[2], r[3])};
      }
    }
    STAMP(30); STAMPC(39);
    if (w == 0) { STAMP_DUMP(0); }
  } else {
    const char* xbase = (const char*)(x + (size_t)row0 * D_);
    const char* fbase = (const char*)(w1 + 96 * j);
    const char* qbase = (const char*)(wqkv + 128 * (j < 15 ? j : 14));
    unsigned voffA[4], voffB[4], voffF[3];
#pragma unroll
    for (int e = 0; e < 4; e++) {
      voffA[e] = ((32 * s + 8 * e + (lane >> 3)) * D_ + (((lane & 7) ^ (4 * (e & 1) + (lane >> 4))) * 8)) * 2 - e * 1024;
      voffB[e] = (16 * s + 4 * e + (lane >> 4)) * (NQKV * 2) + ((lane & 15) ^ ((lane >> 4) << 2)) * 16 - e * 1024;
    }
    const unsigned voffT = (8 * s + (lane >> 4)) * (D_ * 2) + ((lane & 15) ^ ((lane >> 4) << 2)) * 16;
#pragma unroll
    for (int e = 0; e < 3; e++) { const int Lb = e * 1024 + lane * 16; voffF[e] = (16 * s + Lb / 192) * (F_ * 2) + Lb % 192 - e * 1024; }
    const unsigned l4 = lds0 + s * 4096, l3k = lds0 + s * 3072;
    auto kst = [&](int p) { const int v = p + rot; return v >= 10 ? v - 10 : v; };
    auto dmaP = [&](auto P, const char* xs, const char* qs) {
      constexpr int p = P;
      glds16x4<L_A + (p % 5) * 16384>(voffA, xs, l4);
      glds16x4<L_B + (p & 3) * 16384>(voffB, qs, l4);
    };
    auto dmaF = [&](auto Fs) {
      constexpr int f = Fs;
      glds16x3<L_B + FP[f % 6]>(voffF, fbase + (size_t)(kst((f + 5) % 10) * (64 * F_ * 2)), l3k);
    };
    auto dmaT = [&](int hc) {
      const unsigned kb = (unsigned)hc / 5u, cb = hc - kb * 5;
      const size_t base = kb < 48 ? (size_t)w2 : (size_t)wo - (size_t)48 * 32 * D_ * 2;
      const char* src = (const char*)(base + (size_t)(kb * (32 * D_ * 2) + cb * 256));
      glds16<L_T>(voffT, src, lds0 + s * 2048);
      glds16<L_T + 1024>(voffT, src + 4 * D_ * 2, lds0 + s * 2048);
    };
    if (w == 4) { if (lane < 12) glds16<AUX_B1>(lane * 16, (const char*)(b1 + 96 * j), lds0); }
    sfor<0, 3>([&](auto P) {
      const char* xs = xbase + kst(P) * 128;
      const char* qs = qbase + (size_t)(kst(P) * (64 * NQKV * 2));
      dmaP(P, xs, qs);
    });
    if (w == 5 && lane < 32) ((LAS unsigned*)(L + AUX_C))[lane] = lane < 4 ? 0x3f803f80u : 0u;
    const unsigned tag = read_epoch(ws) + 1u;
    const unsigned cspub = WS_CSP + ((j * 16 + rb) * 224 + 32 * s + r32) * 8;
    const char* csgat = ws + WS_CSP + (size_t)(j * 16) * 224 * 8;
    const unsigned voffG = (8 * h * 224 + 32 * s + r32) * 8;
    u32x2 gr[8];
    auto gather_issue = [&](int off) {
#pragma unroll
      for (int i = 0; i < 8; i++)
        asm volatile("global_load_dwordx2 %0, %1, %2 sc1" : "=v"(gr[i]) : "v"(voffG), "s"(csgat + off + i * 224 * 8) : "memory");
    };
    auto gather_sum = [&](int off) -> float {
      for (int spin = 0; spin < (1 << 22); spin++) {
#pragma unroll
        for (int i = 0; i < 8; i++) asm volatile("" : "+v"(gr[i]));
        bool ok = true;
#pragma unroll
        for (int i = 0; i < 8; i++) ok &= gr[i].y == tag;
        if (__all(ok)) break;
        gather_issue(off);
        drain();
      }
      float a = 0.f;
#pragma unroll
      for (int i = 0; i < 8; i++) a += __uint_as_float(gr[i].x);
      return xhalf_add(a);
    };
    float cpart = 0.f;
    sfor<0, 10>([&](auto G) {
      constexpr int g = G;
      constexpr int NW[10] = {16, 18, 18, 17, 17, 18, 18, 16, 19, 9};
      wait_bar<NW[g]>();
      if constexpr (g == 1) { if (w == 7 && lane < 48) {
        const unsigned v = *(const LAS unsigned*)(L + AUX_B1 + lane * 4);
        *(LAS u32x2*)(L + AUX_BF + lane * 8) = u32x2{v << 16, v & 0xffff0000u};
      } }
      if constexpr (g == 0) dmaT(b);
      if constexpr (g == 4) dmaT(b < 84 ? 256 + b : b);
      if constexpr (g == 2) { cpart = xhalf_add(cpart); if (h == 0) granule_store(ws, cspub, cpart, tag); else asm volatile("" ::: "memory"); }
      if constexpr (g == 7) gather_issue(0);
      const char* xs = xbase + kst(g < 7 ? g + 3 : 0) * 128;
      const char* qs = qbase + (size_t)(kst(g < 7 ? g + 3 : 0) * (64 * NQKV * 2));
      if constexpr (g < 7) dmaP(IC<(g < 7 ? g + 3 : 0)>{}, xs, qs);
      else if constexpr (g == 7) dmaF(IC<0>{});
      else if constexpr (g == 8) { dmaF(IC<1>{}); dmaF(IC<2>{}); }
      else dmaF(IC<3>{});
      if constexpr (g < 2) {
#pragma unroll
        for (int t = 0; t < 4; t++)
          if (4 * g + t >= kc0 && 4 * g + t < kc1) cpart += vsum8(tr_frag(bb ^ (64 * s), (g & 3) * 16384 + t * 4096, 1024));
      }
      if constexpr (g == 9) {
        const float cs = gather_sum(0);
        if (h == 0) ((LAS float*)(L + AUX_CS))[32 * s + r32] = cs;
      }
    });
    cpart = 0.f;
    const int uf = s < 3 ? s : 2;
    unsigned voffW[5];
#pragma unroll
    for (int i = 0; i < 5; i++) voffW[i] = ((lane >> 4) * D_ + (lane & 15) + 16 * i) * 16 + 4096 - (i - 2) * 1024;
    const char* wbase = ws + WS_WT - 4096 + (size_t)n0 * 16 + (size_t)s * 4 * D_ * 16;
    const int rot3 = ((bi >> 2) + 3 * (bi & 3)) % 12, rot4 = ((bi >> 2) + (bi & 3)) % 5;
    auto kq = [&](int st) { int v; if (st < 12) { v = st + rot3; if (v >= 12) v -= 12; } else { v = st + rot4; if (v >= 17) v -= 5; } return v; };
    const unsigned l5 = lds0 + s * 5120;
    auto issueW = [&](auto ST) {
      constexpr int st = ST;
      const char* ww = wbase + (size_t)(kq(st) * (16 * D_ * 16));
      glds16x5c<W3[st % 7]>(voffW, ww, l5);
    };
    int idxq;
    {
      const int ty = lane >> 3, rbi = (lane >> 1) & 3, hi = lane & 1;
      const int c0 = ty == 2 ? 20 * hd : 8 * (odd ? cgX : cgA), c1 = ty == 2 ? 20 * hd + 19 : 8 * (odd ? cgA + 1 : cgX) + 7;
      const int tile = 5 * ty + (hi ? c1 : c0) / 128, rr = 2 * qi - 2 + rbi;
      idxq = (lane < 24 && rr >= 0 && (ty > 0 || rbi >= 2)) ? FL_1 + rr * 16 + tile : FL_1 + rb * 16 + j;
    }
    unsigned pv[5];
    auto poll_issue = [&](unsigned& dst, int idx) {
      asm volatile("global_load_dword %0, %1, %2 sc1" : "=v"(dst) : "v"(idx * 64), "s"(ws + WS_FLAG) : "memory");
    };
    wait_bar<6>();
    if (w == 4 && lane == 0) flag_store(ws, FL_T + b, tag);
    dmaF(IC<4>{}); dmaF(IC<5>{});
#pragma unroll
    for (int t = 0; t < 8; t++)
      if (t >= kc0 && t < kc1) cpart += vsum8(tr_frag(fa + uf * 64, FP[t >> 2] + (t & 3) * 3072, 768));
    cpart = xhalf_add(cpart);
    if (h == 0 && s < 3) granule_store(ws, cspub + 128 * 8, cpart, tag);
    wait_bar<6>();
    dmaF(IC<6>{}); dmaF(IC<7>{});
    wait_bar<6>();
    dmaF(IC<8>{}); dmaF(IC<9>{});
    gather_issue(128 * 8);
#pragma unroll
    for (int i = 0; i < 4; i++) poll_issue(pv[i], FL_T + 64 * i + lane);
    wait_bar<18>();
    poll_issue(pv[4], idxq);
    asm volatile("s_waitcnt vmcnt(5)" ::: "memory");
    if (s < 3) {
      const float cf = gather_sum(128 * 8);
      if (h == 0) ((LAS float*)(L + AUX_CF))[32 * s + r32] = cf;
      asm volatile("s_waitcnt lgkmcnt(0)" ::: "memory");
      if (lane == 0) *(volatile LAS unsigned*)(L + AUX_W + 4 * s) = 1u;
    }
    lds_bar();
    STAMP(1);
    asm volatile("s_waitcnt vmcnt(0)" : "+v"(pv[4])::"memory");
    if (!__all(pv[4] == tag)) flags_wait(ws, idxq, tag);
    if (lane == 0) *(volatile LAS unsigned*)(L + AUX_W + 16 + 4 * s) = 1u;
    STAMP(2);
    {
      const int key0 = 256 * qi - 256;
      const char* kb = ws + WS_QK + (size_t)80 * S_ * 16;
      const u32x4 z = {0u, 0u, 0u, 0u};
#pragma unroll
      for (int i = 0; i < 2; i++) {
        const int pc = 2 * s + i, kk = key0 + 64 * pc;
        if (kk >= 0) {
          glds16c<L_K>(lane * 16, kb + ((size_t)cgA * S_ + kk) * 16, lds0 + pc * 1024);
          glds16c<L_K + 8192>(lane * 16, kb + ((size_t)(cgA + 1) * S_ + kk) * 16, lds0 + pc * 1024);
          glds16c<L_K + 16384>(lane * 16, kb + ((size_t)cgX * S_ + kk) * 16, lds0 + pc * 1024);
        } else {
#pragma unroll
          for (int c = 0; c < 3; c++) *(LAS u32x4*)(L + L_K + c * 8192 + pc * 1024 + lane * 16) = z;
        }
      }
      const char* vb = ws + WS_VT + ((long)hd * 128 + key0 / 16) * 640;
#pragma unroll
      for (int i = 0; i < 5; i++) {
        const int pc = 5 * s + i;
        if (qi > 0 || pc >= 10) glds16c<L_V>(lane * 16, vb + pc * 1024, lds0 + pc * 1024);
        else *(LAS u32x4*)(L + L_V + pc * 1024 + lane * 16) = z;
      }
    }
    u32x4 qf0, qf1;
    load_q(rws, w, lane, qi, cgA, cgX, odd, qf0, qf1);
#pragma unroll
    for (int i = 0; i < 4; i++) asm volatile("" : "+v"(pv[i]));
    if (!__all(pv[0] == tag && pv[1] == tag && pv[2] == tag && pv[3] == tag)) {
#pragma unroll
      for (int i = 0; i < 4; i++) flags_wait(ws, FL_T + 64 * i + lane, tag);
    }
    asm volatile("" : "+v"(qf0), "+v"(qf1));
    drain();
    issueW(IC<0>{}); issueW(IC<1>{});
    lds_bar();
    STAMP(3);
    if (w == 4 && lane == 0) flag_store(ws, FL_2 + rb * 16 + j, tag);
    issueW(IC<2>{}); issueW(IC<3>{}); issueW(IC<4>{});
    attention_task(rws, lds0, w, lane, qi, cgA, cgX, odd, qf0, qf1,
                   [&]() { drain(); if (lane == 0) *(volatile LAS unsigned*)(L + AUX_W + 48 + 4 * s) = 1u; }, [&](auto T) {}, []() {});
    STAMP(4);
    sfor<3, 17>([&](auto ST) {
      constexpr int st = ST;
      constexpr int NW[17] = {0, 0, 0, 63, 63, 15, 20, 25, 25, 25, 25, 25, 20, 15, 10, 5, 0};
      wait_bar<NW[st]>();
      if constexpr (st == 3) { STAMP(6); }
      if constexpr (st <= 6) { issueW(IC<(st <= 6 ? 2 * st - 1 : 0)>{}); issueW(IC<(st <= 6 ? 2 * st : 0)>{}); }
      else if constexpr (st + 6 < 17) issueW(IC<(st + 6 < 17 ? st + 6 : 0)>{});
      if constexpr (st == 3) { asm volatile("s_waitcnt vmcnt(10)" ::: "memory"); arrive_publish(L, AUX_W + 32, 8, ws, FL_3 + hd * 8 + qi, tag, lane); }
    });
    lds_bar();
    if (b == 0 && w == 7) {
      for (int spin = 0; spin < (1 << 22); spin++) {
        const unsigned f0 = flag_load(ws, FL_2 + lane), f1 = flag_load(ws, FL_2 + 64 + lane), f2 = flag_load(ws, FL_2 + 128 + lane), f3 = flag_load(ws, FL_2 + 192 + lane);
        if (__all(f0 == tag && f1 == tag && f2 == tag && f3 == tag)) break;
      }
      if (lane == 0) flag_store(ws, FL_EPOCH, tag);
    }
    STAMP(7);
    if (w == 4) { STAMP_DUMP(1); }
  }
}

extern "C" void kernel_launch(void* const* d_in, const int* in_sizes, int n_in, void* d_out, int out_size, void* d_ws, size_t ws_size,
                              hipStream_t stream) {
  (void)hipFuncSetAttribute((const void*)k_fused, hipFuncAttributeMaxDynamicSharedMemorySize, LDS_BYTES);
  k_fused<<<256, 512, LDS_BYTES, stream>>>((const u16*)d_in[0], (const u16*)d_in[1], (const u16*)d_in[2], (const u16*)d_in[4],
                                           (const u16*)d_in[5], (const u16*)d_in[6], (const u16*)d_in[7], (char*)d_ws, (u16*)d_out);
}
```

```cpp
#include <hip/hip_runtime.h>
#include <type_traits>

typedef unsigned short u16;
typedef unsigned u32x4 __attribute__((ext_vector_type(4)));
typedef unsigned u32x2 __attribute__((ext_vector_type(2)));
typedef float f32x4 __attribute__((ext_vector_type(4)));
typedef float f32x16 __attribute__((ext_vector_type(16)));
typedef short s16x8 __attribute__((ext_vector_type(8)));
typedef short s16x4 __attribute__((ext_vector_type(4)));
typedef __bf16 bf16x2_t __attribute__((ext_vector_type(2)));
#define LAS __attribute__((address_space(3)))
#define GAS __attribute__((address_space(1)))
#define DI __device__ __forceinline__
#define SB() __builtin_amdgcn_sched_barrier(0)
template <int N> using IC = std::integral_constant<int, N>;

constexpr int S_ = 2048, D_ = 640, F_ = 1536, NQKV = 1920;

constexpr unsigned WS_QK = 0;
constexpr unsigned WS_VT = 0x500000;
constexpr unsigned WS_ACT = 0x800000;
constexpr unsigned WS_WT = 0x1100000;
constexpr unsigned WS_FLAG = 0x1400000;
constexpr unsigned WS_STAT = 0x1418000;
constexpr unsigned WS_CSP = 0x1420000;
constexpr unsigned WS_BYTES = 0x14a0000;
constexpr int FL_EPOCH = 0, FL_1 = 16, FL_2 = FL_1 + 256, FL_3 = FL_2 + 256, FL_T = FL_3 + 256;

constexpr int L_B = 0;
constexpr int L_T = 65536;
constexpr int L_A = 78592;
constexpr int L_K = 115456;
constexpr int L_V = L_K + 24576;
constexpr int L_AUX = 160512;
constexpr int AUX_ST = L_AUX;
constexpr int AUX_CS = AUX_ST + 1024;
constexpr int AUX_CF = AUX_CS + 512;
constexpr int AUX_B1 = AUX_CF + 512;
constexpr int AUX_BF = AUX_B1 + 256;
constexpr int AUX_C = AUX_BF + 512;
constexpr int AUX_W = AUX_C + 32;
constexpr int LDS_BYTES = 163840;
constexpr int W3[7] = {73728, 94208, 0, 20480, 40960, 115456, 135936};
constexpr float QSCALE = 0.22360679774997896f * 1.4426950408889634f;

template <int I, int N, class Fn> DI void sfor(Fn&& f) {
  if constexpr (I < N) { f(IC<I>{}); sfor<I + 1, N>(f); }
}
template <int N> DI void wait_bar() { SB(); asm volatile("s_waitcnt vmcnt(%0) lgkmcnt(0)\n\ts_barrier" ::"n"(N) : "memory"); SB(); }
DI void lds_bar() { SB(); asm volatile("s_waitcnt lgkmcnt(0)\n\ts_barrier" ::: "memory"); SB(); }
DI void drain() { asm volatile("s_waitcnt vmcnt(0)" ::: "memory"); }
template <int IMM> DI void glds16(unsigned voff, const void* sbase, unsigned ldsbase) {
  asm volatile("s_add_i32 m0, %2, %3\n\ts_nop 0\n\tglobal_load_lds_dwordx4 %0, %1" ::"v"(voff), "s"(sbase), "s"(ldsbase), "n"(IMM) : "memory", "scc");
}
template <int IMM> DI void glds16c(unsigned voff, const void* sbase, unsigned ldsbase) {
  asm volatile("s_add_i32 m0, %2, %3\n\ts_nop 0\n\tglobal_load_lds_dwordx4 %0, %1 sc1" ::"v"(voff), "s"(sbase), "s"(ldsbase), "n"(IMM) : "memory", "scc");
}
template <int IMM> DI void glds16x4(const unsigned* v, const void* sbase, unsigned ldsbase) {
  asm volatile("s_add_i32 m0, %5, %6\n\ts_nop 0\n\tglobal_load_lds_dwordx4 %0, %4\n\tglobal_load_lds_dwordx4 %1, %4 offset:1024\n\t"
               "global_load_lds_dwordx4 %2, %4 offset:2048\n\tglobal_load_lds_dwordx4 %3, %4 offset:3072"
               ::"v"(v[0]), "v"(v[1]), "v"(v[2]), "v"(v[3]), "s"(sbase), "s"(ldsbase), "n"(IMM) : "memory", "scc");
}
template <int IMM> DI void glds16x3(const unsigned* v, const void* sbase, unsigned ldsbase) {
  asm volatile("s_add_i32 m0, %4, %5\n\ts_nop 0\n\tglobal_load_lds_dwordx4 %0, %3\n\tglobal_load_lds_dwordx4 %1, %3 offset:1024\n\t"
               "global_load_lds_dwordx4 %2, %3 offset:2048"
               ::"v"(v[0]), "v"(v[1]), "v"(v[2]), "s"(sbase), "s"(ldsbase), "n"(IMM) : "memory", "scc");
}
template <int IMM> DI void glds16x5c(const unsigned* v, const void* sbase, unsigned ldsbase) {
  asm volatile("s_add_i32 m0, %6, %7\n\ts_nop 0\n\tglobal_load_lds_dwordx4 %0, %5 offset:-2048 sc1\n\tglobal_load_lds_dwordx4 %1, %5 offset:-1024 sc1\n\t"
               "global_load_lds_dwordx4 %2, %5 sc1\n\tglobal_load_lds_dwordx4 %3, %5 offset:1024 sc1\n\tglobal_load_lds_dwordx4 %4, %5 offset:2048 sc1"
               ::"v"(v[0]), "v"(v[1]), "v"(v[2]), "v"(v[3]), "v"(v[4]), "s"(sbase), "s"(ldsbase), "n"(IMM + 2048) : "memory", "scc");
}
typedef float f32x2 __attribute__((ext_vector_type(2)));
DI f32x2 fma2(f32x2 a, f32x2 b, f32x2 c) { return __builtin_elementwise_fma(a, b, c); }
DI unsigned pk2v(f32x2 v) { bf16x2_t p = {(__bf16)v.x, (__bf16)v.y}; return __builtin_bit_cast(unsigned, p); }
DI float max3(float a, float b, float c) { float r; asm("v_max3_f32 %0, %1, %2, %3" : "=v"(r) : "v"(a), "v"(b), "v"(c)); return r; }
DI float relu(float a) { float r; asm("v_max_f32 %0, 0, %1" : "=v"(r) : "v"(a)); return r; }
DI float bf2f(unsigned b) { return __uint_as_float(b << 16); }
DI unsigned pk2(float lo, float hi) { bf16x2_t p = {(__bf16)lo, (__bf16)hi}; return __builtin_bit_cast(unsigned, p); }
DI f32x16 mfma32(u32x4 a, u32x4 b, f32x16 c) {
  return __builtin_amdgcn_mfma_f32_32x32x16_bf16(__builtin_bit_cast(s16x8, a), __builtin_bit_cast(s16x8, b), c, 0, 0, 0);
}
DI f32x4 mfma16(u32x4 a, u32x4 b, f32x4 c) {
  return __builtin_amdgcn_mfma_f32_16x16x32_bf16(__builtin_bit_cast(s16x8, a), __builtin_bit_cast(s16x8, b), c, 0, 0, 0);
}
DI u32x4 lds128(unsigned a, int off = 0) { return *(const LAS u32x4*)((const LAS char*)(size_t)a + off); }
DI u32x4 tr_frag(unsigned a, int off, int second) {
  s16x4 lo = __builtin_amdgcn_ds_read_tr16_b64_v4i16((LAS s16x4*)((LAS char*)(size_t)a + off));
  s16x4 hi = __builtin_amdgcn_ds_read_tr16_b64_v4i16((LAS s16x4*)((LAS char*)(size_t)a + off + second));
  u32x2 x = __builtin_bit_cast(u32x2, lo), y = __builtin_bit_cast(u32x2, hi);
  return u32x4{x.x, x.y, y.x, y.y};
}
DI float vsum8(u32x4 v) {
  const float a = __uint_as_float(v.x << 16) + __uint_as_float(v.y << 16), b = __uint_as_float(v.x & 0xffff0000u) + __uint_as_float(v.y & 0xffff0000u);
  const float c = __uint_as_float(v.z << 16) + __uint_as_float(v.w << 16), d = __uint_as_float(v.z & 0xffff0000u) + __uint_as_float(v.w & 0xffff0000u);
  return (a + b) + (c + d);
}
DI void granule_store(char* ws, unsigned off, float v, unsigned tag) {
  __hip_atomic_store((GAS unsigned long long*)(ws + off), ((unsigned long long)tag << 32) | __float_as_uint(v), __ATOMIC_RELAXED, __HIP_MEMORY_SCOPE_AGENT);
}
DI unsigned long long granule_load(char* ws, unsigned off) {
  return __hip_atomic_load((GAS unsigned long long*)(ws + off), __ATOMIC_RELAXED, __HIP_MEMORY_SCOPE_AGENT);
}
template <int CTRL> DI float dpp_add(float v) {
  return v + __uint_as_float(__builtin_amdgcn_update_dpp(0u, __float_as_uint(v), CTRL, 0xf, 0xf, false));
}
DI float half_sum(float v) {
  v = dpp_add<0xB1>(v);
  v = dpp_add<0x4E>(v);
  v = dpp_add<0x141>(v);
  v = dpp_add<0x140>(v);
  auto r = __builtin_amdgcn_permlane16_swap(__float_as_uint(v), __float_as_uint(v), false, false);
  return __uint_as_float(r[0]) + __uint_as_float(r[1]);
}
DI float xhalf_add(float v) {
  auto r = __builtin_amdgcn_permlane32_swap(__float_as_uint(v), __float_as_uint(v), false, false);
  return __uint_as_float(r[0]) + __uint_as_float(r[1]);
}
DI u32x4 pack_feat8(unsigned ax, unsigned ay, unsigned bx, unsigned by) {
  auto rx = __builtin_amdgcn_permlane32_swap(ax, bx, false, false);
  auto ry = __builtin_amdgcn_permlane32_swap(ay, by, false, false);
  return u32x4{rx[0], ry[0], rx[1], ry[1]};
}
DI f32x2 lo2(const f32x4& v) { return f32x2{v.x, v.y}; }
DI f32x2 hi2(const f32x4& v) { return f32x2{v.z, v.w}; }
template <int I> DI f32x2 pair(const f32x16& v) { return f32x2{v[2 * I], v[2 * I + 1]}; }
DI int fresh_lane() { int l; asm volatile("v_mbcnt_lo_u32_b32 %0, -1, 0\n\tv_mbcnt_hi_u32_b32 %0, -1, %0" : "=v"(l)); return l; }
typedef __amdgpu_buffer_rsrc_t rsrc_t;
DI void st128c(rsrc_t r, unsigned off, u32x4 v) { __builtin_amdgcn_raw_buffer_store_b128(v, r, off, 0, 16); }
DI void st64c(rsrc_t r, unsigned off, u32x2 v) { __builtin_amdgcn_raw_buffer_store_b64(v, r, off, 0, 16); }
DI u32x4 ld128c(rsrc_t r, unsigned off) { return __builtin_amdgcn_raw_buffer_load_b128(r, off, 0, 16); }
DI u32x2 ld64c(rsrc_t r, unsigned off) { return __builtin_amdgcn_raw_buffer_load_b64(r, off, 0, 16); }
DI unsigned flag_load(char* ws, int idx) {
  return __hip_atomic_load((GAS unsigned*)(ws + WS_FLAG) + idx * 16, __ATOMIC_RELAXED, __HIP_MEMORY_SCOPE_AGENT);
}
DI void flag_store(char* ws, int idx, unsigned v) {
  __hip_atomic_store((GAS unsigned*)(ws + WS_FLAG) + idx * 16, v, __ATOMIC_RELAXED, __HIP_MEMORY_SCOPE_AGENT);
}
DI unsigned read_epoch(const char* ws) {
  unsigned e;
  asm volatile("s_load_dword %0, %1, 0x0\n\ts_waitcnt lgkmcnt(0)" : "=s"(e) : "s"(ws + WS_FLAG + FL_EPOCH * 64) : "memory");
  return e;
}
DI void arrive_publish(LAS char* L, int word, unsigned total, char* ws, int flag, unsigned tag, int lane) {
  if (lane == 0) {
    if (__hip_atomic_fetch_add((LAS unsigned*)(L + word), 1u, __ATOMIC_RELAXED, __HIP_MEMORY_SCOPE_WORKGROUP) == total - 1u) flag_store(ws, flag, tag);
  }
}
DI void flags_wait(char* ws, int idx, unsigned tag) {
  for (int spin = 0; spin < (1 << 22); spin++) {
    const bool ok = idx < 0 || flag_load(ws, idx) == tag;
    if (__all(ok)) break;
    __builtin_amdgcn_s_sleep(1);
  }
  __builtin_amdgcn_fence(__ATOMIC_ACQUIRE, "wavefront");
}


template <class Hook0, class Hook1, class Hook2>
DI void attention_task(rsrc_t rws, unsigned lds0, int w, int lane, int qi, int cgA, int cgX, int odd, u32x4 qf0, u32x4 qf1, Hook0&& mid,
                       Hook1&& per_tile, Hook2&& pre_store) {
  const int r32 = lane & 31, h = lane >> 5;
  const int q0 = 256 * qi + 32 * w;
  const unsigned ka = lds0 + L_K + h * 8192 + (32 * w + r32) * 16;
  const unsigned kx = lds0 + L_K + 16384 + (32 * w + r32) * 16;
  f32x16 sc[9];
  const f32x16 zero = {};
  const unsigned va = r32 < 20 ? lds0 + L_V + (2 * w) * 640 + h * 320 + r32 * 16 : lds0 + AUX_C + (r32 == 20 ? 0 : 16);
  const int vstep = r32 < 20 ? 640 : 0;
  const int tmin = qi == 0 ? 8 - w : 0;
  u32x4 vf[18];
  unsigned dm[8];
  float mxa[4] = {-INFINITY, -INFINITY, -INFINITY, -INFINITY};
  {
    u32x4 k0[9], k1[9];
#pragma unroll
    for (int n = 0; n < 9; n++) { k0[n] = lds128(ka, ((n + 8) % 9) * 512); k1[n] = lds128(kx, ((n + 8) % 9) * 512); }
    sfor<0, 11>([&](auto N) {
      constexpr int n = N, t = (n + 8) % 9, u = n - 3;
      SB();
      if constexpr (n < 9) { sc[t] = mfma32(k0[n], qf0, zero); sc[t] = mfma32(k1[n], qf1, sc[t]); }
      if constexpr (n >= 2 && n < 11) { vf[2 * (n - 2)] = lds128(va + (2 * (n - 2)) * vstep); vf[2 * (n - 2) + 1] = lds128(va + (2 * (n - 2) + 1) * vstep); }
      if constexpr (u >= 0) {
        if (u < tmin) {
#pragma unroll
          for (int i = 0; i < 16; i++) sc[u][i] = -INFINITY;
        }
        if constexpr (u == 0) {
#pragma unroll
          for (int i = 0; i < 16; i += 2) {
            const int kr = (i & 3) + 8 * (i >> 2) + 4 * h;
            const bool d0 = kr <= r32, d1 = kr + 1 <= r32;
            sc[0][i] = d0 ? sc[8][i] : sc[0][i];
            sc[0][i + 1] = d1 ? sc[8][i + 1] : sc[0][i + 1];
            dm[i >> 1] = d1 ? 0xffffffffu : (d0 ? 0x0000ffffu : 0u);
          }
        }
#pragma unroll
        for (int i = 0; i < 8; i++) mxa[i & 3] = max3(mxa[i & 3], sc[u][2 * i], sc[u][2 * i + 1]);
      }
    });
  }
  SB();
  float mx = max3(mxa[0], mxa[1], max3(mxa[2], mxa[3], mxa[3]));
  {
    auto r = __builtin_amdgcn_permlane32_swap(__float_as_uint(mx), __float_as_uint(mx), false, false);
    mx = max3(__uint_as_float(r[0]), __uint_as_float(r[1]), __uint_as_float(r[1]));
  }
  u32x4 nm;
  {
    const unsigned m0 = __float_as_uint(mx) & 0xffff0000u;
    const float r1 = mx - __uint_as_float(m0);
    const unsigned m1 = __float_as_uint(r1) & 0xffff0000u;
    const unsigned m2 = __float_as_uint(r1 - __uint_as_float(m1)) & 0xffff0000u;
    const unsigned a = ((m0 >> 16) | m1) ^ 0x80008000u, c = (m2 >> 16) ^ 0x8000u;
    nm = u32x4{h ? 0u : a, h ? 0u : c, 0u, 0u};
  }
  const u32x4 ones3 = {0x3f803f80u, 0x00003f80u, 0u, 0u};
  mid();
  SB();
  sc[1] = mfma32(ones3, nm, sc[1]);
  sc[2] = mfma32(ones3, nm, sc[2]);
  f32x16 o = {};
  sfor<1, 9>([&](auto T) {
    constexpr int tt = T, t = tt & 7;
    SB();
    if constexpr (tt + 2 < 9) sc[(tt + 2) & 7] = mfma32(ones3, nm, sc[(tt + 2) & 7]);
    per_tile(T);
    unsigned pk[8];
#pragma unroll
    for (int i = 0; i < 8; i++) pk[i] = pk2(__builtin_amdgcn_exp2f(sc[t][2 * i]), __builtin_amdgcn_exp2f(sc[t][2 * i + 1]));
    if constexpr (t != 0) {
      o = mfma32(vf[2 * t], u32x4{pk[0], pk[1], pk[2], pk[3]}, o);
      o = mfma32(vf[2 * t + 1], u32x4{pk[4], pk[5], pk[6], pk[7]}, o);
    } else {
      o = mfma32(vf[16], u32x4{pk[0] & dm[0], pk[1] & dm[1], pk[2] & dm[2], pk[3] & dm[3]}, o);
      o = mfma32(vf[17], u32x4{pk[4] & dm[4], pk[5] & dm[5], pk[6] & dm[6], pk[7] & dm[7]}, o);
      o = mfma32(vf[0], u32x4{pk[0] & ~dm[0], pk[1] & ~dm[1], pk[2] & ~dm[2], pk[3] & ~dm[3]}, o);
      o = mfma32(vf[1], u32x4{pk[4] & ~dm[4], pk[5] & ~dm[5], pk[6] & ~dm[6], pk[7] & ~dm[7]}, o);
    }
  });
  SB();
  pre_store();
  SB();
  auto lr = __builtin_amdgcn_permlane32_swap(__float_as_uint(o[8]), __float_as_uint(o[8]), false, false);
  const float inv = __builtin_amdgcn_rcpf(__uint_as_float(lr[1]));
  const unsigned ob = WS_ACT + 192u * S_ * 16;
  st128c(rws, ob + ((cgA + h) * S_ + q0 + r32) * 16,
         u32x4{pk2(o[0] * inv, o[1] * inv), pk2(o[2] * inv, o[3] * inv), pk2(o[4] * inv, o[5] * inv), pk2(o[6] * inv, o[7] * inv)});
  if (h == 0) st64c(rws, ob + (cgX * S_ + q0 + r32) * 16 + odd * 8, u32x2{pk2(o[8] * inv, o[9] * inv), pk2(o[10] * inv, o[11] * inv)});
}
DI void load_q(rsrc_t rws, int w, int lane, int qi, int cgA, int cgX, int odd, u32x4& qf0, u32x4& qf1) {
  const int r32 = lane & 31, h = lane >> 5, q0 = 256 * qi + 32 * w;
  qf0 = ld128c(rws, WS_QK + ((cgA + h) * S_ + q0 + r32) * 16);
  const u32x2 xq = ld64c(rws, WS_QK + (cgX * S_ + q0 + r32) * 16 + odd * 8);
  const unsigned x0 = h ? 0u : xq.x, x1 = h ? 0u : xq.y;
  qf1 = odd ? u32x4{0u, 0u, x0, x1} : u32x4{x0, x1, 0u, 0u};
}

__global__ __launch_bounds__(512, 2) void k_fused(const u16* __restrict__ x, const u16* __restrict__ wqkv, const u16* __restrict__ wo,
                                                  const u16* __restrict__ w1, const u16* __restrict__ b1, const u16* __restrict__ w2,
                                                  const u16* __restrict__ b2, char* __restrict__ ws, u16* __restrict__ out) {
  extern __shared__ __attribute__((aligned(16))) char smem[];
  LAS char* L = (LAS char*)smem;
  const unsigned lds0 = (unsigned)(size_t)L;
  const int tid = threadIdx.x, lane = tid & 63, w = __builtin_amdgcn_readfirstlane(tid >> 6);
  const int b = blockIdx.x, xcd = b & 7, bi = b >> 3;
  const int s = w & 3;
  const rsrc_t rws = __builtin_amdgcn_make_buffer_rsrc(ws, 0, WS_BYTES, 0x00020000);
  const int rb = (xcd & 1) * 8 + (bi >> 2), j = (xcd >> 1) * 4 + (bi & 3);
  const int row0 = rb * 128;
  const int kc = (5 * rb) >> 1, rot = ((kc >> 2) + 3 * j) % 10, kc0 = kc & 3, kc1 = kc0 + 2 + (rb & 1);
  const int hd = xcd * 4 + (bi & 3), qi = bi >> 2;
  const int odd = hd & 1, cgA = (hd >> 1) * 5 + (odd ? 3 : 0), cgX = (hd >> 1) * 5 + 2;
  const int rb3 = (xcd >> 1) * 8 + (bi >> 2), cgp = (xcd & 1) * 4 + (bi & 3);
  const int row3 = rb3 * 64, n0 = cgp * 80;
  const int r32 = lane & 31, h = lane >> 5;
  const int blk = (lane >> 4) & 1, q4 = (lane & 15) >> 2, p4 = lane & 3;
  const unsigned a0 = lds0 + L_A + (32 * s + r32) * 128 + ((h ^ ((r32 >> 1) & 7)) * 16);
  auto rdA = [&](int p, int t) -> u32x4 {
    if (p % 5 < 4) return lds128(a0 ^ (t * 32), (p % 5) * 16384);
    unsigned ad;
    asm volatile("v_add_u32 %0, 0x10000, %1" : "=v"(ad) : "v"(a0 ^ (t * 32)));
    return lds128(ad);
  };
  const unsigned bb = lds0 + L_B + (8 * h + q4) * 256 + (4 * q4 + 2 * blk + (p4 >> 1)) * 16 + 8 * (p4 & 1);
  const unsigned fa = lds0 + L_B + (8 * h + q4) * 192 + (2 * blk + (p4 >> 1)) * 16 + 8 * (p4 & 1);
  constexpr int FP[6] = {36864, 49152, 61440, 0, 12288, 24576};
  auto rdB = [&](int p, int t, int u) { return tr_frag(bb ^ (64 * u), (p & 3) * 16384 + t * 4096, 1024); };
  auto rdF = [&](int f, int t, int u) { return tr_frag(fa, FP[f % 6] + t * 3072 + u * 64, 768); };

  if (w < 4) {
    const int srow = row0 + 8 * j + 2 * s + h;
    u32x2 sx[5];
#pragma unroll
    for (int i = 0; i < 5; i++) sx[i] = *(const u32x2*)(x + (size_t)srow * D_ + 128 * i + 4 * r32);
    const unsigned tag = read_epoch(ws) + 1u;
    u32x4 af[20], at[4];
    auto getA = [&](auto P, auto T) -> u32x4& { if constexpr (P < 5) return af[4 * P + T]; else return at[T]; };
    auto row_stats = [&]() {
      float v[20];
#pragma unroll
      for (int i = 0; i < 5; i++) {
        v[4 * i] = bf2f(sx[i].x & 0xffffu); v[4 * i + 1] = bf2f(sx[i].x >> 16); v[4 * i + 2] = bf2f(sx[i].y & 0xffffu); v[4 * i + 3] = bf2f(sx[i].y >> 16);
      }
      float sm = ((v[0] + v[1]) + (v[2] + v[3])) + ((v[4] + v[5]) + (v[6] + v[7])) + ((v[8] + v[9]) + (v[10] + v[11]));
      sm += ((v[12] + v[13]) + (v[14] + v[15])) + ((v[16] + v[17]) + (v[18] + v[19]));
      sm = half_sum(sm);
      const float mu = sm * (1.0f / D_);
      float q4[4] = {0.f, 0.f, 0.f, 0.f};
#pragma unroll
      for (int i = 0; i < 20; i++) q4[i & 3] = __builtin_fmaf(v[i] - mu, v[i] - mu, q4[i & 3]);
      float sq = (q4[0] + q4[1]) + (q4[2] + q4[3]);
      sq = half_sum(sq);
      const float r = __builtin_amdgcn_rsqf(sq * (1.0f / D_) + 1e-5f);
      const int lc = fresh_lane();
      if ((lc & 31) < 2) granule_store(ws, WS_STAT + (row0 + 8 * j + 2 * s + (lc >> 5)) * 16 + (lc & 1) * 8, (lc & 1) ? r : mu * r, tag);
    };
    row_stats();
    unsigned long long stg = 0;
    auto stoff = [&]() { const int lc = fresh_lane(); return WS_STAT + (row0 + 32 * s + (lc & 31)) * 16 + (lc >> 5) * 8; };
    auto conv = [&](int hc) {
      const int kb = hc / 5, cb = hc % 5, lc = fresh_lane();
      const unsigned dst = WS_WT + ((kb * 4 + 2 * (s >> 1) + (lc >> 5)) * D_ + cb * 128 + 64 * (s & 1) + (lc & 31)) * 16;
      const unsigned src = (bb ^ (128 * (s & 1))) + (s >> 1) * 4096;
      st128c(rws, dst, tr_frag(src, L_T, 1024));
      st128c(rws, dst + 512, tr_frag(src ^ 64, L_T, 1024));
    };
    f32x16 acc[4] = {};
    auto body = [&](auto MODE) {
      u32x4 bs[3][4];
      auto mm = [&](auto N, int u) {
        constexpr int n = N;
        if constexpr (MODE != 2) {
          const u32x4 a = getA(IC<n / 4>{}, IC<n % 4>{});
          if constexpr (MODE == 0) acc[u] = mfma32(bs[n % 3][u], a, acc[u]); else acc[u] = mfma32(a, bs[n % 3][u], acc[u]);
        }
      };
      auto rB = [&](int g, int t, int u) { if constexpr (MODE != 2) bs[(4 * g + t) % 3][u] = rdB(g, t, u); };
      sfor<0, 10>([&](auto G) {
        constexpr int g = G;
        lds_bar();
#pragma unroll
        for (int u = 0; u < 4; u++) rB(g, 0, u);
        sfor<1, 4>([&](auto T) {
          constexpr int t = T;
          SB();
#pragma unroll
          for (int u = 0; u < 4; u++) {
            if constexpr (4 * g + t - 3 >= 0) mm(IC<(4 * g + t - 3 >= 0 ? 4 * g + t - 3 : 0)>{}, u);
            if (t == 1 && u == 3) getA(G, IC<0>{}) = rdA(g, 0);
            if (t == 1 && u == 3) getA(G, IC<1>{}) = rdA(g, 1);
            if (t == 2 && u == 3) getA(G, IC<2>{}) = rdA(g, 2);
            if (t == 3 && u == 0) getA(G, IC<3>{}) = rdA(g, 3);
            rB(g, t, u);
            SB();
          }
        });
#pragma unroll
        for (int u = 0; u < 4; u++) mm(IC<4 * g + 1>{}, u);
        if constexpr (g == 8) stg = granule_load(ws, stoff());
        if constexpr (g == 3) conv(b);
        if constexpr (g == 7) { if (b < 84) conv(256 + b); }
      });
#pragma unroll
      for (int u = 0; u < 4; u++) mm(IC<38>{}, u);
#pragma unroll
      for (int u = 0; u < 4; u++) mm(IC<39>{}, u);
    };
    if (j < 10) body(IC<0>{}); else if (j < 15) body(IC<1>{}); else body(IC<2>{});
    float mur, rs;
    {
      for (int spin = 0; spin < (1 << 22) && !__all((unsigned)(stg >> 32) == tag); spin++) stg = granule_load(ws, stoff());
      auto r = __builtin_amdgcn_permlane32_swap((unsigned)stg, (unsigned)stg, false, false);
      mur = __uint_as_float(r[0]); rs = __uint_as_float(r[1]);
    }
    const int l2 = fresh_lane(), r2 = l2 & 31, h2 = l2 >> 5;
    if (j >= 10 && h2 == 0) {
      ((LAS float*)(L + AUX_ST))[32 * s + r2] = mur;
      ((LAS float*)(L + AUX_ST))[128 + 32 * s + r2] = rs;
    }
    wait_bar<0>();
    if (j < 10) {
      const float sc = (j < 5) ? QSCALE : 1.0f;
      const f32x2 a2 = {rs * sc, rs * sc}, b2q = {-mur * sc, -mur * sc};
      const unsigned dst = WS_QK + ((16 * j + h2) * S_ + row0 + 32 * s + r2) * 16;
      const unsigned csa = lds0 + AUX_CS + 16 * h2;
      sfor<0, 2>([&](auto UU) {
        f32x4 cs[8];
#pragma unroll
        for (int i = 0; i < 8; i++) cs[i] = __builtin_bit_cast(f32x4, lds128(csa, (64 * UU + 8 * i) * 4));
        SB();
        sfor<0, 4>([&](auto C) {
          constexpr int u = 2 * UU + C / 2, k = C % 2, ci = 2 * C;
          const unsigned p0 = pk2v(fma2(pair<4 * k>(acc[u]), a2, lo2(cs[ci]) * b2q)), p1 = pk2v(fma2(pair<4 * k + 1>(acc[u]), a2, hi2(cs[ci]) * b2q));
          const unsigned p2 = pk2v(fma2(pair<4 * k + 2>(acc[u]), a2, lo2(cs[ci + 1]) * b2q)), p3 = pk2v(fma2(pair<4 * k + 3>(acc[u]), a2, hi2(cs[ci + 1]) * b2q));
          st128c(rws, dst + (4 * u + 2 * k) * (S_ * 16), pack_feat8(p0, p1, p2, p3));
        });
      });
    } else if (j < 15) {
      f32x4 murv[4], rsv[4];
      float csv[4];
#pragma unroll
      for (int g = 0; g < 4; g++) {
        murv[g] = *(const LAS f32x4*)(L + AUX_ST + (32 * s + 8 * g + 4 * h2) * 4);
        rsv[g] = *(const LAS f32x4*)(L + AUX_ST + 512 + (32 * s + 8 * g + 4 * h2) * 4);
        csv[g] = -*(const LAS float*)(L + AUX_CS + (32 * g + r2) * 4);
      }
      sfor<0, 4>([&](auto U) {
        constexpr int u = U;
        const f32x2 cs2 = {csv[u], csv[u]};
        const int colv = 128 * (j - 10) + 32 * u + r2;
        const int head = colv / 20, d = colv - head * 20;
        const int dd = (head & 1) ? (d < 4 ? d + 16 : d - 4) : d;
        const int slot = dd < 16 ? ((dd & 3) + 8 * ((dd >> 2) & 1) + 4 * (dd >> 3)) : dd;
        const unsigned dst = WS_VT + ((head * 128 + (row0 + 32 * s) / 16) * 2 + h2) * 320 + slot * 16;
        sfor<0, 2>([&](auto SP) {
          constexpr int sp = SP;
          const unsigned p0 = pk2v(fma2(pair<4 * sp>(acc[u]), lo2(rsv[2 * sp]), cs2 * lo2(murv[2 * sp])));
          const unsigned p1 = pk2v(fma2(pair<4 * sp + 1>(acc[u]), hi2(rsv[2 * sp]), cs2 * hi2(murv[2 * sp])));
          const unsigned p2 = pk2v(fma2(pair<4 * sp + 2>(acc[u]), lo2(rsv[2 * sp + 1]), cs2 * lo2(murv[2 * sp + 1])));
          const unsigned p3 = pk2v(fma2(pair<4 * sp + 3>(acc[u]), hi2(rsv[2 * sp + 1]), cs2 * hi2(murv[2 * sp + 1])));
          st128c(rws, dst + sp * 640, u32x4{p0, p1, p2, p3});
        });
      });
    }
    f32x16 facc[3] = {};
    {
      u32x4 bs[3][3];
      auto pos = [](int f) { return (f + 5) % 10; };
      auto mm = [&](auto N, int u) {
        constexpr int n = N;
        facc[u] = mfma32(bs[n % 3][u], getA(IC<(n / 4 + 5) % 10>{}, IC<n % 4>{}), facc[u]);
      };
      sfor<0, 10>([&](auto Fs) {
        constexpr int f = Fs;
        if constexpr (f == 2 || f == 4 || f == 6 || f == 8) lds_bar();
        if constexpr (f == 3) { asm volatile("" : "+v"(facc[0]), "+v"(facc[1]), "+v"(facc[2])); drain(); arrive_publish(L, AUX_W + 12, 4, ws, FL_1 + rb * 16 + j, tag, fresh_lane()); SB(); }
#pragma unroll
        for (int u = 0; u < 3; u++) bs[(4 * f) % 3][u] = rdF(f, 0, u);
        sfor<1, 4>([&](auto T) {
          constexpr int t = T;
          SB();
#pragma unroll
          for (int u = 0; u < 3; u++) {
            if constexpr (4 * f + t - 3 >= 0) mm(IC<(4 * f + t - 3 >= 0 ? 4 * f + t - 3 : 0)>{}, u);
            if constexpr (f < 5) {
              if (t == 1 && u == 2) at[0] = rdA(pos(f), 0);
              if (t == 1 && u == 2) at[1] = rdA(pos(f), 1);
              if (t == 2 && u == 2) at[2] = rdA(pos(f), 2);
              if (t == 3 && u == 0) at[3] = rdA(pos(f), 3);
            }
            bs[(4 * f + t) % 3][u] = rdF(f, t, u);
            SB();
          }
        });
#pragma unroll
        for (int u = 0; u < 3; u++) mm(IC<4 * f + 1>{}, u);
      });
#pragma unroll
      for (int u = 0; u < 3; u++) mm(IC<38>{}, u);
#pragma unroll
      for (int u = 0; u < 3; u++) mm(IC<39>{}, u);
    }
    {
      const int l3 = fresh_lane(), r3 = l3 & 31, h3 = l3 >> 5;
      while (true) {
        const u32x4 rdy = *(const volatile LAS u32x4*)(L + AUX_W);
        if (__builtin_amdgcn_readfirstlane(rdy.x & rdy.y & rdy.z) == 1u) break;
      }
      asm volatile("" ::: "memory");
      const unsigned dst = WS_ACT + ((12 * j + h3) * S_ + row0 + 32 * s + r3) * 16;
      const unsigned ca = lds0 + AUX_CF + 16 * h3;
      const f32x2 rs2 = {rs, rs}, nm2 = {-mur, -mur};
      auto act = [&](f32x2 a, f32x2 cs, f32x2 bv) {
        f32x2 t = fma2(a, rs2, fma2(cs, nm2, bv));
        t = f32x2{relu(t.x), relu(t.y)};
        return pk2v(t * t);
      };
      sfor<0, 3>([&](auto U) {
        constexpr int u = U;
        f32x4 cs[4], bv[4];
#pragma unroll
        for (int i = 0; i < 4; i++) {
          cs[i] = __builtin_bit_cast(f32x4, lds128(ca, (32 * u + 8 * i) * 4));
          bv[i] = __builtin_bit_cast(f32x4, lds128(ca, AUX_BF - AUX_CF + (32 * u + 8 * i) * 4));
        }
        SB();
        sfor<0, 2>([&](auto K) {
          constexpr int k = K;
          const unsigned p0 = act(pair<4 * k>(facc[u]), lo2(cs[2 * k]), lo2(bv[2 * k])), p1 = act(pair<4 * k + 1>(facc[u]), hi2(cs[2 * k]), hi2(bv[2 * k]));
          const unsigned p2 = act(pair<4 * k + 2>(facc[u]), lo2(cs[2 * k + 1]), lo2(bv[2 * k + 1]));
          const unsigned p3 = act(pair<4 * k + 3>(facc[u]), hi2(cs[2 * k + 1]), hi2(bv[2 * k + 1]));
          st128c(rws, dst + (4 * u + 2 * k) * (S_ * 16), pack_feat8(p0, p1, p2, p3));
        });
      });
    }
    while (__builtin_amdgcn_readfirstlane(*(const volatile LAS unsigned*)(L + AUX_W + 16 + 4 * s)) != 1u) __builtin_amdgcn_s_sleep(1);
    asm volatile("" ::: "memory");
    const int lanem = fresh_lane();
    u32x4 qf0, qf1;
    load_q(rws, w, lanem, qi, cgA, cgX, odd, qf0, qf1);
    wait_bar<0>();
    const int mh = s & 1, kh = s >> 1;
    const int rot3 = ((bi >> 2) + 3 * (bi & 3)) % 12, rot4 = ((bi >> 2) + (bi & 3)) % 5;
    auto kq = [&](int st) { int v; if (st < 12) { v = st + rot3; if (v >= 12) v -= 12; } else { v = st + rot4; if (v >= 17) v -= 5; } return v; };
    u32x4 ar[4][4];
    unsigned avo;
    auto ldA = [&](auto ST) {
      constexpr int st = ST;
      const unsigned so = kq(st) * (16 * S_ * 16);
#pragma unroll
      for (int i = 0; i < 4; i++) ar[st & 3][i] = __builtin_amdgcn_raw_buffer_load_b128(rws, (avo ^ ((i & 1) * 256)) + (i >> 1) * (8 * S_ * 16), so, 16);
    };
    unsigned f2 = tag;
    attention_task(rws, lds0, w, lanem, qi, cgA, cgX, odd, qf0, qf1,
                   [&]() {}, [&](auto T) { if constexpr (T == 5) f2 = flag_load(ws, FL_2 + (rb3 >> 1) * 16 + (lanem & 15)); },
                   [&]() {
                     if (!__all(f2 == tag)) flags_wait(ws, lanem < 16 ? FL_2 + (rb3 >> 1) * 16 + lanem : -1, tag);
                     avo = WS_ACT + ((4 * kh + (lanem >> 4)) * S_ + row3 + 32 * mh + 16 * kh + (lanem & 15)) * 16;
                     ldA(IC<0>{}); ldA(IC<1>{}); ldA(IC<2>{});
                   });
    while (true) {
      const u32x4 rdy = *(const volatile LAS u32x4*)(L + AUX_W + 48);
      if (__builtin_amdgcn_readfirstlane(rdy.x & rdy.y & rdy.z & rdy.w) == 1u) break;
      __builtin_amdgcn_s_sleep(1);
    }
    asm volatile("" ::: "memory");
    const int tok = row3 + 32 * mh + 16 * kh + (lanem & 15);
    u32x2 xv[5], bv[5];
    f32x4 accf[2][5] = {}, accs[2][5] = {}, gate[5];
    const unsigned wr = lds0 + kh * 5120 + lanem * 16;
    u32x4 fw[2][5];
    unsigned f3 = tag;
    auto mm1 = [&](auto ST, auto KK, auto X) {
      constexpr int st = ST, kk = KK, i = X / 2, r = X % 2;
      if constexpr (st < 12) accf[r][i] = mfma16(fw[kk][i], ar[st & 3][2 * kk + r], accf[r][i]);
      else accs[r][i] = mfma16(fw[kk][i], ar[st & 3][2 * kk + r], accs[r][i]);
    };
    auto ldA1 = [&](auto ST, auto I) {
      constexpr int st = ST, i = I;
      ar[st & 3][i] = __builtin_amdgcn_raw_buffer_load_b128(rws, (avo ^ ((i & 1) * 256)) + (i >> 1) * (8 * S_ * 16), kq(st) * (16 * S_ * 16), 16);
    };
    sfor<0, 17>([&](auto ST) {
      constexpr int st = ST;
      if constexpr (st == 5) wait_bar<12>(); else if constexpr (st >= 3) lds_bar();
      if constexpr (st == 9) { if (!__all(f3 == tag)) flags_wait(ws, lanem < 32 ? FL_3 + lanem * 8 + (rb3 >> 2) : -1, tag); }
      sfor<0, 10>([&](auto X) {
        constexpr int xx = X;
        if constexpr (xx < 5) fw[0][xx] = lds128(wr, W3[st % 7] + xx * 1024);
        if constexpr (st > 0) mm1(IC<(st > 0 ? st - 1 : 0)>{}, IC<1>{}, X);
        if constexpr (st == 13 && xx >= 5) bv[xx - 5] = *(const u32x2*)(b2 + n0 + 16 * (xx - 5) + 4 * (lanem >> 4));
        if constexpr (st == 14 && xx >= 5) xv[xx - 5] = *(const u32x2*)(x + (size_t)tok * D_ + n0 + 16 * (xx - 5) + 4 * (lanem >> 4));
        SB();
      });
      sfor<0, 10>([&](auto X) {
        constexpr int xx = X;
        if constexpr (xx < 5) fw[1][xx] = lds128(wr, W3[st % 7] + 10240 + xx * 1024);
        mm1(ST, IC<0>{}, X);
        if constexpr (xx >= 6 && st + 3 < 17) ldA1(IC<(st + 3 < 17 ? st + 3 : 0)>{}, IC<(xx >= 6 ? xx - 6 : 0)>{});
        SB();
      });
      if constexpr (st == 1) {
#pragma unroll
        for (int i = 0; i < 5; i++) asm volatile("" : "+v"(accf[0][i]), "+v"(accf[1][i]));
        asm volatile("s_waitcnt vmcnt(8)" ::: "memory");
        arrive_publish(L, AUX_W + 32, 8, ws, FL_3 + hd * 8 + qi, tag, lanem);
      }
      if constexpr (st == 7) f3 = flag_load(ws, FL_3 + (lanem & 31) * 8 + (rb3 >> 2));
      if constexpr (st == 12) {
        LAS f32x4* ex = (LAS f32x4*)(L + W3[3]) + s * 320 + lanem;
#pragma unroll
        for (int i = 0; i < 5; i++) ex[i * 64] = accf[1][i];
      }
      if constexpr (st == 15) {
        const LAS f32x4* ey = (const LAS f32x4*)(L + W3[3]) + (s ^ 2) * 320 + lanem;
#pragma unroll
        for (int i = 0; i < 5; i++) {
          const f32x4 pf = ey[i * 64];
          const float bf[4] = {bf2f(bv[i].x & 0xffffu), __uint_as_float(bv[i].x & 0xffff0000u), bf2f(bv[i].y & 0xffffu), __uint_as_float(bv[i].y & 0xffff0000u)};
#pragma unroll
          for (int e = 0; e < 4; e++) {
            const float g = accf[0][i][e] + pf[e] + bf[e];
            gate[i][e] = g * __builtin_amdgcn_rcpf(1.0f + __builtin_amdgcn_exp2f(-1.4426950408889634f * g));
          }
        }
      }
    });
    sfor<0, 10>([&](auto X) { mm1(IC<16>{}, IC<1>{}, X); });
    {
      LAS f32x4* ez = (LAS f32x4*)(L + W3[5]) + s * 320 + lanem;
#pragma unroll
      for (int i = 0; i < 5; i++) ez[i * 64] = accs[1][i];
    }
    lds_bar();
    {
      const LAS f32x4* ey = (const LAS f32x4*)(L + W3[5]) + (s ^ 2) * 320 + lanem;
      f32x4 ps[5];
#pragma unroll
      for (int i = 0; i < 5; i++) ps[i] = ey[i * 64];
#pragma unroll
      for (int i = 0; i < 5; i++) {
        const float xf[4] = {bf2f(xv[i].x & 0xffffu), __uint_as_float(xv[i].x & 0xffff0000u), bf2f(xv[i].y & 0xffffu), __uint_as_float(xv[i].y & 0xffff0000u)};
        float r[4];
#pragma unroll
        for (int e = 0; e < 4; e++) r[e] = __builtin_fmaf(accs[0][i][e] + ps[i][e], gate[i][e], xf[e]);
        *(u32x2*)(out + (size_t)tok * D_ + n0 + 16 * i + 4 * (lanem >> 4)) = u32x2{pk2(r[0], r[1]), pk2(r[2], r[3])};
      }
    }
  } else {
    const char* xbase = (const char*)(x + (size_t)row0 * D_);
    const char* fbase = (const char*)(w1 + 96 * j);
    const char* qbase = (const char*)(wqkv + 128 * (j < 15 ? j : 14));
    unsigned voffA[4], voffB[4], voffF[3];
#pragma unroll
    for (int e = 0; e < 4; e++) {
      voffA[e] = ((32 * s + 8 * e + (lane >> 3)) * D_ + (((lane & 7) ^ (4 * (e & 1) + (lane >> 4))) * 8)) * 2 - e * 1024;
      voffB[e] = (16 * s + 4 * e + (lane >> 4)) * (NQKV * 2) + ((lane & 15) ^ ((lane >> 4) << 2)) * 16 - e * 1024;
    }
    const unsigned voffT = (8 * s + (lane >> 4)) * (D_ * 2) + ((lane & 15) ^ ((lane >> 4) << 2)) * 16;
#pragma unroll
    for (int e = 0; e < 3; e++) { const int Lb = e * 1024 + lane * 16; voffF[e] = (16 * s + Lb / 192) * (F_ * 2) + Lb % 192 - e * 1024; }
    const unsigned l4 = lds0 + s * 4096, l3k = lds0 + s * 3072;
    auto kst = [&](int p) { const int v = p + rot; return v >= 10 ? v - 10 : v; };
    auto dmaP = [&](auto P, const char* xs, const char* qs) {
      constexpr int p = P;
      glds16x4<L_A + (p % 5) * 16384>(voffA, xs, l4);
      glds16x4<L_B + (p & 3) * 16384>(voffB, qs, l4);
    };
    auto dmaF = [&](auto Fs) {
      constexpr int f = Fs;
      glds16x3<L_B + FP[f % 6]>(voffF, fbase + (size_t)(kst((f + 5) % 10) * (64 * F_ * 2)), l3k);
    };
    auto dmaT = [&](int hc) {
      const unsigned kb = (unsigned)hc / 5u, cb = hc - kb * 5;
      const size_t base = kb < 48 ? (size_t)w2 : (size_t)wo - (size_t)48 * 32 * D_ * 2;
      const char* src = (const char*)(base + (size_t)(kb * (32 * D_ * 2) + cb * 256));
      glds16<L_T>(voffT, src, lds0 + s * 2048);
      glds16<L_T + 1024>(voffT, src + 4 * D_ * 2, lds0 + s * 2048);
    };
    if (w == 4) { if (lane < 12) glds16<AUX_B1>(lane * 16, (const char*)(b1 + 96 * j), lds0); }
    sfor<0, 3>([&](auto P) {
      const char* xs = xbase + kst(P) * 128;
      const char* qs = qbase + (size_t)(kst(P) * (64 * NQKV * 2));
      dmaP(P, xs, qs);
    });
    if (w == 5 && lane < 32) ((LAS unsigned*)(L + AUX_C))[lane] = lane < 4 ? 0x3f803f80u : 0u;
    const unsigned tag = read_epoch(ws) + 1u;
    const unsigned cspub = WS_CSP + ((j * 16 + rb) * 224 + 32 * s + r32) * 8;
    const char* csgat = ws + WS_CSP + (size_t)(j * 16) * 224 * 8;
    const unsigned voffG = (8 * h * 224 + 32 * s + r32) * 8;
    u32x2 gr[8];
    auto gather_issue = [&](int off) {
#pragma unroll
      for (int i = 0; i < 8; i++)
        asm volatile("global_load_dwordx2 %0, %1, %2 sc1" : "=v"(gr[i]) : "v"(voffG), "s"(csgat + off + i * 224 * 8) : "memory");
    };
    auto gather_sum = [&](int off) -> float {
      for (int spin = 0; spin < (1 << 22); spin++) {
#pragma unroll
        for (int i = 0; i < 8; i++) asm volatile("" : "+v"(gr[i]));
        bool ok = true;
#pragma unroll
        for (int i = 0; i < 8; i++) ok &= gr[i].y == tag;
        if (__all(ok)) break;
        gather_issue(off);
        drain();
      }
      float a = 0.f;
#pragma unroll
      for (int i = 0; i < 8; i++) a += __uint_as_float(gr[i].x);
      return xhalf_add(a);
    };
    float cpart = 0.f;
    sfor<0, 10>([&](auto G) {
      constexpr int g = G;
      constexpr int NW[10] = {16, 18, 18, 17, 17, 18, 18, 16, 19, 9};
      wait_bar<NW[g]>();
      if constexpr (g == 1) { if (w == 7 && lane < 48) {
        const unsigned v = *(const LAS unsigned*)(L + AUX_B1 + lane * 4);
        *(LAS u32x2*)(L + AUX_BF + lane * 8) = u32x2{v << 16, v & 0xffff0000u};
      } }
      if constexpr (g == 0) dmaT(b);
      if constexpr (g == 4) dmaT(b < 84 ? 256 + b : b);
      if constexpr (g == 2) { cpart = xhalf_add(cpart); if (h == 0) granule_store(ws, cspub, cpart, tag); else asm volatile("" ::: "memory"); }
      if constexpr (g == 7) gather_issue(0);
      const char* xs = xbase + kst(g < 7 ? g + 3 : 0) * 128;
      const char* qs = qbase + (size_t)(kst(g < 7 ? g + 3 : 0) * (64 * NQKV * 2));
      if constexpr (g < 7) dmaP(IC<(g < 7 ? g + 3 : 0)>{}, xs, qs);
      else if constexpr (g == 7) dmaF(IC<0>{});
      else if constexpr (g == 8) { dmaF(IC<1>{}); dmaF(IC<2>{}); }
      else dmaF(IC<3>{});
      if constexpr (g < 2) {
#pragma unroll
        for (int t = 0; t < 4; t++)
          if (4 * g + t >= kc0 && 4 * g + t < kc1) cpart += vsum8(tr_frag(bb ^ (64 * s), (g & 3) * 16384 + t * 4096, 1024));
      }
      if constexpr (g == 9) {
        const float cs = gather_sum(0);
        if (h == 0) ((LAS float*)(L + AUX_CS))[32 * s + r32] = cs;
      }
    });
    cpart = 0.f;
    const int uf = s < 3 ? s : 2;
    unsigned voffW[5];
#pragma unroll
    for (int i = 0; i < 5; i++) voffW[i] = ((lane >> 4) * D_ + (lane & 15) + 16 * i) * 16 + 4096 - (i - 2) * 1024;
    const char* wbase = ws + WS_WT - 4096 + (size_t)n0 * 16 + (size_t)s * 4 * D_ * 16;
    const int rot3 = ((bi >> 2) + 3 * (bi & 3)) % 12, rot4 = ((bi >> 2) + (bi & 3)) % 5;
    auto kq = [&](int st) { int v; if (st < 12) { v = st + rot3; if (v >= 12) v -= 12; } else { v = st + rot4; if (v >= 17) v -= 5; } return v; };
    const unsigned l5 = lds0 + s * 5120;
    auto issueW = [&](auto ST) {
      constexpr int st = ST;
      const char* ww = wbase + (size_t)(kq(st) * (16 * D_ * 16));
      glds16x5c<W3[st % 7]>(voffW, ww, l5);
    };
    int idxq;
    {
      const int ty = lane >> 3, rbi = (lane >> 1) & 3, hi = lane & 1;
      const int c0 = ty == 2 ? 20 * hd : 8 * (odd ? cgX : cgA), c1 = ty == 2 ? 20 * hd + 19 : 8 * (odd ? cgA + 1 : cgX) + 7;
      const int tile = 5 * ty + (hi ? c1 : c0) / 128, rr = 2 * qi - 2 + rbi;
      idxq = (lane < 24 && rr >= 0 && (ty > 0 || rbi >= 2)) ? FL_1 + rr * 16 + tile : FL_1 + rb * 16 + j;
    }
    unsigned pv[5];
    auto poll_issue = [&](unsigned& dst, int idx) {
      asm volatile("global_load_dword %0, %1, %2 sc1" : "=v"(dst) : "v"(idx * 64), "s"(ws + WS_FLAG) : "memory");
    };
    wait_bar<6>();
    if (w == 4 && lane == 0) flag_store(ws, FL_T + b, tag);
    dmaF(IC<4>{}); dmaF(IC<5>{});
#pragma unroll
    for (int t = 0; t < 8; t++)
      if (t >= kc0 && t < kc1) cpart += vsum8(tr_frag(fa + uf * 64, FP[t >> 2] + (t & 3) * 3072, 768));
    cpart = xhalf_add(cpart);
    if (h == 0 && s < 3) granule_store(ws, cspub + 128 * 8, cpart, tag);
    wait_bar<6>();
    dmaF(IC<6>{}); dmaF(IC<7>{});
    wait_bar<6>();
    dmaF(IC<8>{}); dmaF(IC<9>{});
    gather_issue(128 * 8);
#pragma unroll
    for (int i = 0; i < 4; i++) poll_issue(pv[i], FL_T + 64 * i + lane);
    wait_bar<18>();
    poll_issue(pv[4], idxq);
    asm volatile("s_waitcnt vmcnt(5)" ::: "memory");
    if (s < 3) {
      const float cf = gather_sum(128 * 8);
      if (h == 0) ((LAS float*)(L + AUX_CF))[32 * s + r32] = cf;
      asm volatile("s_waitcnt lgkmcnt(0)" ::: "memory");
      if (lane == 0) *(volatile LAS unsigned*)(L + AUX_W + 4 * s) = 1u;
    }
    lds_bar();
    asm volatile("s_waitcnt vmcnt(0)" : "+v"(pv[4])::"memory");
    if (!__all(pv[4] == tag)) flags_wait(ws, idxq, tag);
    if (lane == 0) *(volatile LAS unsigned*)(L + AUX_W + 16 + 4 * s) = 1u;
    {
      const int key0 = 256 * qi - 256;
      const char* kb = ws + WS_QK + (size_t)80 * S_ * 16;
      const u32x4 z = {0u, 0u, 0u, 0u};
#pragma unroll
      for (int i = 0; i < 2; i++) {
        const int pc = 2 * s + i, kk = key0 + 64 * pc;
        if (kk >= 0) {
          glds16c<L_K>(lane * 16, kb + ((size_t)cgA * S_ + kk) * 16, lds0 + pc * 1024);
          glds16c<L_K + 8192>(lane * 16, kb + ((size_t)(cgA + 1) * S_ + kk) * 16, lds0 + pc * 1024);
          glds16c<L_K + 16384>(lane * 16, kb + ((size_t)cgX * S_ + kk) * 16, lds0 + pc * 1024);
        } else {
#pragma unroll
          for (int c = 0; c < 3; c++) *(LAS u32x4*)(L + L_K + c * 8192 + pc * 1024 + lane * 16) = z;
        }
      }
      const char* vb = ws + WS_VT + ((long)hd * 128 + key0 / 16) * 640;
#pragma unroll
      for (int i = 0; i < 5; i++) {
        const int pc = 5 * s + i;
        if (qi > 0 || pc >= 10) glds16c<L_V>(lane * 16, vb + pc * 1024, lds0 + pc * 1024);
        else *(LAS u32x4*)(L + L_V + pc * 1024 + lane * 16) = z;
      }
    }
    u32x4 qf0, qf1;
    load_q(rws, w, lane, qi, cgA, cgX, odd, qf0, qf1);
#pragma unroll
    for (int i = 0; i < 4; i++) asm volatile("" : "+v"(pv[i]));
    if (!__all(pv[0] == tag && pv[1] == tag && pv[2] == tag && pv[3] == tag)) {
#pragma unroll
      for (int i = 0; i < 4; i++) flags_wait(ws, FL_T + 64 * i + lane, tag);
    }
    asm volatile("" : "+v"(qf0), "+v"(qf1));
    wait_bar<0>();
    if (w == 4 && lane == 0) flag_store(ws, FL_2 + rb * 16 + j, tag);
    issueW(IC<0>{}); issueW(IC<1>{}); issueW(IC<2>{}); issueW(IC<3>{}); issueW(IC<4>{});
    attention_task(rws, lds0, w, lane, qi, cgA, cgX, odd, qf0, qf1,
                   [&]() { drain(); if (lane == 0) *(volatile LAS unsigned*)(L + AUX_W + 48 + 4 * s) = 1u; }, [&](auto T) {}, []() {});
    sfor<3, 17>([&](auto ST) {
      constexpr int st = ST;
      constexpr int NW[17] = {0, 0, 0, 63, 63, 15, 20, 25, 25, 25, 25, 25, 20, 15, 10, 5, 0};
      wait_bar<NW[st]>();
      if constexpr (st <= 6) { issueW(IC<(st <= 6 ? 2 * st - 1 : 0)>{}); issueW(IC<(st <= 6 ? 2 * st : 0)>{}); }
      else if constexpr (st + 6 < 17) issueW(IC<(st + 6 < 17 ? st + 6 : 0)>{});
      if constexpr (st == 3) { asm volatile("s_waitcnt vmcnt(10)" ::: "memory"); arrive_publish(L, AUX_W + 32, 8, ws, FL_3 + hd * 8 + qi, tag, lane); }
    });
    lds_bar();
    if (b == 0 && w == 7) {
      for (int spin = 0; spin < (1 << 22); spin++) {
        const unsigned f0 = flag_load(ws, FL_2 + lane), f1 = flag_load(ws, FL_2 + 64 + lane), f2 = flag_load(ws, FL_2 + 128 + lane), f3 = flag_load(ws, FL_2 + 192 + lane);
        if (__all(f0 == tag && f1 == tag && f2 == tag && f3 == tag)) break;
      }
      if (lane == 0) flag_store(ws, FL_EPOCH, tag);
    }
  }
}

extern "C" void kernel_launch(void* const* d_in, const int* in_sizes, int n_in, void* d_out, int out_size, void* d_ws, size_t ws_size,
                              hipStream_t stream) {
  (void)hipFuncSetAttribute((const void*)k_fused, hipFuncAttributeMaxDynamicSharedMemorySize, LDS_BYTES);
  k_fused<<<256, 512, LDS_BYTES, stream>>>((const u16*)d_in[0], (const u16*)d_in[1], (const u16*)d_in[2], (const u16*)d_in[4],
                                           (const u16*)d_in[5], (const u16*)d_in[6], (const u16*)d_in[7], (char*)d_ws, (u16*)d_out);
}
```
